```python
import torch

def _softmax(x, axis=-1):
    return torch.softmax(x, dim=axis)

def _gelu(x):
    return torch.nn.functional.gelu(x, approximate='tanh')

def _silu(x):
    return torch.nn.functional.silu(x)

def _sigmoid(x):
    return torch.sigmoid(x)

def _layernorm(x, axis=-1, eps=1e-05):
    m = x.mean(dim=axis, keepdim=True)
    v = x.var(dim=axis, keepdim=True, unbiased=False)
    return (x - m) / torch.sqrt(v + eps)

def _scatter_add(tgt, idx, src, axis=0):
    return tgt.clone().index_add_(axis, idx, src)

def _segment_sum(src, seg, n):
    out = torch.zeros((n,) + src.shape[1:], dtype=src.dtype)
    return out.index_add_(0, seg, src)

def _pad(x, pw):
    flat = tuple((v for pair in reversed(pw) for v in pair))
    return torch.nn.functional.pad(x, flat)
kernel_name = 'interleave_layers__memory_aug_recurrent_mem_tr__linear_attn_block__prompt'

def setup_inputs():

    _w = lambda a: (lambda t: t.to(torch.bfloat16) if t.is_floating_point() else t)(torch.as_tensor(a))

    def _A_setup():
        B, S, Dm = (1, 2048, 512)
        H, Dh = (32, 128)
        M, seg = (8, 32)
        seg = min(seg, S)
        S = S // seg * seg
        return {'x': _w((lambda *s: torch.randn(*s, dtype=torch.bfloat16) * 0.05)(B, S, Dm)), 'mem0': _w((lambda *s: torch.randn(*s, dtype=torch.bfloat16) * 0.2)(B, M, Dm)), 'Wq': _w((lambda *s: torch.randn(*s, dtype=torch.bfloat16) * 1.6)(Dm, H, Dh) * Dm ** (-0.5)), 'Wk': _w((lambda *s: torch.randn(*s, dtype=torch.bfloat16) * 1.6)(Dm, H, Dh) * Dm ** (-0.5)), 'Wv': _w((lambda *s: torch.randn(*s, dtype=torch.bfloat16) * 0.2)(Dm, H, Dh) * Dm ** (-0.5)), 'Wo': _w((lambda *s: torch.randn(*s, dtype=torch.bfloat16) * 0.2)(H, Dh, Dm) * (H * Dh) ** (-0.5)), 'seg_len': seg}
    _A_in = _A_setup()

    def _B_setup():
        B, S, D = (1, 2048, 512)
        return {'x': _w((lambda *s: torch.randn(*s, dtype=torch.bfloat16) * 0.05)(B, S, D)), 'wqkv': _w((lambda *s: torch.randn(*s, dtype=torch.bfloat16) * 0.1)(D, 3 * D)), 'wo': _w((lambda *s: torch.randn(*s, dtype=torch.bfloat16) * 0.05)(D, D))}
    _B_in = _B_setup()
    return {'A_x': _A_in['x'], 'A_mem0': _A_in['mem0'], 'A_Wq': _A_in['Wq'], 'A_Wk': _A_in['Wk'], 'A_Wv': _A_in['Wv'], 'A_Wo': _A_in['Wo'], 'A_seg_len': _A_in['seg_len'], 'B_x': _B_in['x'], 'B_wqkv': _B_in['wqkv'], 'B_wo': _B_in['wo']}

def reference(A_x, A_mem0, A_Wq, A_Wk, A_Wv, A_Wo, A_seg_len, B_x, B_wqkv, B_wo):

    def _A_ref(x, mem0, Wq, Wk, Wv, Wo, seg_len):
        B, S, Dm = x.shape
        H, Dh = (Wq.shape[1], Wq.shape[2])
        M = mem0.shape[1]
        scale = Dh ** (-0.5)
        n_seg = S // seg_len
        L = M + seg_len
        idx = torch.arange(L)
        mask = (idx[:, None] < M) | (idx[:, None] >= idx[None, :])
        mem = mem0
        outs = []
        for s in range(n_seg):
            xs = x[:, s * seg_len:(s + 1) * seg_len, :].clone()
            h = torch.concatenate([mem, xs], axis=1)
            q = torch.einsum('btd,dhe->bhte', h, Wq)
            k = torch.einsum('btd,dhe->bhte', h, Wk)
            v = torch.einsum('btd,dhe->bhte', h, Wv)
            scores = torch.einsum('bhqe,bhke->bhqk', q, k) * scale
            scores = torch.where(mask[None, None], scores, -1000000000.0)
            p = _softmax(scores, axis=-1)
            o = torch.einsum('bhqk,bhke->bhqe', p, v)
            o = torch.einsum('bhte,hed->btd', o, Wo)
            mem = o[:, :M, :].clone()
            outs.append(o[:, M:, :])
        return torch.concatenate(outs, axis=1)

    def _B_ref(x, wqkv, wo):
        B, S, D = x.shape
        H = 32
        Dh = D // H
        qkv = (lambda x, *d: x.permute(*d))((x @ wqkv).reshape(B, S, 3, H, Dh), 2, 0, 3, 1, 4)
        q, k, v = (qkv[0], qkv[1], qkv[2])
        phi = lambda t: torch.where(t > 0, t + 1, torch.exp(t)) + 1e-06
        pq, pk = (phi(q), phi(k))
        kv = torch.einsum('bhsd,bhse->bhde', pk, v)
        z = torch.einsum('bhsd->bhd', pk)
        num = torch.einsum('bhqd,bhde->bhqe', pq, kv)
        den = torch.einsum('bhqd,bhd->bhq', pq, z)[..., None] + 1e-06
        o = num / den
        return (lambda x, *d: x.permute(*d))(o, 0, 2, 1, 3).reshape(B, S, D) @ wo
    x = A_x
    x = x + _A_ref(_layernorm(x, axis=-1), A_mem0, A_Wq, A_Wk, A_Wv, A_Wo, A_seg_len)
    x = x + _B_ref(_layernorm(x, axis=-1), B_wqkv, B_wo)
    x = x + _A_ref(_layernorm(x, axis=-1), A_mem0, A_Wq, A_Wk, A_Wv, A_Wo, A_seg_len)
    x = x + _B_ref(_layernorm(x, axis=-1), B_wqkv, B_wo)
    return x
```

```cpp
#include <hip/hip_runtime.h>
#include <hip/hip_bf16.h>
#include <stdint.h>

typedef unsigned v2u __attribute__((ext_vector_type(2)));
typedef unsigned v4u __attribute__((ext_vector_type(4)));
typedef unsigned v8u __attribute__((ext_vector_type(8)));
typedef float v2f __attribute__((ext_vector_type(2)));
typedef float v4f __attribute__((ext_vector_type(4)));
typedef short s16x4 __attribute__((ext_vector_type(4)));
typedef __bf16 bf16x2 __attribute__((ext_vector_type(2)));
typedef __bf16 bf16x4 __attribute__((ext_vector_type(4)));
typedef __bf16 bf16x8 __attribute__((ext_vector_type(8)));
typedef __bf16 bf16x16 __attribute__((ext_vector_type(16)));
typedef unsigned char u8;
typedef unsigned short u16;
#define DI __device__ __forceinline__

DI unsigned pack2(float a, float b) {
  v2f f = {a, b};
  return __builtin_bit_cast(unsigned, __builtin_convertvector(f, bf16x2));
}
DI float bflo(unsigned u) { return __uint_as_float(u << 16); }
DI float bfhi(unsigned u) { return __uint_as_float(u & 0xffff0000u); }
DI float rbf(float a) { return bflo(pack2(a, a)); }
DI v4f mfma32(v4u a, v4u b, v4f c) {
  return __builtin_amdgcn_mfma_f32_16x16x32_bf16(__builtin_bit_cast(bf16x8, a), __builtin_bit_cast(bf16x8, b), c, 0, 0, 0);
}
DI v4f mfma16(v2u a, v2u b, v4f c) {
  return __builtin_amdgcn_mfma_f32_16x16x16bf16_1k(__builtin_bit_cast(s16x4, a), __builtin_bit_cast(s16x4, b), c, 0, 0, 0);
}
DI v4f smfmac(v4u a, v8u b, v4f c, int idx) {
  return __builtin_amdgcn_smfmac_f32_16x16x64_bf16(__builtin_bit_cast(bf16x8, a), __builtin_bit_cast(bf16x16, b), c, idx, 0, 0);
}
typedef __attribute__((address_space(3))) bf16x4* lds_bf4p;
DI v2u trread(const u8* lds_ptr) {
  return __builtin_bit_cast(v2u, __builtin_amdgcn_ds_read_tr16_b64_v4bf16((lds_bf4p)lds_ptr));
}
DI unsigned ror8(unsigned v) { return __builtin_amdgcn_update_dpp(0u, v, 0x128, 0xf, 0xf, false); }
DI float ror8f(float v) { return __uint_as_float(ror8(__float_as_uint(v))); }
DI unsigned max3u(unsigned a, unsigned b, unsigned c) { unsigned d; asm("v_max3_u32 %0, %1, %2, %3" : "=v"(d) : "v"(a), "v"(b), "v"(c)); return d; }
DI v4u ldc16(__amdgpu_buffer_rsrc_t r, unsigned off) { return __builtin_amdgcn_raw_buffer_load_b128(r, off, 0, 16); }
DI void stc16(__amdgpu_buffer_rsrc_t r, unsigned off, v4u v) { __builtin_amdgcn_raw_buffer_store_b128(v, r, off, 0, 16); }
DI void stc8(__amdgpu_buffer_rsrc_t r, unsigned off, v2u v) { __builtin_amdgcn_raw_buffer_store_b64(v, r, off, 0, 16); }
DI void stc4(__amdgpu_buffer_rsrc_t r, unsigned off, unsigned v) { __builtin_amdgcn_raw_buffer_store_b32(v, r, off, 0, 16); }
DI unsigned nosent(unsigned v) { return v < 0xfffeffffu ? v : 0xfffeffffu; }

constexpr int NSEG = 64;
constexpr size_t MB = 1u << 20;
constexpr size_t OFF_WA = 0;
constexpr size_t OFF_WB = 8 * MB;
constexpr size_t OFF_WQK = 16 * MB;
constexpr size_t OFF_WVU = 24 * MB;
constexpr size_t OFF_WB1 = 32 * MB;
constexpr size_t OFF_WB2 = 34 * MB;
constexpr size_t OFF_Q = 36 * MB;
constexpr size_t OFF_K = 52 * MB;
constexpr size_t OFF_UT = 68 * MB;
constexpr size_t OFF_KM = 132 * MB;
constexpr size_t OFF_YT = 136 * MB;
constexpr size_t OFF_PF = 152 * MB;
constexpr size_t OFF_PM = 156 * MB;
constexpr size_t OFF_X1 = 158 * MB;
constexpr size_t OFF_X2 = 160 * MB;
constexpr size_t OFF_X3 = 162 * MB;
constexpr size_t OFF_PQ = 164 * MB;
constexpr size_t OFF_KVP = 166 * MB;
constexpr size_t OFF_COMM = 168 * MB;
constexpr unsigned C_PX = 0, C_PY = 1u << 20, C_PM = 2u << 20, C_M = 3u << 20;
constexpr size_t OFF_ERR = 176 * MB;
constexpr unsigned SPIN_LIMIT = 1u << 17;

DI unsigned cmap(unsigned o) { return ((o >> 13) << 14) + (o & 8191u); }

__global__ void __launch_bounds__(256) k_prep(const u16* __restrict__ Wq, const u16* __restrict__ Wk, const u16* __restrict__ Wv,
                                              const u16* __restrict__ Wo, const u16* __restrict__ wqkv, const u16* __restrict__ wo2,
                                              const u16* __restrict__ mem0, u8* __restrict__ ws) {
  const unsigned gid = blockIdx.x * 256 + threadIdx.x;
  constexpr unsigned N8 = 8 * MB / 16;
  const unsigned lane = gid & 63, n = lane & 15, g = lane >> 4;
  u16 v[8];
  size_t dst;
  if (gid < N8) {
    unsigned c = gid >> 6;
    const unsigned half = c & 1, which = (c >> 1) & 1, kk = (c >> 2) & 7, tp = (c >> 5) & 1, w = (c >> 6) & 3, h = c >> 8;
    const u16* W = which ? Wk : Wq;
    const unsigned e = 16 * (2 * w + tp) + n;
#pragma unroll
    for (int bb = 0; bb < 8; ++bb) {
      const unsigned gA = 2 * half + (g >> 1), ei = 4 * (g & 1) + 2 * ((bb >> 2) & 1) + (bb & 1), kh = (bb >> 1) & 1;
      const unsigned d = 64 * kk + 8 * (2 * gA + kh) + ei;
      v[bb] = W[(size_t)d * 4096 + h * 128 + e];
    }
    dst = OFF_WA + (size_t)gid * 16;
  } else if (gid < 2 * N8) {
    unsigned c = (gid - N8) >> 6;
    const unsigned h = c >> 8, r = c & 255;
    if (r < 128) {
      const unsigned half = r & 1, kk = (r >> 1) & 7, which = (r >> 4) & 1, w = r >> 5;
      const unsigned e = 16 * (2 * w + which) + n;
#pragma unroll
      for (int bb = 0; bb < 8; ++bb) {
        const unsigned gA = 2 * half + (g >> 1), ei = 4 * (g & 1) + 2 * ((bb >> 2) & 1) + (bb & 1), kh = (bb >> 1) & 1;
        const unsigned d = 64 * kk + 8 * (2 * gA + kh) + ei;
        v[bb] = Wv[(size_t)d * 4096 + h * 128 + e];
      }
    } else {
      const unsigned r2 = r - 128;
      const unsigned half = r2 & 1, ks = (r2 >> 1) & 3, f = (r2 >> 3) & 3, w = r2 >> 5;
#pragma unroll
      for (int bb = 0; bb < 8; ++bb) {
        const unsigned gA = 2 * half + (g >> 1), ei = 4 * (g & 1) + 2 * ((bb >> 2) & 1) + (bb & 1), fold = (bb >> 1) & 1;
        const unsigned e = 32 * ks + 16 * (ei >> 2) + 4 * gA + (ei & 3);
        const unsigned col = 128 * w + 16 * (2 * f + fold) + n;
        v[bb] = Wo[(size_t)(h * 128 + e) * 512 + col];
      }
    }
    dst = OFF_WB + (size_t)(gid - N8) * 16;
  } else if (gid < 3 * N8) {
    unsigned c = (gid - 2 * N8) >> 6;
    const unsigned ks = c & 15, mt = (c >> 4) & 3, w = (c >> 6) & 3, h = c >> 8;
    const u16* W = (w >> 1) ? Wk : Wq;
    const unsigned e = 64 * (w & 1) + 16 * mt + n;
#pragma unroll
    for (int j = 0; j < 8; ++j) v[j] = W[(size_t)(32 * ks + 8 * g + j) * 4096 + h * 128 + e];
    dst = OFF_WQK + (size_t)(gid - 2 * N8) * 16;
  } else if (gid < 4 * N8) {
    unsigned c = (gid - 3 * N8) >> 6;
    const unsigned h = c >> 8, r = c & 255;
    if (r < 128) {
      const unsigned ks = r & 15, mt = (r >> 4) & 1, w = r >> 5;
      const unsigned e = 32 * w + 16 * mt + n;
#pragma unroll
      for (int j = 0; j < 8; ++j) v[j] = Wv[(size_t)(32 * ks + 8 * g + j) * 4096 + h * 128 + e];
    } else {
      const unsigned r2 = r - 128;
      const unsigned ks = r2 & 3, nt = (r2 >> 2) & 7, w = r2 >> 5;
      const unsigned col = 128 * w + 16 * nt + n;
#pragma unroll
      for (int j = 0; j < 8; ++j) v[j] = Wo[(size_t)(h * 128 + 32 * ks + 16 * (j >> 2) + 4 * g + (j & 3)) * 512 + col];
    }
    dst = OFF_WVU + (size_t)(gid - 3 * N8) * 16;
  } else if (gid < 4 * N8 + 96 * 16 * 64) {
    unsigned c = (gid - 4 * N8) >> 6;
    const unsigned ks = c & 15, ct = c >> 4;
#pragma unroll
    for (int j = 0; j < 8; ++j) v[j] = wqkv[(size_t)(32 * ks + 8 * g + j) * 1536 + 16 * ct + n];
    dst = OFF_WB1 + (size_t)(gid - 4 * N8) * 16;
  } else if (gid < 4 * N8 + 128 * 16 * 64) {
    const unsigned o = gid - 4 * N8 - 96 * 16 * 64;
    unsigned c = o >> 6;
    const unsigned ks = c & 15, ct = c >> 4;
#pragma unroll
    for (int j = 0; j < 8; ++j) v[j] = wo2[(size_t)(32 * ks + 16 * (j >> 2) + 4 * g + (j & 3)) * 512 + 16 * ct + n];
    dst = OFF_WB2 + (size_t)o * 16;
  } else {
    const unsigned o = gid - 4 * N8 - 128 * 16 * 64;
    if (o >= 8 * MB / 16) return;
    const unsigned ob = (o * 16) & (4 * MB - 1);
    if (ob >= C_M && ob < C_M + 8192) {
      const unsigned q = (ob - C_M) >> 4;
      const unsigned bw = q >> 3, row = q & 7;
#pragma unroll
      for (int j = 0; j < 8; ++j) v[j] = mem0[row * 512 + bw * 8 + j];
#pragma unroll
      for (int j = 1; j < 8; j += 2) if (v[j] == 0xffffu) v[j] = 0xfffeu;
    } else {
#pragma unroll
      for (int j = 0; j < 8; ++j) v[j] = 0xffffu;
    }
    dst = OFF_COMM + (size_t)o * 16;
    if (o == 0) *(unsigned*)(ws + OFF_ERR) = 0u;
  }
  v4u out;
#pragma unroll
  for (int j = 0; j < 4; ++j) out[j] = (unsigned)v[2 * j] | ((unsigned)v[2 * j + 1] << 16);
  *(v4u*)(ws + dst) = out;
}

DI void ln_load(const u16* __restrict__ x, int seg, v4u (&r)[8]) {
  const int tid = threadIdx.x, row = tid >> 3, part = tid & 7;
  const v4u* src = (const v4u*)(x + (size_t)(seg * 32 + row) * 512);
#pragma unroll
  for (int i = 0; i < 8; ++i) r[i] = src[part + 8 * i];
}
DI void ln_store(const v4u (&r)[8], u8* tile) {
  const int tid = threadIdx.x, row = tid >> 3, part = tid & 7;
  float s = 0.f;
#pragma unroll
  for (int i = 0; i < 8; ++i)
#pragma unroll
    for (int j = 0; j < 4; ++j) s += bflo(r[i][j]) + bfhi(r[i][j]);
  s += __shfl_xor(s, 1); s += __shfl_xor(s, 2); s += __shfl_xor(s, 4);
  const float m = s * (1.f / 512.f);
  float q = 0.f;
#pragma unroll
  for (int i = 0; i < 8; ++i)
#pragma unroll
    for (int j = 0; j < 4; ++j) { const float a = bflo(r[i][j]) - m, b = bfhi(r[i][j]) - m; q += a * a + b * b; }
  q += __shfl_xor(q, 1); q += __shfl_xor(q, 2); q += __shfl_xor(q, 4);
  const float rs = rsqrtf(q * (1.f / 512.f) + 1e-5f);
#pragma unroll
  for (int i = 0; i < 8; ++i) {
    v4u o;
#pragma unroll
    for (int j = 0; j < 4; ++j) o[j] = pack2((bflo(r[i][j]) - m) * rs, (bfhi(r[i][j]) - m) * rs);
    const int ch = part + 8 * i;
    *(v4u*)(tile + ((((row >> 4) * 16 + (ch >> 2)) * 64) + (ch & 3) * 16 + (row & 15)) * 16) = o;
  }
}

DI void role_qk(const u16* __restrict__ x, u8* __restrict__ ws, int h, int s0, int s1, u8* lds) {
  const int tid = threadIdx.x, w = __builtin_amdgcn_readfirstlane(tid >> 6), lane = tid & 63;
  v4u W[4][16];
  {
    const v4u* src = (const v4u*)(ws + OFF_WQK + (size_t)(h * 4 + w) * 65536) + lane;
#pragma unroll
    for (int mt = 0; mt < 4; ++mt)
#pragma unroll
      for (int ks = 0; ks < 16; ++ks) W[mt][ks] = src[(mt * 16 + ks) * 64];
  }
  v4u xr[8];
  ln_load(x, s0, xr);
  u8* dstbase = ws + ((w >> 1) ? OFF_K : OFF_Q);
  for (int s = s0; s < s1; ++s) {
    u8* tile = lds + (s & 1) * 32768;
    ln_store(xr, tile);
    if (s + 1 < s1) ln_load(x, s + 1, xr);
    __syncthreads();
    v4f acc[4][2];
#pragma unroll
    for (int mt = 0; mt < 4; ++mt) { acc[mt][0] = (v4f){0, 0, 0, 0}; acc[mt][1] = (v4f){0, 0, 0, 0}; }
    const v4u* tf = (const v4u*)tile + lane;
#pragma unroll
    for (int ks = 0; ks < 16; ++ks) {
      const v4u b0 = tf[ks * 64], b1 = tf[(16 + ks) * 64];
#pragma unroll
      for (int mt = 0; mt < 4; ++mt) { acc[mt][0] = mfma32(W[mt][ks], b0, acc[mt][0]); acc[mt][1] = mfma32(W[mt][ks], b1, acc[mt][1]); }
    }
    v4u* dst = (v4u*)(dstbase + (size_t)(s * 32 + h) * 8192) + lane;
#pragma unroll
    for (int nt = 0; nt < 2; ++nt)
#pragma unroll
      for (int a = 0; a < 2; ++a) {
        const v4f c0 = acc[2 * a][nt], c1 = acc[2 * a + 1][nt];
        v4u o = {pack2(c0[0], c0[1]), pack2(c0[2], c0[3]), pack2(c1[0], c1[1]), pack2(c1[2], c1[3])};
        dst[(nt * 4 + 2 * (w & 1) + a) * 64] = o;
      }
  }
}

DI void role_vu(const u16* __restrict__ x, u8* __restrict__ ws, int h, int s0, int s1, u8* lds) {
  const int tid = threadIdx.x, w = __builtin_amdgcn_readfirstlane(tid >> 6), lane = tid & 63, n = lane & 15, g = lane >> 4;
  v4u Wv[2][16], Wo[8][4];
  {
    const v4u* src = (const v4u*)(ws + OFF_WVU + (size_t)h * 262144 + w * 32768) + lane;
#pragma unroll
    for (int mt = 0; mt < 2; ++mt)
#pragma unroll
      for (int ks = 0; ks < 16; ++ks) Wv[mt][ks] = src[(mt * 16 + ks) * 64];
    const v4u* src2 = (const v4u*)(ws + OFF_WVU + (size_t)h * 262144 + 131072 + w * 32768) + lane;
#pragma unroll
    for (int nt = 0; nt < 8; ++nt)
#pragma unroll
      for (int ks = 0; ks < 4; ++ks) Wo[nt][ks] = src2[(nt * 4 + ks) * 64];
  }
  v4u xr[8];
  ln_load(x, s0, xr);
  v4u* vex = (v4u*)(lds + 65536);
  for (int s = s0; s < s1; ++s) {
    u8* tile = lds + (s & 1) * 32768;
    ln_store(xr, tile);
    if (s + 1 < s1) ln_load(x, s + 1, xr);
    __syncthreads();
    v4f acc[2][2];
#pragma unroll
    for (int mt = 0; mt < 2; ++mt) { acc[mt][0] = (v4f){0, 0, 0, 0}; acc[mt][1] = (v4f){0, 0, 0, 0}; }
    const v4u* tf = (const v4u*)tile + lane;
#pragma unroll
    for (int ks = 0; ks < 16; ++ks) {
      const v4u b0 = tf[ks * 64], b1 = tf[(16 + ks) * 64];
#pragma unroll
      for (int mt = 0; mt < 2; ++mt) { acc[mt][0] = mfma32(Wv[mt][ks], b0, acc[mt][0]); acc[mt][1] = mfma32(Wv[mt][ks], b1, acc[mt][1]); }
    }
#pragma unroll
    for (int t = 0; t < 2; ++t) {
      const v4f c0 = acc[0][t], c1 = acc[1][t];
      v4u o = {pack2(c0[0], c0[1]), pack2(c0[2], c0[3]), pack2(c1[0], c1[1]), pack2(c1[2], c1[3])};
      vex[(t * 4 + w) * 64 + lane] = o;
    }
    __syncthreads();
    v4u vf[2][4];
#pragma unroll
    for (int t = 0; t < 2; ++t)
#pragma unroll
      for (int ks = 0; ks < 4; ++ks) vf[t][ks] = vex[(t * 4 + ks) * 64 + lane];
    u8* ut = ws + OFF_UT + (size_t)(s * 32 + h) * 32768;
#pragma unroll
    for (int nt = 0; nt < 8; ++nt) {
      v4f u0 = {0, 0, 0, 0}, u1 = {0, 0, 0, 0};
#pragma unroll
      for (int ks = 0; ks < 4; ++ks) { u0 = mfma32(vf[0][ks], Wo[nt][ks], u0); u1 = mfma32(vf[1][ks], Wo[nt][ks], u1); }
      v4u o = {pack2(u0[0], u0[1]), pack2(u0[2], u0[3]), pack2(u1[0], u1[1]), pack2(u1[2], u1[3])};
      *(v4u*)(ut + (128 * w + 16 * nt + n) * 64 + g * 16) = o;
    }
  }
}

__global__ void __launch_bounds__(256, 1) k_qku(const u16* __restrict__ x, u8* __restrict__ ws) {
  extern __shared__ __attribute__((aligned(16))) u8 lds[];
  const int b = blockIdx.x, h = b & 31, type = (b >> 5) & 1, q = b >> 6;
  if (type == 0) role_qk(x, ws, h, 16 * q, 16 * q + 16, lds);
  else role_vu(x, ws, h, 16 * q, 16 * q + 16, lds);
}

template <int NL>
DI bool poll(__amdgpu_buffer_rsrc_t r, unsigned off, v4u (&m)[NL], unsigned* err) {
  unsigned spins = 0;
  for (;;) {
    asm volatile("" ::: "memory");
#pragma unroll
    for (int k = 0; k < NL; ++k) m[k] = ldc16(r, off + k * 1024);
    unsigned mx = 0;
#pragma unroll
    for (int k = 0; k < NL; ++k) { mx = max3u(m[k][0], m[k][1], mx); mx = max3u(m[k][2], m[k][3], mx); }
    if (__ballot(mx == 0xffffffffu) == 0ull) return true;
    if (++spins > SPIN_LIMIT) { if ((threadIdx.x & 63) == 0) atomicAdd(err, 1u); return false; }
  }
}
DI bool poll2(__amdgpu_buffer_rsrc_t r, unsigned offa, v4u (&a)[4], unsigned offb, v4u (&b)[4], unsigned* err) {
  unsigned spins = 0;
  for (;;) {
    asm volatile("" ::: "memory");
#pragma unroll
    for (int k = 0; k < 4; ++k) a[k] = ldc16(r, offa + k * 1024);
#pragma unroll
    for (int k = 0; k < 4; ++k) b[k] = ldc16(r, offb + k * 1024);
    unsigned mx = 0;
#pragma unroll
    for (int k = 0; k < 4; ++k) { mx = max3u(a[k][0], a[k][1], mx); mx = max3u(a[k][2], a[k][3], mx); }
#pragma unroll
    for (int k = 0; k < 4; ++k) { mx = max3u(b[k][0], b[k][1], mx); mx = max3u(b[k][2], b[k][3], mx); }
    if (__ballot(mx == 0xffffffffu) == 0ull) return true;
    if (++spins > SPIN_LIMIT) { if ((threadIdx.x & 63) == 0) atomicAdd(err, 1u); return false; }
  }
}
DI v4f swapadd(v4f a, v4f b) {
  v4f o;
#pragma unroll
  for (int r = 0; r < 4; ++r) {
    auto t = __builtin_amdgcn_permlane32_swap(__float_as_uint(a[r]), __float_as_uint(b[r]), false, false);
    o[r] = __uint_as_float(t[0]) + __uint_as_float(t[1]);
  }
  return o;
}
DI float xmax(float v) {
  v = fmaxf(v, ror8f(v));
  { auto t = __builtin_amdgcn_permlane16_swap(__float_as_uint(v), __float_as_uint(v), false, false); v = fmaxf(__uint_as_float(t[0]), __uint_as_float(t[1])); }
  { auto t = __builtin_amdgcn_permlane32_swap(__float_as_uint(v), __float_as_uint(v), false, false); v = fmaxf(__uint_as_float(t[0]), __uint_as_float(t[1])); }
  return v;
}
DI float xsum(float v) {
  v = v + ror8f(v);
  { auto t = __builtin_amdgcn_permlane16_swap(__float_as_uint(v), __float_as_uint(v), false, false); v = __uint_as_float(t[0]) + __uint_as_float(t[1]); }
  { auto t = __builtin_amdgcn_permlane32_swap(__float_as_uint(v), __float_as_uint(v), false, false); v = __uint_as_float(t[0]) + __uint_as_float(t[1]); }
  return v;
}
constexpr float SCL = 0.08838834764831845f * 1.4426950408889634f;

DI void role_a(u8* __restrict__ ws, u8* comm, int h, u8* lds) {
  const int tid = threadIdx.x, w = __builtin_amdgcn_readfirstlane(tid >> 6), lane = tid & 63, n = lane & 15, g = lane >> 4;
  unsigned* err = (unsigned*)(ws + OFF_ERR);
  const __amdgpu_buffer_rsrc_t rc = __builtin_amdgcn_make_buffer_rsrc(comm, 0, 4 << 20, 0x00020000);
  v8u Wq[2][8], Wk[2][8];
  {
    const v4u* src = (const v4u*)(ws + OFF_WA + (size_t)(h * 4 + w) * 65536) + lane;
#pragma unroll
    for (int tp = 0; tp < 2; ++tp)
#pragma unroll
      for (int kk = 0; kk < 8; ++kk) {
        const v4u a0 = src[(((tp * 8 + kk) * 2 + 0) * 2 + 0) * 64], a1 = src[(((tp * 8 + kk) * 2 + 0) * 2 + 1) * 64];
        const v4u b0 = src[(((tp * 8 + kk) * 2 + 1) * 2 + 0) * 64], b1 = src[(((tp * 8 + kk) * 2 + 1) * 2 + 1) * 64];
        Wq[tp][kk] = __builtin_shufflevector(a0, a1, 0, 1, 2, 3, 4, 5, 6, 7);
        Wk[tp][kk] = __builtin_shufflevector(b0, b1, 0, 1, 2, 3, 4, 5, 6, 7);
      }
  }
  const int idx = (n < 8) ? 0x4444 : 0xEEEE;
  const bool hi = n >= 8;
  v4u kf[2][4];
  v8u uf[4];
  auto prefetch = [&](int s) {
    const v4u* ks_ = (const v4u*)(ws + OFF_K + (size_t)(s * 32 + h) * 8192) + lane;
#pragma unroll
    for (int t = 0; t < 2; ++t)
#pragma unroll
      for (int ks = 0; ks < 4; ++ks) kf[t][ks] = ks_[(t * 4 + ks) * 64];
    const u8* ut = ws + OFF_UT + (size_t)(s * 32 + h) * 32768 + (128 * w + n) * 64 + (g >> 1) * 16 + (g & 1) * 8;
#pragma unroll
    for (int f = 0; f < 4; ++f) {
      const v2u a0 = *(const v2u*)(ut + f * 2048), a1 = *(const v2u*)(ut + f * 2048 + 32);
      const v2u b0 = *(const v2u*)(ut + f * 2048 + 1024), b1 = *(const v2u*)(ut + f * 2048 + 1024 + 32);
      uf[f] = (v8u){a0[0], b0[0], a0[1], b0[1], a1[0], b1[0], a1[1], b1[1]};
    }
#pragma unroll
    for (int f = 0; f < 4; ++f) asm volatile("" : "+v"(uf[f]));
  };
  prefetch(0);
  const unsigned xoff = (unsigned)C_PX;
  for (int s = 0; s < NSEG; ++s) {
    v4u a[8];
    if (!poll<8>(rc, C_M + cmap((s & 3) * 8192) + lane * 16, a, err)) return;
    u8* img = lds + (s & 1) * 4096;
#pragma unroll
    for (int tp = 0; tp < 2; ++tp) {
      v4f aq = {0, 0, 0, 0}, ak = {0, 0, 0, 0};
#pragma unroll
      for (int kk = 0; kk < 8; ++kk) { aq = smfmac(a[kk], Wq[tp][kk], aq, idx); ak = smfmac(a[kk], Wk[tp][kk], ak, idx); }
      const v4f c = swapadd(aq, ak);
      v2u o = {pack2(c[0], c[1]), pack2(c[2], c[3])};
      *(v2u*)(img + (32 * w + 16 * tp + n) * 32 + g * 8) = o;
    }
    __syncthreads();
    v4u F[4], Fq[4];
#pragma unroll
    for (int ks = 0; ks < 4; ++ks) {
      const u8* base = img + (32 * ks + 4 * g + (n >> 2)) * 32;
      const v2u f0 = trread(base + (n & 3) * 8), f1 = trread(base + 512 + (n & 3) * 8);
      F[ks] = (v4u){f0[0], f0[1], f1[0], f1[1]};
      const v2u q0 = trread(base + (n & 1) * 8), q1 = trread(base + 512 + (n & 1) * 8);
      Fq[ks] = (v4u){q0[0], q0[1], q1[0], q1[1]};
    }
    if (s < NSEG - 1) {
      v4f t0 = {0, 0, 0, 0}, t1 = {0, 0, 0, 0}, mm = {0, 0, 0, 0};
#pragma unroll
      for (int ks = 0; ks < 4; ++ks) { t0 = mfma32(kf[0][ks], Fq[ks], t0); t1 = mfma32(kf[1][ks], Fq[ks], t1); mm = mfma32(F[ks], Fq[ks], mm); }
      float v[6];
#pragma unroll
      for (int r = 0; r < 4; ++r) v[r] = hi ? t1[r] : t0[r];
      v[4] = hi ? mm[2] : mm[0]; v[5] = hi ? mm[3] : mm[1];
      if (g < 2) { v[4] = -INFINITY; v[5] = -INFINITY; }
      float mx = fmaxf(fmaxf(fmaxf(v[0], v[1]), fmaxf(v[2], v[3])), fmaxf(v[4], v[5]));
      mx = xmax(mx) * SCL;
      float sum = 0.f;
#pragma unroll
      for (int r = 0; r < 6; ++r) { v[r] = __builtin_amdgcn_exp2f(v[r] * SCL - mx); sum += v[r]; }
      sum = xsum(sum);
      const float inv = __builtin_amdgcn_rcpf(sum);
      const unsigned p01 = pack2(v[0] * inv, v[1] * inv), p23 = pack2(v[2] * inv, v[3] * inv), p45 = pack2(v[4] * inv, v[5] * inv);
      const unsigned o01 = ror8(p01), o23 = ror8(p23);
      const v4u pa = {hi ? o01 : p01, hi ? o23 : p23, hi ? p01 : o01, hi ? p23 : o23};
      const unsigned slot = (s & 1) * 262144;
#pragma unroll
      for (int f = 0; f < 4; ++f) {
        const v4f d = smfmac(pa, uf[f], (v4f){0, 0, 0, 0}, idx);
        const unsigned col = 128 * w + 32 * f + 16 * (g >> 1) + n;
        v2u o = {nosent(pack2(d[0], d[1])), nosent(pack2(d[2], d[3]))};
        stc8(rc, xoff + cmap(slot + ((col >> 3) * 32 + h) * 128 + (col & 7) * 16 + (g & 1) * 8), o);
      }
      if (w == 0 && g >= 2) {
        const unsigned po = h * 128 + (n & 7) * 16 + (g - 2) * 8 + (hi ? 4 : 0);
        stc4(rc, C_PM + cmap((s & 3) * 4096 + po), nosent(p45));
        stc4(rc, C_PM + cmap(((s + 2) & 3) * 4096 + po), 0xffffffffu);
      }
    }
    if (hi) *(v4u*)(ws + OFF_KM + (size_t)(s * 32 + h) * 2048 + ((w * 4 + g) * 8 + (n & 7)) * 16) = (w == 0) ? F[0] : (w == 1) ? F[1] : (w == 2) ? F[2] : F[3];
    if (s + 1 < NSEG - 1) prefetch(s + 1);
  }
}

DI void role_b(u8* __restrict__ ws, u8* comm, int h, u8* lds) {
  const int tid = threadIdx.x, w = __builtin_amdgcn_readfirstlane(tid >> 6), lane = tid & 63, n = lane & 15, g = lane >> 4;
  unsigned* err = (unsigned*)(ws + OFF_ERR);
  const __amdgpu_buffer_rsrc_t rc = __builtin_amdgcn_make_buffer_rsrc(comm, 0, 4 << 20, 0x00020000);
  v8u Wv[2][8], Wo[4][4];
  {
    const v4u* src = (const v4u*)(ws + OFF_WB + (size_t)h * 262144 + w * 32768) + lane;
#pragma unroll
    for (int t = 0; t < 2; ++t)
#pragma unroll
      for (int kk = 0; kk < 8; ++kk) {
        const v4u a0 = src[((t * 8 + kk) * 2 + 0) * 64], a1 = src[((t * 8 + kk) * 2 + 1) * 64];
        Wv[t][kk] = __builtin_shufflevector(a0, a1, 0, 1, 2, 3, 4, 5, 6, 7);
      }
    const v4u* src2 = (const v4u*)(ws + OFF_WB + (size_t)h * 262144 + 131072 + w * 32768) + lane;
#pragma unroll
    for (int f = 0; f < 4; ++f)
#pragma unroll
      for (int ks = 0; ks < 4; ++ks) {
        const v4u a0 = src2[((f * 4 + ks) * 2 + 0) * 64], a1 = src2[((f * 4 + ks) * 2 + 1) * 64];
        Wo[f][ks] = __builtin_shufflevector(a0, a1, 0, 1, 2, 3, 4, 5, 6, 7);
      }
  }
  const int idx = (n < 8) ? 0x4444 : 0xEEEE;
  for (int s = 0; s < NSEG; ++s) {
    v4u a[8];
    if (!poll<8>(rc, C_M + cmap((s & 3) * 8192) + lane * 16, a, err)) return;
    u8* img = lds + (s & 1) * 2048;
    v4f c0 = {0, 0, 0, 0}, c1 = {0, 0, 0, 0};
#pragma unroll
    for (int kk = 0; kk < 8; ++kk) { c0 = smfmac(a[kk], Wv[0][kk], c0, idx); c1 = smfmac(a[kk], Wv[1][kk], c1, idx); }
    const v4f c = swapadd(c0, c1);
    v2u o = {pack2(c[0], c[1]), pack2(c[2], c[3])};
    *(v2u*)(img + (32 * w + 16 * (g >> 1) + n) * 16 + (g & 1) * 8) = o;
    __syncthreads();
    v4u vf[4];
#pragma unroll
    for (int ks = 0; ks < 4; ++ks) {
      const u8* base = img + (32 * ks + 4 * g + (n >> 2)) * 16 + (n & 1) * 8;
      const v2u f0 = trread(base), f1 = trread(base + 256);
      vf[ks] = (v4u){f0[0], f0[1], f1[0], f1[1]};
    }
    const unsigned slot = (s & 1) * 262144;
    u8* yt = ws + OFF_YT + (size_t)(s * 32 + h) * 8192;
#pragma unroll
    for (int f = 0; f < 4; ++f) {
      v4f d = {0, 0, 0, 0};
#pragma unroll
      for (int ks = 0; ks < 4; ++ks) d = smfmac(vf[ks], Wo[f][ks], d, idx);
      const unsigned col = 128 * w + 32 * f + 16 * (g >> 1) + n;
      v2u oy = {nosent(pack2(d[0], d[1])), nosent(pack2(d[2], d[3]))};
      if (s < NSEG - 1) stc8(rc, C_PY + cmap(slot + ((col >> 3) * 32 + h) * 128 + (col & 7) * 16 + (g & 1) * 8), oy);
      *(v2u*)(yt + col * 16 + (g & 1) * 8) = oy;
    }
  }
}

DI void role_red(u8* __restrict__ ws, u8* comm, int bw) {
  const int lane = threadIdx.x & 63, n = lane & 15, g = lane >> 4;
  unsigned* err = (unsigned*)(ws + OFF_ERR);
  const __amdgpu_buffer_rsrc_t rc = __builtin_amdgcn_make_buffer_rsrc(comm, 0, 4 << 20, 0x00020000);
  v4u sel;
#pragma unroll
  for (int j = 0; j < 4; ++j) sel[j] = ((n & 7) == 2 * j ? 0x3f80u : 0u) | ((n & 7) == 2 * j + 1 ? 0x3f800000u : 0u);
  const v4u sent = {0xffffffffu, 0xffffffffu, 0xffffffffu, 0xffffffffu};
  const bool act = (lane < 32) && (n < 8);
  const unsigned mo = bw * 128 + n * 16 + g * 8;
  for (int s = 0; s < NSEG - 1; ++s) {
    const unsigned blk = cmap((s & 1) * 262144 + bw * 4096) + lane * 16;
    v4u y[4], x[4], p[4];
    if (!poll<4>(rc, C_PY + blk, y, err)) return;
    if (!poll2(rc, C_PX + blk, x, C_PM + cmap((s & 3) * 4096) + lane * 16, p, err)) return;
    v4f d = {0, 0, 0, 0};
#pragma unroll
    for (int u = 0; u < 4; ++u) d = mfma32(x[u], sel, d);
#pragma unroll
    for (int u = 0; u < 4; ++u) d = mfma32(y[u], p[u], d);
    float o[4];
#pragma unroll
    for (int r = 0; r < 4; ++r) {
      const unsigned t = ror8(__float_as_uint(d[r]));
      auto q = __builtin_amdgcn_permlane32_swap(t, t, false, false);
      o[r] = d[r] + __uint_as_float(q[1]);
    }
    v2u ov = {nosent(pack2(o[0], o[1])), nosent(pack2(o[2], o[3]))};
    if (act) stc8(rc, C_M + cmap(((s + 1) & 3) * 8192 + mo), ov);
#pragma unroll
    for (int u = 0; u < 4; ++u) { stc16(rc, C_PX + blk + u * 1024, sent); stc16(rc, C_PY + blk + u * 1024, sent); }
    if (act) stc8(rc, C_M + cmap(((s + 3) & 3) * 8192 + mo), (v2u){0xffffffffu, 0xffffffffu});
  }
}

__global__ void __launch_bounds__(256, 1) k_chain(u8* __restrict__ ws, int layer) {
  extern __shared__ __attribute__((aligned(16))) u8 lds[];
  const int b = blockIdx.x;
  if ((b & 7) >= 4) return;
  const int r = (b >> 3) * 4 + (b & 3);
  u8* comm = ws + OFF_COMM + (size_t)layer * 4 * MB;
  if (r < 32) role_a(ws, comm, r, lds);
  else if (r < 64) role_b(ws, comm, r - 32, lds);
  else if (r < 96) { const int w = threadIdx.x >> 6; if (w < 2) role_red(ws, comm, (r - 64) * 2 + w); }
}

DI float gmax(float v) {
  { auto t = __builtin_amdgcn_permlane16_swap(__float_as_uint(v), __float_as_uint(v), false, false); v = fmaxf(__uint_as_float(t[0]), __uint_as_float(t[1])); }
  { auto t = __builtin_amdgcn_permlane32_swap(__float_as_uint(v), __float_as_uint(v), false, false); v = fmaxf(__uint_as_float(t[0]), __uint_as_float(t[1])); }
  return v;
}
DI float gsum(float v) {
  { auto t = __builtin_amdgcn_permlane16_swap(__float_as_uint(v), __float_as_uint(v), false, false); v = __uint_as_float(t[0]) + __uint_as_float(t[1]); }
  { auto t = __builtin_amdgcn_permlane32_swap(__float_as_uint(v), __float_as_uint(v), false, false); v = __uint_as_float(t[0]) + __uint_as_float(t[1]); }
  return v;
}
DI void unit_p(u8* __restrict__ ws, int s, int h) {
  const int lane = threadIdx.x & 63, n = lane & 15, g = lane >> 4;
  const v4u* qs = (const v4u*)(ws + OFF_Q + (size_t)(s * 32 + h) * 8192) + lane;
  const v4u* ks_ = (const v4u*)(ws + OFF_K + (size_t)(s * 32 + h) * 8192) + lane;
  const v4u* km = (const v4u*)(ws + OFF_KM + (size_t)(s * 32 + h) * 2048) + g * 8 + (n & 7);
  v4u kf[2][4], mf[4];
#pragma unroll
  for (int ks = 0; ks < 4; ++ks) { kf[0][ks] = ks_[ks * 64]; kf[1][ks] = ks_[(4 + ks) * 64]; mf[ks] = km[ks * 32]; }
#pragma unroll
  for (int nt = 0; nt < 2; ++nt) {
    v4f t0 = {0, 0, 0, 0}, t1 = {0, 0, 0, 0}, mm = {0, 0, 0, 0};
#pragma unroll
    for (int ks = 0; ks < 4; ++ks) {
      const v4u qf = qs[(nt * 4 + ks) * 64];
      t0 = mfma32(kf[0][ks], qf, t0);
      if (nt == 1) t1 = mfma32(kf[1][ks], qf, t1);
      mm = mfma32(mf[ks], qf, mm);
    }
    const int tok = 16 * nt + n;
    float v[12];
#pragma unroll
    for (int r = 0; r < 4; ++r) {
      v[r] = (4 * g + r <= tok) ? t0[r] : -INFINITY;
      v[4 + r] = (nt == 1 && 16 + 4 * g + r <= tok) ? t1[r] : -INFINITY;
      v[8 + r] = (g < 2) ? mm[r] : -INFINITY;
    }
    float mx = v[0];
#pragma unroll
    for (int r = 1; r < 12; ++r) mx = fmaxf(mx, v[r]);
    mx = gmax(mx) * SCL;
    float sum = 0.f;
#pragma unroll
    for (int r = 0; r < 12; ++r) { v[r] = __builtin_amdgcn_exp2f(v[r] * SCL - mx); sum += v[r]; }
    const float inv = 1.f / gsum(sum);
    v4u o = {pack2(v[0] * inv, v[1] * inv), pack2(v[2] * inv, v[3] * inv), pack2(v[4] * inv, v[5] * inv), pack2(v[6] * inv, v[7] * inv)};
    *(v4u*)(ws + OFF_PF + (size_t)(s * 32 + h) * 2048 + (nt * 64 + lane) * 16) = o;
    if (g < 2) {
      v2u om = {pack2(v[8] * inv, v[9] * inv), pack2(v[10] * inv, v[11] * inv)};
      *(v2u*)(ws + OFF_PM + (size_t)(s * 8 + (h >> 2)) * 2048 + (nt * 64 + (h & 3) * 16 + n) * 16 + g * 8) = om;
    }
  }
}
__global__ void __launch_bounds__(256) k_attnp(u8* __restrict__ ws) {
  const int b = blockIdx.x, w = threadIdx.x >> 6;
  unit_p(ws, b >> 3, (b & 7) * 4 + w);
}

DI void unit_o(const u16* __restrict__ xin, u16* __restrict__ xout, u8* __restrict__ ws, int s, int cb, u8* lds) {
  const int tid = threadIdx.x, w = __builtin_amdgcn_readfirstlane(tid >> 6), lane = tid & 63, n = lane & 15, g = lane >> 4;
  v4f acc[4][2];
#pragma unroll
  for (int mt = 0; mt < 4; ++mt) { acc[mt][0] = (v4f){0, 0, 0, 0}; acc[mt][1] = (v4f){0, 0, 0, 0}; }
#pragma unroll
  for (int hh = 0; hh < 8; ++hh) {
    const int h = w * 8 + hh;
    const v4u* pf = (const v4u*)(ws + OFF_PF + (size_t)(s * 32 + h) * 2048) + lane;
    const v4u b0 = pf[0], b1 = pf[64];
    const u8* ut = ws + OFF_UT + (size_t)(s * 32 + h) * 32768 + (cb * 64 + n) * 64 + g * 16;
#pragma unroll
    for (int mt = 0; mt < 4; ++mt) {
      const v4u a = *(const v4u*)(ut + mt * 1024);
      acc[mt][0] = mfma32(a, b0, acc[mt][0]); acc[mt][1] = mfma32(a, b1, acc[mt][1]);
    }
  }
#pragma unroll
  for (int hq2 = 0; hq2 < 2; ++hq2) {
    const int hq = w * 2 + hq2;
    const v4u* pm = (const v4u*)(ws + OFF_PM + (size_t)(s * 8 + hq) * 2048) + lane;
    const v4u b0 = pm[0], b1 = pm[64];
    const u8* yt = ws + OFF_YT + (size_t)(s * 32 + hq * 4 + g) * 8192 + (cb * 64 + n) * 16;
#pragma unroll
    for (int mt = 0; mt < 4; ++mt) {
      const v4u a = *(const v4u*)(yt + mt * 256);
      acc[mt][0] = mfma32(a, b0, acc[mt][0]); acc[mt][1] = mfma32(a, b1, acc[mt][1]);
    }
  }
  v4f* red = (v4f*)lds;
#pragma unroll
  for (int mt = 0; mt < 4; ++mt) { red[(w * 8 + mt * 2 + 0) * 64 + lane] = acc[mt][0]; red[(w * 8 + mt * 2 + 1) * 64 + lane] = acc[mt][1]; }
  __syncthreads();
#pragma unroll
  for (int i = 0; i < 2; ++i) {
    const int tile = w * 2 + i, mt = tile >> 1, nt = tile & 1;
    v4f c = red[tile * 64 + lane] + red[(8 + tile) * 64 + lane] + red[(16 + tile) * 64 + lane] + red[(24 + tile) * 64 + lane];
    const size_t off = (size_t)(s * 32 + 16 * nt + n) * 512 + cb * 64 + 16 * mt + 4 * g;
    const v2u xv = *(const v2u*)(xin + off);
    v2u o = {pack2(bflo(xv[0]) + rbf(c[0]), bfhi(xv[0]) + rbf(c[1])), pack2(bflo(xv[1]) + rbf(c[2]), bfhi(xv[1]) + rbf(c[3]))};
    *(v2u*)(xout + off) = o;
  }
  __syncthreads();
}
__global__ void __launch_bounds__(256) k_attno(const u16* __restrict__ xin, u16* __restrict__ xout, u8* __restrict__ ws) {
  __shared__ __attribute__((aligned(16))) u8 lds[32768];
  unit_o(xin, xout, ws, blockIdx.x >> 3, blockIdx.x & 7, lds);
}

DI float phi(float t) {
  const float a = t > 0.f ? rbf(t + 1.f) : rbf(__expf(t));
  return a + 1e-6f;
}
DI void role_b1(const u16* __restrict__ x, u8* __restrict__ ws, int j, int s0, int s1, int grp, u8* lds) {
  const int tid = threadIdx.x, w = __builtin_amdgcn_readfirstlane(tid >> 6), lane = tid & 63;
  const bool isq = j >= 8;
  const int ct0 = isq ? (8 * (j - 8) + 2 * w) : (32 + 4 * j + w), ct1 = isq ? ct0 + 1 : ct0 + 32;
  v4u W[2][16];
  {
    const v4u* src = (const v4u*)(ws + OFF_WB1) + lane;
#pragma unroll
    for (int ks = 0; ks < 16; ++ks) { W[0][ks] = src[(ct0 * 16 + ks) * 64]; W[1][ks] = src[(ct1 * 16 + ks) * 64]; }
  }
  v4f kv = {0, 0, 0, 0}, zz = {0, 0, 0, 0};
  const v4u ones = {0x3f803f80u, 0x3f803f80u, 0x3f803f80u, 0x3f803f80u};
  v4u xr[8];
  ln_load(x, s0, xr);
  for (int s = s0; s < s1; ++s) {
    u8* tile = lds + (s & 1) * 32768;
    ln_store(xr, tile);
    if (s + 1 < s1) ln_load(x, s + 1, xr);
    __syncthreads();
    v4f acc[2][2];
#pragma unroll
    for (int c = 0; c < 2; ++c) { acc[c][0] = (v4f){0, 0, 0, 0}; acc[c][1] = (v4f){0, 0, 0, 0}; }
    const v4u* tf = (const v4u*)tile + lane;
    if (isq) {
#pragma unroll
      for (int ks = 0; ks < 16; ++ks) {
        const v4u b0 = tf[ks * 64], b1 = tf[(16 + ks) * 64];
#pragma unroll
        for (int c = 0; c < 2; ++c) { acc[c][0] = mfma32(W[c][ks], b0, acc[c][0]); acc[c][1] = mfma32(W[c][ks], b1, acc[c][1]); }
      }
#pragma unroll
      for (int nt = 0; nt < 2; ++nt) {
        v4u o;
#pragma unroll
        for (int c = 0; c < 2; ++c) {
          o[2 * c] = pack2(phi(rbf(acc[c][nt][0])), phi(rbf(acc[c][nt][1])));
          o[2 * c + 1] = pack2(phi(rbf(acc[c][nt][2])), phi(rbf(acc[c][nt][3])));
        }
        *(v4u*)(ws + OFF_PQ + (size_t)(((s * 2 + nt) * 16 + (ct0 >> 1)) * 64 + lane) * 16) = o;
      }
    } else {
#pragma unroll
      for (int ks = 0; ks < 16; ++ks) {
        const v4u b0 = tf[ks * 64], b1 = tf[(16 + ks) * 64];
#pragma unroll
        for (int c = 0; c < 2; ++c) { acc[c][0] = mfma32(b0, W[c][ks], acc[c][0]); acc[c][1] = mfma32(b1, W[c][ks], acc[c][1]); }
      }
      v4u pk, vv;
#pragma unroll
      for (int t = 0; t < 2; ++t) {
        pk[2 * t] = pack2(phi(rbf(acc[0][t][0])), phi(rbf(acc[0][t][1])));
        pk[2 * t + 1] = pack2(phi(rbf(acc[0][t][2])), phi(rbf(acc[0][t][3])));
        vv[2 * t] = pack2(acc[1][t][0], acc[1][t][1]);
        vv[2 * t + 1] = pack2(acc[1][t][2], acc[1][t][3]);
      }
      kv = mfma32(pk, vv, kv);
      zz = mfma32(pk, ones, zz);
    }
  }
  if (!isq) {
    v4f* dst = (v4f*)(ws + OFF_KVP) + ((size_t)(grp * 32 + 4 * j + w) * 2) * 64 + lane;
    dst[0] = kv; dst[64] = zz;
  }
}
__global__ void __launch_bounds__(256) k_b1(const u16* __restrict__ x, u8* __restrict__ ws) {
  extern __shared__ __attribute__((aligned(16))) u8 lds[];
  const int b = blockIdx.x, j = b % 12, grp = b / 12;
  role_b1(x, ws, j, 4 * grp, 4 * grp + 4, grp, lds);
}

__global__ void __launch_bounds__(256) k_b2(const u16* __restrict__ xin, u16* __restrict__ xout, u8* __restrict__ ws) {
  __shared__ __attribute__((aligned(16))) v2u kvs[32 * 2 * 64];
  const int tid = threadIdx.x, w = __builtin_amdgcn_readfirstlane(tid >> 6), lane = tid & 63, n = lane & 15, g = lane >> 4;
  const int tb = blockIdx.x >> 3, cb = blockIdx.x & 7;
  for (int i = tid; i < 32 * 2 * 64; i += 256) {
    v4f c = {0, 0, 0, 0};
    const v4f* src = (const v4f*)(ws + OFF_KVP) + i;
#pragma unroll
    for (int gq = 0; gq < 16; ++gq) c += src[(size_t)gq * 4096];
    kvs[i] = (v2u){pack2(c[0], c[1]), pack2(c[2], c[3])};
  }
  __syncthreads();
  const int s = tb * 4 + w;
  v4f acc[4][2];
#pragma unroll
  for (int mt = 0; mt < 4; ++mt) { acc[mt][0] = (v4f){0, 0, 0, 0}; acc[mt][1] = (v4f){0, 0, 0, 0}; }
  const v4u* wsrc = (const v4u*)(ws + OFF_WB2) + (size_t)(cb * 4) * 16 * 64 + lane;
#pragma unroll 2
  for (int ks = 0; ks < 16; ++ks) {
    v4u of[2];
    const v2u kva = kvs[((2 * ks) * 2 + 0) * 64 + lane], za = kvs[((2 * ks) * 2 + 1) * 64 + lane];
    const v2u kvb = kvs[((2 * ks + 1) * 2 + 0) * 64 + lane], zb = kvs[((2 * ks + 1) * 2 + 1) * 64 + lane];
#pragma unroll
    for (int nt = 0; nt < 2; ++nt) {
      const v4u pq = *(const v4u*)(ws + OFF_PQ + (size_t)(((s * 2 + nt) * 16 + ks) * 64 + lane) * 16);
      const v2u pa = {pq[0], pq[1]}, pb = {pq[2], pq[3]};
      const v4f z0 = {0, 0, 0, 0};
      const v4f na = mfma16(kva, pa, z0), da = mfma16(za, pa, z0), nb = mfma16(kvb, pb, z0), db = mfma16(zb, pb, z0);
      const float ia = 1.f / rbf(rbf(da[0]) + 1e-6f), ib = 1.f / rbf(rbf(db[0]) + 1e-6f);
      of[nt] = (v4u){pack2(rbf(na[0]) * ia, rbf(na[1]) * ia), pack2(rbf(na[2]) * ia, rbf(na[3]) * ia),
                     pack2(rbf(nb[0]) * ib, rbf(nb[1]) * ib), pack2(rbf(nb[2]) * ib, rbf(nb[3]) * ib)};
    }
#pragma unroll
    for (int mt = 0; mt < 4; ++mt) {
      const v4u a = wsrc[(mt * 16 + ks) * 64];
      acc[mt][0] = mfma32(a, of[0], acc[mt][0]); acc[mt][1] = mfma32(a, of[1], acc[mt][1]);
    }
  }
#pragma unroll
  for (int mt = 0; mt < 4; ++mt)
#pragma unroll
    for (int nt = 0; nt < 2; ++nt) {
      const v4f c = acc[mt][nt];
      const size_t off = (size_t)(s * 32 + 16 * nt + n) * 512 + cb * 64 + 16 * mt + 4 * g;
      const v2u xv = *(const v2u*)(xin + off);
      v2u o = {pack2(bflo(xv[0]) + rbf(c[0]), bfhi(xv[0]) + rbf(c[1])), pack2(bflo(xv[1]) + rbf(c[2]), bfhi(xv[1]) + rbf(c[3]))};
      *(v2u*)(xout + off) = o;
    }
}

extern "C" void kernel_launch(void* const* d_in, const int* in_sizes, int n_in, void* d_out, int out_size, void* d_ws,
                              size_t ws_size, hipStream_t stream) {
  const u16* x = (const u16*)d_in[0];
  const u16* mem0 = (const u16*)d_in[1];
  const u16* Wq = (const u16*)d_in[2];
  const u16* Wk = (const u16*)d_in[3];
  const u16* Wv = (const u16*)d_in[4];
  const u16* Wo = (const u16*)d_in[5];
  const u16* wqkv = (const u16*)d_in[8];
  const u16* wo2 = (const u16*)d_in[9];
  u8* ws = (u8*)d_ws;
  hipFuncSetAttribute((const void*)k_qku, hipFuncAttributeMaxDynamicSharedMemorySize, 80 * 1024);
  hipFuncSetAttribute((const void*)k_chain, hipFuncAttributeMaxDynamicSharedMemorySize, 96 * 1024);
  hipFuncSetAttribute((const void*)k_b1, hipFuncAttributeMaxDynamicSharedMemorySize, 64 * 1024);
  const unsigned nprep = 4 * (8 * MB / 16) + 128 * 16 * 64 + 8 * MB / 16;
  k_prep<<<(nprep + 255) / 256, 256, 0, stream>>>(Wq, Wk, Wv, Wo, wqkv, wo2, mem0, ws);
  const u16* xin = x;
  for (int layer = 0; layer < 2; ++layer) {
    u16* xa = (u16*)(ws + (layer ? OFF_X3 : OFF_X1));
    u16* xb = layer ? (u16*)d_out : (u16*)(ws + OFF_X2);
    k_qku<<<256, 256, 80 * 1024, stream>>>(xin, ws);
    k_chain<<<256, 256, 96 * 1024, stream>>>(ws, layer);
    k_attnp<<<512, 256, 0, stream>>>(ws);
    k_attno<<<512, 256, 0, stream>>>(xin, xa, ws);
    k_b1<<<192, 256, 64 * 1024, stream>>>(xa, ws);
    k_b2<<<128, 256, 0, stream>>>(xa, xb, ws);
    xin = xb;
  }
}
```

```cpp
#include <hip/hip_runtime.h>
#include <hip/hip_bf16.h>
#include <stdint.h>

typedef unsigned v2u __attribute__((ext_vector_type(2)));
typedef unsigned v4u __attribute__((ext_vector_type(4)));
typedef unsigned v8u __attribute__((ext_vector_type(8)));
typedef float v2f __attribute__((ext_vector_type(2)));
typedef float v4f __attribute__((ext_vector_type(4)));
typedef short s16x4 __attribute__((ext_vector_type(4)));
typedef __bf16 bf16x2 __attribute__((ext_vector_type(2)));
typedef __bf16 bf16x4 __attribute__((ext_vector_type(4)));
typedef __bf16 bf16x8 __attribute__((ext_vector_type(8)));
typedef __bf16 bf16x16 __attribute__((ext_vector_type(16)));
typedef unsigned char u8;
typedef unsigned short u16;
#define DI __device__ __forceinline__

DI unsigned pack2(float a, float b) {
  v2f f = {a, b};
  return __builtin_bit_cast(unsigned, __builtin_convertvector(f, bf16x2));
}
DI float bflo(unsigned u) { return __uint_as_float(u << 16); }
DI float bfhi(unsigned u) { return __uint_as_float(u & 0xffff0000u); }
DI float rbf(float a) { return bflo(pack2(a, a)); }
DI v4f mfma32(v4u a, v4u b, v4f c) {
  return __builtin_amdgcn_mfma_f32_16x16x32_bf16(__builtin_bit_cast(bf16x8, a), __builtin_bit_cast(bf16x8, b), c, 0, 0, 0);
}
DI v4f mfma16(v2u a, v2u b, v4f c) {
  return __builtin_amdgcn_mfma_f32_16x16x16bf16_1k(__builtin_bit_cast(s16x4, a), __builtin_bit_cast(s16x4, b), c, 0, 0, 0);
}
DI v4f smfmac(v4u a, v8u b, v4f c, int idx) {
  return __builtin_amdgcn_smfmac_f32_16x16x64_bf16(__builtin_bit_cast(bf16x8, a), __builtin_bit_cast(bf16x16, b), c, idx, 0, 0);
}
typedef __attribute__((address_space(3))) bf16x4* lds_bf4p;
DI v2u trread(const u8* lds_ptr) {
  return __builtin_bit_cast(v2u, __builtin_amdgcn_ds_read_tr16_b64_v4bf16((lds_bf4p)lds_ptr));
}
DI unsigned ror8(unsigned v) { return __builtin_amdgcn_update_dpp(0u, v, 0x128, 0xf, 0xf, false); }
DI float ror8f(float v) { return __uint_as_float(ror8(__float_as_uint(v))); }
DI unsigned max3u(unsigned a, unsigned b, unsigned c) { unsigned d; asm("v_max3_u32 %0, %1, %2, %3" : "=v"(d) : "v"(a), "v"(b), "v"(c)); return d; }
DI v4u ldc16(__amdgpu_buffer_rsrc_t r, unsigned off) { return __builtin_amdgcn_raw_buffer_load_b128(r, off, 0, 16); }
DI void stc16(__amdgpu_buffer_rsrc_t r, unsigned off, v4u v) { __builtin_amdgcn_raw_buffer_store_b128(v, r, off, 0, 16); }
DI void stc8(__amdgpu_buffer_rsrc_t r, unsigned off, v2u v) { __builtin_amdgcn_raw_buffer_store_b64(v, r, off, 0, 16); }
DI void stc4(__amdgpu_buffer_rsrc_t r, unsigned off, unsigned v) { __builtin_amdgcn_raw_buffer_store_b32(v, r, off, 0, 16); }
DI unsigned nosent(unsigned v) { return v < 0xfffeffffu ? v : 0xfffeffffu; }

constexpr int NSEG = 64;
constexpr unsigned MB = 1u << 20;
constexpr int NF = 4, SF = NSEG - NF;
constexpr unsigned OFF_KM = 0;
constexpr unsigned OFF_PF = 6 * MB;
constexpr unsigned OFF_PM = 12 * MB;
constexpr unsigned OFF_YT = 14 * MB;
constexpr unsigned OFF_Q = 33 * MB;
constexpr unsigned OFF_K = 50 * MB;
constexpr unsigned OFF_UT = 68 * MB;
constexpr unsigned HQ = 66 * 8192, HU = 66 * 32768;
constexpr unsigned OFF_XF = 166 * MB;
constexpr unsigned OFF_PQ = 134 * MB;
constexpr unsigned OFF_COMM = 176 * MB;
constexpr unsigned C_PX = 0, C_RING = 1u << 20, C_PY = 2u << 20;
constexpr unsigned C_FL = 0x1a0000u;
constexpr unsigned L_PR = C_FL + 8192;
constexpr unsigned L_A = C_FL + 128 * 128;
constexpr unsigned L_P = C_FL + 512 * 128;
constexpr unsigned L_O = C_FL + 256 * 128;
constexpr unsigned L_X2 = C_FL + 736 * 128;
constexpr unsigned L_B1 = C_FL + 416 * 128;
constexpr unsigned L_KV = C_FL + 448 * 128;
constexpr unsigned C_PP = 0x1c0000u;
constexpr unsigned C_CAL = 0x1f0000u;
constexpr unsigned OFF_ERR = 180 * MB;
constexpr unsigned OFF_EP = OFF_ERR + 128, OFF_IN = OFF_ERR + 4096;
constexpr unsigned OFF_ST = 181 * MB;
constexpr unsigned OFF_KV = 182 * MB;
constexpr unsigned WS_BYTES = 188 * MB;
constexpr int POLL_LIMIT = 1 << 16;
constexpr int SLOW = 8, NAPM = 60, INAP = 800, FSL = 24;
constexpr int PLACE = 0, TCH = 0;

#define TS(role, idx) do { } while (0)
DI int slot_of(int s, int layer) { return s + (s >= SF ? NF * layer : 0); }
DI unsigned cmap(unsigned o) { return ((o >> 13) << 14) + (o & 8191u); }
DI float gmax(float v) {
  { auto t = __builtin_amdgcn_permlane16_swap(__float_as_uint(v), __float_as_uint(v), false, false); v = fmaxf(__uint_as_float(t[0]), __uint_as_float(t[1])); }
  { auto t = __builtin_amdgcn_permlane32_swap(__float_as_uint(v), __float_as_uint(v), false, false); v = fmaxf(__uint_as_float(t[0]), __uint_as_float(t[1])); }
  return v;
}
DI float gsum(float v) {
  { auto t = __builtin_amdgcn_permlane16_swap(__float_as_uint(v), __float_as_uint(v), false, false); v = __uint_as_float(t[0]) + __uint_as_float(t[1]); }
  { auto t = __builtin_amdgcn_permlane32_swap(__float_as_uint(v), __float_as_uint(v), false, false); v = __uint_as_float(t[0]) + __uint_as_float(t[1]); }
  return v;
}
DI v2u chunk_stats(v4u o) {
  float s = 0.f;
#pragma unroll
  for (int j = 0; j < 4; ++j) s += bflo(o[j]) + bfhi(o[j]);
  s = gsum(s);
  const float m = s * (1.f / 32.f);
  float q = 0.f;
#pragma unroll
  for (int j = 0; j < 4; ++j) { const float a = bflo(o[j]) - m, b = bfhi(o[j]) - m; q += a * a + b * b; }
  q = gsum(q);
  return (v2u){__float_as_uint(s), __float_as_uint(q)};
}
DI unsigned st_off(int buf, int s, int nt, int ks, int n) {
  return OFF_ST + buf * 262144u + s * 4096u + (((ks >> 1) & 3) * 64 + (nt * 2 + (ks >> 3)) * 16 + n) * 16 + (ks & 1) * 8;
}

typedef __amdgpu_buffer_rsrc_t rsrc_t;
DI v2u ldc8(rsrc_t r, unsigned off) { return __builtin_amdgcn_raw_buffer_load_b64(r, off, 0, 16); }
DI unsigned ldc4(rsrc_t r, unsigned off) { return __builtin_amdgcn_raw_buffer_load_b32(r, off, 0, 16); }
#define DRAIN() asm volatile("s_waitcnt vmcnt(0)" ::: "memory")
#define CBAR() asm volatile("" ::: "memory")
DI int ltid() { int t = threadIdx.x; asm volatile("" : "+v"(t)); return t; }
DI v4u ldc16s(rsrc_t r, unsigned v, unsigned so) { return __builtin_amdgcn_raw_buffer_load_b128(r, v, so, 16); }
DI v2u ldc8s(rsrc_t r, unsigned v, unsigned so) { return __builtin_amdgcn_raw_buffer_load_b64(r, v, so, 16); }
DI void stc16s(rsrc_t r, unsigned v, unsigned so, v4u d) { stc16(r, v + so, d); }
DI void touch(rsrc_t r, unsigned off) { const unsigned d = ldc4(r, off); asm volatile("" :: "v"(d)); }
DI void fail(u8* ws) { if ((threadIdx.x & 63) == 0) atomicAdd((unsigned*)(ws + OFF_ERR), 1u); }
template <int SL> DI bool wait_flags(rsrc_t rc, unsigned voff, int tag) {
  for (int it = 0; it < POLL_LIMIT; ++it) {
    CBAR();
    const int v = (int)ldc4(rc, voff);
    if (__all(v >= tag)) { CBAR(); return true; }
    __builtin_amdgcn_s_sleep(SL);
  }
  return false;
}
DI bool wait_flags2(rsrc_t rc, unsigned voff, int tag, bool fast) { return fast ? wait_flags<1>(rc, voff, tag) : wait_flags<SLOW>(rc, voff, tag); }
DI unsigned now32() { return __builtin_amdgcn_readfirstlane((unsigned)__builtin_amdgcn_s_memrealtime()); }
constexpr int STEP = 170;
DI void nap(unsigned until) { for (int i = 0; i < 64 && (int)(until - now32()) > 0; ++i) __builtin_amdgcn_s_sleep(6); }
DI void set_flag(rsrc_t rc, unsigned off, int tag) {
  DRAIN();
  if ((threadIdx.x & 63) == 0) stc4(rc, off, (unsigned)tag);
}
DI void wg_flag(rsrc_t rc, unsigned off, int tag) {
  DRAIN();
  __syncthreads();
  if (threadIdx.x == 0) stc4(rc, off, (unsigned)tag);
}
DI unsigned mx4(v4u a, unsigned m) { return max3u(max3u(a[0], a[1], a[2]), a[3], m); }
DI bool all_set(unsigned m) { return !__any(m == 0xffffffffu); }
DI v4u nosent4(v4u v) { return (v4u){nosent(v[0]), nosent(v[1]), nosent(v[2]), nosent(v[3])}; }
typedef int v4i __attribute__((ext_vector_type(4)));
DI v4i mkrs(const void* p, unsigned bytes) {
  return (v4i){__builtin_amdgcn_readfirstlane((int)(uintptr_t)p), __builtin_amdgcn_readfirstlane((int)((uintptr_t)p >> 32)),
               __builtin_amdgcn_readfirstlane((int)bytes), 0x00020000};
}
DI void tile_dma(v4i rs, unsigned vl, int w, int buf, int s, unsigned ldst, unsigned ldss) {
  const unsigned o = OFF_XF + buf * 2 * MB + s * 32768u + w * 8192, os = OFF_ST + buf * 262144u + s * 4096u + w * 1024;
  asm volatile(
      "s_mov_b32 m0, %4\n\ts_nop 0\n\t"
      "buffer_load_dwordx4 %0, %1, %2 offen sc1 lds\n\tbuffer_load_dwordx4 %0, %1, %2 offen offset:1024 sc1 lds\n\t"
      "buffer_load_dwordx4 %0, %1, %2 offen offset:2048 sc1 lds\n\tbuffer_load_dwordx4 %0, %1, %2 offen offset:3072 sc1 lds\n\t"
      "s_mov_b32 m0, %5\n\ts_nop 0\n\t"
      "buffer_load_dwordx4 %0, %1, %3 offen sc1 lds\n\tbuffer_load_dwordx4 %0, %1, %3 offen offset:1024 sc1 lds\n\t"
      "buffer_load_dwordx4 %0, %1, %3 offen offset:2048 sc1 lds\n\tbuffer_load_dwordx4 %0, %1, %3 offen offset:3072 sc1 lds\n\t"
      "s_mov_b32 m0, %7\n\ts_nop 0\n\t"
      "buffer_load_dwordx4 %0, %1, %6 offen sc1 lds\n\t"
      :: "v"(vl), "s"(rs), "s"(o), "s"(o + 4096), "s"(ldst + w * 8192), "s"(ldst + w * 8192 + 4096), "s"(os), "s"(ldss + w * 1024) : "m0", "memory");
}
DI int fx_val(int buf, int s) { return (buf == 3 ? 64 : 0) + s + 1; }
#define LDS3(T, off) ((__attribute__((address_space(3))) T*)(off))
DI float sw16(float v) { auto t = __builtin_amdgcn_permlane16_swap(__float_as_uint(v), __float_as_uint(v), false, false); return __uint_as_float(t[0]) + __uint_as_float(t[1]); }
DI void row_stats(const v4u (&a)[4], float& rs0, float& mr0, float& rs1, float& mr1) {
  float sm = 0.f, q = 0.f, d2 = 0.f;
#pragma unroll
  for (int i = 0; i < 4; ++i) { sm += __uint_as_float(a[i][0]) + __uint_as_float(a[i][2]); q += __uint_as_float(a[i][1]) + __uint_as_float(a[i][3]); }
  const float m = sw16(sm) * (1.f / 512.f);
#pragma unroll
  for (int i = 0; i < 4; ++i) {
    const float d0 = __uint_as_float(a[i][0]) * (1.f / 32.f) - m, d1 = __uint_as_float(a[i][2]) * (1.f / 32.f) - m;
    d2 += d0 * d0 + d1 * d1;
  }
  const float rs = __builtin_amdgcn_rsqf(sw16(q + 32.f * d2) * (1.f / 512.f) + 1e-5f), mr = -m * rs;
  auto t = __builtin_amdgcn_permlane32_swap(__float_as_uint(rs), __float_as_uint(rs), false, false);
  auto u = __builtin_amdgcn_permlane32_swap(__float_as_uint(mr), __float_as_uint(mr), false, false);
  rs0 = __uint_as_float(t[0]); rs1 = __uint_as_float(t[1]); mr0 = __uint_as_float(u[0]); mr1 = __uint_as_float(u[1]);
}
DI void wg_prewait(rsrc_t rc, unsigned voff, int tag) {
  if (threadIdx.x < 64) wait_flags<32>(rc, voff, tag);
  __syncthreads();
}
DI v4u lnpack(v4f c0, v4f c1, v4f cs0, v4f cs1, float rs, float mr) {
  return (v4u){pack2(c0[0] * rs + cs0[0] * mr, c0[1] * rs + cs0[1] * mr), pack2(c0[2] * rs + cs0[2] * mr, c0[3] * rs + cs0[3] * mr),
               pack2(c1[0] * rs + cs1[0] * mr, c1[1] * rs + cs1[1] * mr), pack2(c1[2] * rs + cs1[2] * mr, c1[3] * rs + cs1[3] * mr)};
}
constexpr unsigned ONE2 = 0x3f803f80u;
constexpr unsigned OOB = 0x80000000u;

DI unsigned call_tag(const u8* ws) { return __builtin_amdgcn_readfirstlane(*(const unsigned*)(ws + OFF_EP)) * 0x9e3779b1u + 0x7f4a7c15u; }
DI unsigned near_par(rsrc_t rw) {
  const v4u a = ldc16(rw, OFF_IN), b = ldc16(rw, OFF_IN + 16);
  return __builtin_amdgcn_readfirstlane(b[1] + b[3] < a[1] + a[3] ? 8192u : 0u);
}
DI void w_stage(const u16* __restrict__ pa, const u16* __restrict__ pb, int ld, int c0, int c1, v4u (&W)[2][16], u8* lds) {
  const int tid = ltid(), lane = tid & 63, n = lane & 15, g = lane >> 4, r = tid >> 4;
  const u16* src = (n < 8 ? pa : pb) + (size_t)r * ld + (n & 7) * 8;
  v4u v[32];
#pragma unroll
  for (int i = 0; i < 32; ++i) v[i] = *(const v4u*)(src + (size_t)i * 16 * ld);
  __syncthreads();
  u8* d = lds + r * 256 + ((n >> 1) ^ (r & 7)) * 32 + (n & 1) * 16;
#pragma unroll
  for (int i = 0; i < 32; ++i) *(v4u*)(d + i * 4096) = v[i];
  __syncthreads();
  const int key = 4 * (g & 1) + (n >> 2);
  const u8* s = lds + (4 * g + (n >> 2)) * 256 + (n & 3) * 8;
#pragma unroll
  for (int c = 0; c < 2; ++c)
#pragma unroll
    for (int ks = 0; ks < 16; ++ks) {
      const u8* p = s + (((c ? c1 : c0) ^ key) * 32) + ks * 8192;
      const v2u lo = trread(p), hi = trread(p + 4096);
      W[c][ks] = (v4u){lo[0], lo[1], hi[0], hi[1]};
    }
#pragma unroll
  for (int ks = 0; ks < 16; ++ks) asm volatile("" : "+a"(W[0][ks]), "+a"(W[1][ks]));
  __syncthreads();
}
DI int b2_s0(int t) { const int t8 = t >> 3; return t8 < 4 ? t8 + 9 : t8 == 4 || t8 >= 13 ? t8 : t8 < 9 ? t8 - 5 : t8 - 4; }
DI bool init_ws(u8* ws, rsrc_t rw, int t, int en, unsigned tag, const u16* __restrict__ mem0, const u16* __restrict__ x) {
  const int tid = ltid(), w = __builtin_amdgcn_readfirstlane(tid >> 6), lane = tid & 63, n = lane & 15, g = lane >> 4;
  const unsigned wv = t * 4 + w, tn = now32() + INAP;
  unsigned cal = 0xffffffffu;
  if (en < 8 && tid == 0) {
    for (int rep = 0; rep < 5; ++rep) {
      const unsigned long long t0 = __builtin_amdgcn_s_memtime();
      stc4(rw, OFF_COMM + (en >> 2) * 0x200000u + C_CAL + (en & 3) * 4096 + rep * 128, rep);
      DRAIN();
      cal = min(cal, (unsigned)(__builtin_amdgcn_s_memtime() - t0));
    }
    if (en == 0) stc4(rw, OFF_ERR, 0u);
  }
  v2u xa[4], xb[4];
  v4u m0[4];
#pragma unroll
  for (int i = 0; i < 4; ++i) {
    const unsigned c = wv + 512 * i;
    const u16* xr = x + (size_t)(16 * (c >> 4) + n) * 512 + 32 * (c & 15) + 4 * g;
    xa[i] = *(const v2u*)xr; xb[i] = *(const v2u*)(xr + 16);
  }
#pragma unroll
  for (int u = 0; u < 4; ++u) {
    const unsigned q = (wv & 1) * 256 + u * 64 + lane;
    m0[u] = *(const v4u*)(mem0 + (q & 7) * 512 + (q >> 3) * 8);
  }
  const v4u sent = {0xffffffffu, 0xffffffffu, 0xffffffffu, 0xffffffffu};
#pragma unroll
  for (int u = 0; u < 2; ++u) stc16(rw, OFF_COMM + C_PY + wv * 2048 + u * 1024 + lane * 16, sent);
  {
    const unsigned ob = wv * 4096, o = ob < C_CAL ? OFF_COMM + ob : OOB;
    if (ob - C_RING < 0x20000u) {
#pragma unroll
      for (int u = 0; u < 4; ++u) stc16(rw, o + u * 1024 + lane * 16, nosent4(m0[u]));
    } else {
#pragma unroll
      for (int u = 0; u < 4; ++u) {
        const unsigned ln = u * 8 + (lane >> 3), f = ((ob - L_X2) & ~8192u) ? 0xffffffffu : ln == 4 || ln - 9 < 4 ? 32u : 64u;
        stc16(rw, o + u * 1024 + lane * 16, (v4u){f, f, f, f});
      }
    }
  }
#pragma unroll
  for (int i = 0; i < 4; ++i) {
    const unsigned c = wv + 512 * i;
    const v4u ov = {xa[i][0], xa[i][1], xb[i][0], xb[i][1]};
    stc16(rw, OFF_XF + c * 1024 + lane * 16, ov);
    const v2u st = chunk_stats(ov);
    if (g == 0) stc8(rw, st_off(0, c >> 5, (c >> 4) & 1, c & 15, n), st);
  }
  DRAIN();
  __syncthreads();
  if (tid == 0) stc8(rw, OFF_IN + en * 8, (v2u){tag, cal});
  TS(t, 300);
  nap(tn);
  for (int it = 0;; ++it) {
    CBAR();
    const v4u e = ldc16(rw, OFF_IN + lane * 16);
    if (__all(e[0] == tag && e[2] == tag)) break;
    if (it > POLL_LIMIT) return false;
    __builtin_amdgcn_s_sleep(16);
  }
  CBAR();
  if (t < 40) {
    for (unsigned c0 = wv; c0 < 1096; c0 += 160) {
      unsigned c = c0, o;
      bool ring = false;
      if (c < 512) o = OFF_YT + SF * 262144u + c * 4096;
      else if ((c -= 512) < 128) o = OFF_KM + SF * 65536u + c * 4096;
      else if ((c -= 128) < 128) o = OFF_PF + SF * 65536u + c * 4096;
      else if ((c -= 128) < 160) { o = OFF_COMM + 0x200000u + C_RING + c * 4096; ring = c < 32; }
      else if ((c -= 160) < 32) o = OFF_PM + SF * 16384u + c * 4096;
      else if ((c -= 32) < 32) o = OFF_KV + c * 4096;
      else if ((c -= 32) < 64) o = OFF_XF + (1 + 2 * (c >> 5)) * 2 * MB + SF * 32768u + (c & 31) * 4096;
      else if ((c -= 64) < 8) o = OFF_ST + (1 + 2 * (c >> 2)) * 262144u + (SF + (c & 3)) * 4096u;
      else o = OFF_PQ + NSEG * 32768u + (c - 8) * 4096;
#pragma unroll
      for (int u = 0; u < 4; ++u) { stc16(rw, o + u * 1024 + lane * 16, ring ? nosent4(m0[u]) : sent); __builtin_amdgcn_s_sleep(FSL); }
    }
    wg_flag(rw, OFF_COMM + near_par(rw) + L_X2 + b2_s0(t) * 128 + (t & 7) * 4, 64);
  }
  return true;
}

#define TSP_OPS(role)
#define PR_OPS [vL] "v"(lane * 16), [vLd] "v"(ldsb + lane * 16), [vFl] "v"(ldsb + 110592 + w * 1024 + lane * 16), \
    [vX2] "v"(L_X2 + ((lane >> 1) & 15) * 128 + (lane & 1) * 16), [vB1] "v"(L_B1 + min(lane, 23) * 128), \
    [vG] "v"((4 * w + g) * 8192 + n * 16), [vSw] "v"((4 * w + g) * 256 + ((n >> 1) ^ (4 * (w & 1) + g)) * 32 + (n & 1) * 16), \
    [rs] "s"(mkrs(ws, WS_BYTES)), [rc] "s"(mkrs(ws + OFF_COMM, 4 << 20)), [wv] "s"(w), [ldsb] "s"(ldsb)
DI bool role_qk(u8* __restrict__ ws, const u16* Wq, const u16* Wk, int h, unsigned ldsb) {
  const int tid = threadIdx.x, w = __builtin_amdgcn_readfirstlane(tid >> 6), lane = tid & 63, n = lane & 15, g = lane >> 4;
  int st;
  asm volatile(
      "s_nop 4\n\t"
      "s_mov_b32 s69, 188743808\n\t"
      "s_buffer_load_dword s60, %[rs], s69\n\t"
      "s_mov_b32 s71, 0\n\t"
      "s_mov_b32 s55, 0\n\t"
      "s_mov_b32 s92, 0x3d800000\n\t"
      "s_mov_b32 s93, 0x38800000\n\t"
      "v_mov_b32 v205, 0x3727c5ac\n\t"
      "s_mov_b32 s95, 0x42000000\n\t"
      "s_lshl_b32 s69, %[wv], 13\n\t"
      "s_add_u32 s96, s69, %[ldsb]\n\t"
      "s_lshl_b32 s69, %[wv], 10\n\t"
      "s_add_u32 s97, s69, %[ldsb]\n\t"
      "s_add_u32 s97, s97, 98304\n\t"
      "s_mov_b32 s69, 0\n\t"
      "buffer_load_dwordx4 a[0:3], %[vG], %[rW], s69 offen\n\t"
      "s_mov_b32 s69, 131072\n\t"
      "buffer_load_dwordx4 a[4:7], %[vG], %[rW], s69 offen\n\t"
      "s_mov_b32 s69, 262144\n\t"
      "buffer_load_dwordx4 a[8:11], %[vG], %[rW], s69 offen\n\t"
      "s_mov_b32 s69, 393216\n\t"
      "buffer_load_dwordx4 a[12:15], %[vG], %[rW], s69 offen\n\t"
      "s_mov_b32 s69, 524288\n\t"
      "buffer_load_dwordx4 a[16:19], %[vG], %[rW], s69 offen\n\t"
      "s_mov_b32 s69, 655360\n\t"
      "buffer_load_dwordx4 a[20:23], %[vG], %[rW], s69 offen\n\t"
      "s_mov_b32 s69, 786432\n\t"
      "buffer_load_dwordx4 a[24:27], %[vG], %[rW], s69 offen\n\t"
      "s_mov_b32 s69, 917504\n\t"
      "buffer_load_dwordx4 a[28:31], %[vG], %[rW], s69 offen\n\t"
      "s_mov_b32 s69, 1048576\n\t"
      "buffer_load_dwordx4 a[64:67], %[vG], %[rW], s69 offen\n\t"
      "s_mov_b32 s69, 1179648\n\t"
      "buffer_load_dwordx4 a[68:71], %[vG], %[rW], s69 offen\n\t"
      "s_mov_b32 s69, 1310720\n\t"
      "buffer_load_dwordx4 a[72:75], %[vG], %[rW], s69 offen\n\t"
      "s_mov_b32 s69, 1441792\n\t"
      "buffer_load_dwordx4 a[76:79], %[vG], %[rW], s69 offen\n\t"
      "s_mov_b32 s69, 1572864\n\t"
      "buffer_load_dwordx4 a[80:83], %[vG], %[rW], s69 offen\n\t"
      "s_mov_b32 s69, 1703936\n\t"
      "buffer_load_dwordx4 a[84:87], %[vG], %[rW], s69 offen\n\t"
      "s_mov_b32 s69, 1835008\n\t"
      "buffer_load_dwordx4 a[88:91], %[vG], %[rW], s69 offen\n\t"
      "s_mov_b32 s69, 1966080\n\t"
      "buffer_load_dwordx4 a[92:95], %[vG], %[rW], s69 offen\n\t"
      "s_mov_b32 s69, 0\n\t"
      "buffer_load_dwordx4 a[128:131], %[vG], %[rW2], s69 offen\n\t"
      "s_mov_b32 s69, 131072\n\t"
      "buffer_load_dwordx4 a[132:135], %[vG], %[rW2], s69 offen\n\t"
      "s_mov_b32 s69, 262144\n\t"
      "buffer_load_dwordx4 a[136:139], %[vG], %[rW2], s69 offen\n\t"
      "s_mov_b32 s69, 393216\n\t"
      "buffer_load_dwordx4 a[140:143], %[vG], %[rW2], s69 offen\n\t"
      "s_mov_b32 s69, 524288\n\t"
      "buffer_load_dwordx4 a[144:147], %[vG], %[rW2], s69 offen\n\t"
      "s_mov_b32 s69, 655360\n\t"
      "buffer_load_dwordx4 a[148:151], %[vG], %[rW2], s69 offen\n\t"
      "s_mov_b32 s69, 786432\n\t"
      "buffer_load_dwordx4 a[152:155], %[vG], %[rW2], s69 offen\n\t"
      "s_mov_b32 s69, 917504\n\t"
      "buffer_load_dwordx4 a[156:159], %[vG], %[rW2], s69 offen\n\t"
      "s_mov_b32 s69, 1048576\n\t"
      "buffer_load_dwordx4 a[192:195], %[vG], %[rW2], s69 offen\n\t"
      "s_mov_b32 s69, 1179648\n\t"
      "buffer_load_dwordx4 a[196:199], %[vG], %[rW2], s69 offen\n\t"
      "s_mov_b32 s69, 1310720\n\t"
      "buffer_load_dwordx4 a[200:203], %[vG], %[rW2], s69 offen\n\t"
      "s_mov_b32 s69, 1441792\n\t"
      "buffer_load_dwordx4 a[204:207], %[vG], %[rW2], s69 offen\n\t"
      "s_mov_b32 s69, 1572864\n\t"
      "buffer_load_dwordx4 a[208:211], %[vG], %[rW2], s69 offen\n\t"
      "s_mov_b32 s69, 1703936\n\t"
      "buffer_load_dwordx4 a[212:215], %[vG], %[rW2], s69 offen\n\t"
      "s_mov_b32 s69, 1835008\n\t"
      "buffer_load_dwordx4 a[216:219], %[vG], %[rW2], s69 offen\n\t"
      "s_mov_b32 s69, 1966080\n\t"
      "buffer_load_dwordx4 a[220:223], %[vG], %[rW2], s69 offen\n\t"
      "s_mov_b32 s69, 2097152\n\t"
      "buffer_load_dwordx4 a[32:35], %[vG], %[rW], s69 offen\n\t"
      "s_mov_b32 s69, 2228224\n\t"
      "buffer_load_dwordx4 a[36:39], %[vG], %[rW], s69 offen\n\t"
      "s_mov_b32 s69, 2359296\n\t"
      "buffer_load_dwordx4 a[40:43], %[vG], %[rW], s69 offen\n\t"
      "s_mov_b32 s69, 2490368\n\t"
      "buffer_load_dwordx4 a[44:47], %[vG], %[rW], s69 offen\n\t"
      "s_mov_b32 s69, 2621440\n\t"
      "buffer_load_dwordx4 a[48:51], %[vG], %[rW], s69 offen\n\t"
      "s_mov_b32 s69, 2752512\n\t"
      "buffer_load_dwordx4 a[52:55], %[vG], %[rW], s69 offen\n\t"
      "s_mov_b32 s69, 2883584\n\t"
      "buffer_load_dwordx4 a[56:59], %[vG], %[rW], s69 offen\n\t"
      "s_mov_b32 s69, 3014656\n\t"
      "buffer_load_dwordx4 a[60:63], %[vG], %[rW], s69 offen\n\t"
      "s_mov_b32 s69, 3145728\n\t"
      "buffer_load_dwordx4 a[96:99], %[vG], %[rW], s69 offen\n\t"
      "s_mov_b32 s69, 3276800\n\t"
      "buffer_load_dwordx4 a[100:103], %[vG], %[rW], s69 offen\n\t"
      "s_mov_b32 s69, 3407872\n\t"
      "buffer_load_dwordx4 a[104:107], %[vG], %[rW], s69 offen\n\t"
      "s_mov_b32 s69, 3538944\n\t"
      "buffer_load_dwordx4 a[108:111], %[vG], %[rW], s69 offen\n\t"
      "s_mov_b32 s69, 3670016\n\t"
      "buffer_load_dwordx4 a[112:115], %[vG], %[rW], s69 offen\n\t"
      "s_mov_b32 s69, 3801088\n\t"
      "buffer_load_dwordx4 a[116:119], %[vG], %[rW], s69 offen\n\t"
      "s_mov_b32 s69, 3932160\n\t"
      "buffer_load_dwordx4 a[120:123], %[vG], %[rW], s69 offen\n\t"
      "s_mov_b32 s69, 4063232\n\t"
      "buffer_load_dwordx4 a[124:127], %[vG], %[rW], s69 offen\n\t"
      "s_mov_b32 s69, 2097152\n\t"
      "buffer_load_dwordx4 a[160:163], %[vG], %[rW2], s69 offen\n\t"
      "s_mov_b32 s69, 2228224\n\t"
      "buffer_load_dwordx4 a[164:167], %[vG], %[rW2], s69 offen\n\t"
      "s_mov_b32 s69, 2359296\n\t"
      "buffer_load_dwordx4 a[168:171], %[vG], %[rW2], s69 offen\n\t"
      "s_mov_b32 s69, 2490368\n\t"
      "buffer_load_dwordx4 a[172:175], %[vG], %[rW2], s69 offen\n\t"
      "s_mov_b32 s69, 2621440\n\t"
      "buffer_load_dwordx4 a[176:179], %[vG], %[rW2], s69 offen\n\t"
      "s_mov_b32 s69, 2752512\n\t"
      "buffer_load_dwordx4 a[180:183], %[vG], %[rW2], s69 offen\n\t"
      "s_mov_b32 s69, 2883584\n\t"
      "buffer_load_dwordx4 a[184:187], %[vG], %[rW2], s69 offen\n\t"
      "s_mov_b32 s69, 3014656\n\t"
      "buffer_load_dwordx4 a[188:191], %[vG], %[rW2], s69 offen\n\t"
      "s_mov_b32 s69, 3145728\n\t"
      "buffer_load_dwordx4 a[224:227], %[vG], %[rW2], s69 offen\n\t"
      "s_mov_b32 s69, 3276800\n\t"
      "buffer_load_dwordx4 a[228:231], %[vG], %[rW2], s69 offen\n\t"
      "s_mov_b32 s69, 3407872\n\t"
      "buffer_load_dwordx4 a[232:235], %[vG], %[rW2], s69 offen\n\t"
      "s_mov_b32 s69, 3538944\n\t"
      "buffer_load_dwordx4 a[236:239], %[vG], %[rW2], s69 offen\n\t"
      "s_mov_b32 s69, 3670016\n\t"
      "buffer_load_dwordx4 a[240:243], %[vG], %[rW2], s69 offen\n\t"
      "s_mov_b32 s69, 3801088\n\t"
      "buffer_load_dwordx4 a[244:247], %[vG], %[rW2], s69 offen\n\t"
      "s_mov_b32 s69, 3932160\n\t"
      "buffer_load_dwordx4 a[248:251], %[vG], %[rW2], s69 offen\n\t"
      "s_mov_b32 s69, 4063232\n\t"
      "buffer_load_dwordx4 a[252:255], %[vG], %[rW2], s69 offen\n\t"
      "v_add_u32 v41, %[ldsb], %[vSw]\n\t"
      "v_add_u32 v42, 0x10000, v41\n\t"
      "v_xor_b32 v43, 0, %[vTr]\n\t"
      "v_add_u32 v43, %[ldsb], v43\n\t"
      "v_xor_b32 v44, 32, %[vTr]\n\t"
      "v_add_u32 v44, %[ldsb], v44\n\t"
      "v_xor_b32 v45, 64, %[vTr]\n\t"
      "v_add_u32 v45, %[ldsb], v45\n\t"
      "v_xor_b32 v46, 96, %[vTr]\n\t"
      "v_add_u32 v46, %[ldsb], v46\n\t"
      "s_waitcnt vmcnt(63)\n\t"
      "ds_write_b128 v41, a[0:3] offset:0\n\t"
      "s_waitcnt vmcnt(62)\n\t"
      "ds_write_b128 v41, a[4:7] offset:4096\n\t"
      "s_waitcnt vmcnt(61)\n\t"
      "ds_write_b128 v41, a[8:11] offset:8192\n\t"
      "s_waitcnt vmcnt(60)\n\t"
      "ds_write_b128 v41, a[12:15] offset:12288\n\t"
      "s_waitcnt vmcnt(59)\n\t"
      "ds_write_b128 v41, a[16:19] offset:16384\n\t"
      "s_waitcnt vmcnt(58)\n\t"
      "ds_write_b128 v41, a[20:23] offset:20480\n\t"
      "s_waitcnt vmcnt(57)\n\t"
      "ds_write_b128 v41, a[24:27] offset:24576\n\t"
      "s_waitcnt vmcnt(56)\n\t"
      "ds_write_b128 v41, a[28:31] offset:28672\n\t"
      "s_waitcnt vmcnt(55)\n\t"
      "ds_write_b128 v41, a[64:67] offset:32768\n\t"
      "s_waitcnt vmcnt(54)\n\t"
      "ds_write_b128 v41, a[68:71] offset:36864\n\t"
      "s_waitcnt vmcnt(53)\n\t"
      "ds_write_b128 v41, a[72:75] offset:40960\n\t"
      "s_waitcnt vmcnt(52)\n\t"
      "ds_write_b128 v41, a[76:79] offset:45056\n\t"
      "s_waitcnt vmcnt(51)\n\t"
      "ds_write_b128 v41, a[80:83] offset:49152\n\t"
      "s_waitcnt vmcnt(50)\n\t"
      "ds_write_b128 v41, a[84:87] offset:53248\n\t"
      "s_waitcnt vmcnt(49)\n\t"
      "ds_write_b128 v41, a[88:91] offset:57344\n\t"
      "s_waitcnt vmcnt(48)\n\t"
      "ds_write_b128 v41, a[92:95] offset:61440\n\t"
      "s_waitcnt vmcnt(47)\n\t"
      "ds_write_b128 v42, a[128:131] offset:0\n\t"
      "s_waitcnt vmcnt(46)\n\t"
      "ds_write_b128 v42, a[132:135] offset:4096\n\t"
      "s_waitcnt vmcnt(45)\n\t"
      "ds_write_b128 v42, a[136:139] offset:8192\n\t"
      "s_waitcnt vmcnt(44)\n\t"
      "ds_write_b128 v42, a[140:143] offset:12288\n\t"
      "s_waitcnt vmcnt(43)\n\t"
      "ds_write_b128 v42, a[144:147] offset:16384\n\t"
      "s_waitcnt vmcnt(42)\n\t"
      "ds_write_b128 v42, a[148:151] offset:20480\n\t"
      "s_waitcnt vmcnt(41)\n\t"
      "ds_write_b128 v42, a[152:155] offset:24576\n\t"
      "s_waitcnt vmcnt(40)\n\t"
      "ds_write_b128 v42, a[156:159] offset:28672\n\t"
      "s_waitcnt vmcnt(39)\n\t"
      "ds_write_b128 v42, a[192:195] offset:32768\n\t"
      "s_waitcnt vmcnt(38)\n\t"
      "ds_write_b128 v42, a[196:199] offset:36864\n\t"
      "s_waitcnt vmcnt(37)\n\t"
      "ds_write_b128 v42, a[200:203] offset:40960\n\t"
      "s_waitcnt vmcnt(36)\n\t"
      "ds_write_b128 v42, a[204:207] offset:45056\n\t"
      "s_waitcnt vmcnt(35)\n\t"
      "ds_write_b128 v42, a[208:211] offset:49152\n\t"
      "s_waitcnt vmcnt(34)\n\t"
      "ds_write_b128 v42, a[212:215] offset:53248\n\t"
      "s_waitcnt vmcnt(33)\n\t"
      "ds_write_b128 v42, a[216:219] offset:57344\n\t"
      "s_waitcnt vmcnt(32)\n\t"
      "ds_write_b128 v42, a[220:223] offset:61440\n\t"
      "s_waitcnt lgkmcnt(0)\n\t"
      "s_barrier\n\t"
      "ds_read_b64_tr_b16 a[0:1], v43 offset:0\n\t"
      "ds_read_b64_tr_b16 a[2:3], v43 offset:4096\n\t"
      "ds_read_b64_tr_b16 a[4:5], v43 offset:8192\n\t"
      "ds_read_b64_tr_b16 a[6:7], v43 offset:12288\n\t"
      "ds_read_b64_tr_b16 a[8:9], v43 offset:16384\n\t"
      "ds_read_b64_tr_b16 a[10:11], v43 offset:20480\n\t"
      "ds_read_b64_tr_b16 a[12:13], v43 offset:24576\n\t"
      "ds_read_b64_tr_b16 a[14:15], v43 offset:28672\n\t"
      "ds_read_b64_tr_b16 a[16:17], v43 offset:32768\n\t"
      "ds_read_b64_tr_b16 a[18:19], v43 offset:36864\n\t"
      "ds_read_b64_tr_b16 a[20:21], v43 offset:40960\n\t"
      "ds_read_b64_tr_b16 a[22:23], v43 offset:45056\n\t"
      "ds_read_b64_tr_b16 a[24:25], v43 offset:49152\n\t"
      "ds_read_b64_tr_b16 a[26:27], v43 offset:53248\n\t"
      "ds_read_b64_tr_b16 a[28:29], v43 offset:57344\n\t"
      "ds_read_b64_tr_b16 a[30:31], v43 offset:61440\n\t"
      "ds_read_b64_tr_b16 a[64:65], v44 offset:0\n\t"
      "ds_read_b64_tr_b16 a[66:67], v44 offset:4096\n\t"
      "ds_read_b64_tr_b16 a[68:69], v44 offset:8192\n\t"
      "ds_read_b64_tr_b16 a[70:71], v44 offset:12288\n\t"
      "ds_read_b64_tr_b16 a[72:73], v44 offset:16384\n\t"
      "ds_read_b64_tr_b16 a[74:75], v44 offset:20480\n\t"
      "ds_read_b64_tr_b16 a[76:77], v44 offset:24576\n\t"
      "ds_read_b64_tr_b16 a[78:79], v44 offset:28672\n\t"
      "ds_read_b64_tr_b16 a[80:81], v44 offset:32768\n\t"
      "ds_read_b64_tr_b16 a[82:83], v44 offset:36864\n\t"
      "ds_read_b64_tr_b16 a[84:85], v44 offset:40960\n\t"
      "ds_read_b64_tr_b16 a[86:87], v44 offset:45056\n\t"
      "ds_read_b64_tr_b16 a[88:89], v44 offset:49152\n\t"
      "ds_read_b64_tr_b16 a[90:91], v44 offset:53248\n\t"
      "ds_read_b64_tr_b16 a[92:93], v44 offset:57344\n\t"
      "ds_read_b64_tr_b16 a[94:95], v44 offset:61440\n\t"
      "ds_read_b64_tr_b16 a[128:129], v45 offset:0\n\t"
      "ds_read_b64_tr_b16 a[130:131], v45 offset:4096\n\t"
      "ds_read_b64_tr_b16 a[132:133], v45 offset:8192\n\t"
      "ds_read_b64_tr_b16 a[134:135], v45 offset:12288\n\t"
      "ds_read_b64_tr_b16 a[136:137], v45 offset:16384\n\t"
      "ds_read_b64_tr_b16 a[138:139], v45 offset:20480\n\t"
      "ds_read_b64_tr_b16 a[140:141], v45 offset:24576\n\t"
      "ds_read_b64_tr_b16 a[142:143], v45 offset:28672\n\t"
      "ds_read_b64_tr_b16 a[144:145], v45 offset:32768\n\t"
      "ds_read_b64_tr_b16 a[146:147], v45 offset:36864\n\t"
      "ds_read_b64_tr_b16 a[148:149], v45 offset:40960\n\t"
      "ds_read_b64_tr_b16 a[150:151], v45 offset:45056\n\t"
      "ds_read_b64_tr_b16 a[152:153], v45 offset:49152\n\t"
      "ds_read_b64_tr_b16 a[154:155], v45 offset:53248\n\t"
      "ds_read_b64_tr_b16 a[156:157], v45 offset:57344\n\t"
      "ds_read_b64_tr_b16 a[158:159], v45 offset:61440\n\t"
      "ds_read_b64_tr_b16 a[192:193], v46 offset:0\n\t"
      "ds_read_b64_tr_b16 a[194:195], v46 offset:4096\n\t"
      "ds_read_b64_tr_b16 a[196:197], v46 offset:8192\n\t"
      "ds_read_b64_tr_b16 a[198:199], v46 offset:12288\n\t"
      "ds_read_b64_tr_b16 a[200:201], v46 offset:16384\n\t"
      "ds_read_b64_tr_b16 a[202:203], v46 offset:20480\n\t"
      "ds_read_b64_tr_b16 a[204:205], v46 offset:24576\n\t"
      "ds_read_b64_tr_b16 a[206:207], v46 offset:28672\n\t"
      "ds_read_b64_tr_b16 a[208:209], v46 offset:32768\n\t"
      "ds_read_b64_tr_b16 a[210:211], v46 offset:36864\n\t"
      "ds_read_b64_tr_b16 a[212:213], v46 offset:40960\n\t"
      "ds_read_b64_tr_b16 a[214:215], v46 offset:45056\n\t"
      "ds_read_b64_tr_b16 a[216:217], v46 offset:49152\n\t"
      "ds_read_b64_tr_b16 a[218:219], v46 offset:53248\n\t"
      "ds_read_b64_tr_b16 a[220:221], v46 offset:57344\n\t"
      "ds_read_b64_tr_b16 a[222:223], v46 offset:61440\n\t"
      "s_waitcnt lgkmcnt(0)\n\t"
      "s_barrier\n\t"
      "s_waitcnt vmcnt(31)\n\t"
      "ds_write_b128 v41, a[32:35] offset:0\n\t"
      "s_waitcnt vmcnt(30)\n\t"
      "ds_write_b128 v41, a[36:39] offset:4096\n\t"
      "s_waitcnt vmcnt(29)\n\t"
      "ds_write_b128 v41, a[40:43] offset:8192\n\t"
      "s_waitcnt vmcnt(28)\n\t"
      "ds_write_b128 v41, a[44:47] offset:12288\n\t"
      "s_waitcnt vmcnt(27)\n\t"
      "ds_write_b128 v41, a[48:51] offset:16384\n\t"
      "s_waitcnt vmcnt(26)\n\t"
      "ds_write_b128 v41, a[52:55] offset:20480\n\t"
      "s_waitcnt vmcnt(25)\n\t"
      "ds_write_b128 v41, a[56:59] offset:24576\n\t"
      "s_waitcnt vmcnt(24)\n\t"
      "ds_write_b128 v41, a[60:63] offset:28672\n\t"
      "s_waitcnt vmcnt(23)\n\t"
      "ds_write_b128 v41, a[96:99] offset:32768\n\t"
      "s_waitcnt vmcnt(22)\n\t"
      "ds_write_b128 v41, a[100:103] offset:36864\n\t"
      "s_waitcnt vmcnt(21)\n\t"
      "ds_write_b128 v41, a[104:107] offset:40960\n\t"
      "s_waitcnt vmcnt(20)\n\t"
      "ds_write_b128 v41, a[108:111] offset:45056\n\t"
      "s_waitcnt vmcnt(19)\n\t"
      "ds_write_b128 v41, a[112:115] offset:49152\n\t"
      "s_waitcnt vmcnt(18)\n\t"
      "ds_write_b128 v41, a[116:119] offset:53248\n\t"
      "s_waitcnt vmcnt(17)\n\t"
      "ds_write_b128 v41, a[120:123] offset:57344\n\t"
      "s_waitcnt vmcnt(16)\n\t"
      "ds_write_b128 v41, a[124:127] offset:61440\n\t"
      "s_waitcnt vmcnt(15)\n\t"
      "ds_write_b128 v42, a[160:163] offset:0\n\t"
      "s_waitcnt vmcnt(14)\n\t"
      "ds_write_b128 v42, a[164:167] offset:4096\n\t"
      "s_waitcnt vmcnt(13)\n\t"
      "ds_write_b128 v42, a[168:171] offset:8192\n\t"
      "s_waitcnt vmcnt(12)\n\t"
      "ds_write_b128 v42, a[172:175] offset:12288\n\t"
      "s_waitcnt vmcnt(11)\n\t"
      "ds_write_b128 v42, a[176:179] offset:16384\n\t"
      "s_waitcnt vmcnt(10)\n\t"
      "ds_write_b128 v42, a[180:183] offset:20480\n\t"
      "s_waitcnt vmcnt(9)\n\t"
      "ds_write_b128 v42, a[184:187] offset:24576\n\t"
      "s_waitcnt vmcnt(8)\n\t"
      "ds_write_b128 v42, a[188:191] offset:28672\n\t"
      "s_waitcnt vmcnt(7)\n\t"
      "ds_write_b128 v42, a[224:227] offset:32768\n\t"
      "s_waitcnt vmcnt(6)\n\t"
      "ds_write_b128 v42, a[228:231] offset:36864\n\t"
      "s_waitcnt vmcnt(5)\n\t"
      "ds_write_b128 v42, a[232:235] offset:40960\n\t"
      "s_waitcnt vmcnt(4)\n\t"
      "ds_write_b128 v42, a[236:239] offset:45056\n\t"
      "s_waitcnt vmcnt(3)\n\t"
      "ds_write_b128 v42, a[240:243] offset:49152\n\t"
      "s_waitcnt vmcnt(2)\n\t"
      "ds_write_b128 v42, a[244:247] offset:53248\n\t"
      "s_waitcnt vmcnt(1)\n\t"
      "ds_write_b128 v42, a[248:251] offset:57344\n\t"
      "s_waitcnt vmcnt(0)\n\t"
      "ds_write_b128 v42, a[252:255] offset:61440\n\t"
      "s_mov_b32 s69, 188747776\n\t"
      "buffer_load_dwordx4 v[64:67], %[vL], %[rs], s69 offen sc1\n\t"
      "s_waitcnt lgkmcnt(0)\n\t"
      "s_barrier\n\t"
      "ds_read_b64_tr_b16 a[32:33], v43 offset:0\n\t"
      "ds_read_b64_tr_b16 a[34:35], v43 offset:4096\n\t"
      "ds_read_b64_tr_b16 a[36:37], v43 offset:8192\n\t"
      "ds_read_b64_tr_b16 a[38:39], v43 offset:12288\n\t"
      "ds_read_b64_tr_b16 a[40:41], v43 offset:16384\n\t"
      "ds_read_b64_tr_b16 a[42:43], v43 offset:20480\n\t"
      "ds_read_b64_tr_b16 a[44:45], v43 offset:24576\n\t"
      "ds_read_b64_tr_b16 a[46:47], v43 offset:28672\n\t"
      "ds_read_b64_tr_b16 a[48:49], v43 offset:32768\n\t"
      "ds_read_b64_tr_b16 a[50:51], v43 offset:36864\n\t"
      "ds_read_b64_tr_b16 a[52:53], v43 offset:40960\n\t"
      "ds_read_b64_tr_b16 a[54:55], v43 offset:45056\n\t"
      "ds_read_b64_tr_b16 a[56:57], v43 offset:49152\n\t"
      "ds_read_b64_tr_b16 a[58:59], v43 offset:53248\n\t"
      "ds_read_b64_tr_b16 a[60:61], v43 offset:57344\n\t"
      "ds_read_b64_tr_b16 a[62:63], v43 offset:61440\n\t"
      "ds_read_b64_tr_b16 a[96:97], v44 offset:0\n\t"
      "ds_read_b64_tr_b16 a[98:99], v44 offset:4096\n\t"
      "ds_read_b64_tr_b16 a[100:101], v44 offset:8192\n\t"
      "ds_read_b64_tr_b16 a[102:103], v44 offset:12288\n\t"
      "ds_read_b64_tr_b16 a[104:105], v44 offset:16384\n\t"
      "ds_read_b64_tr_b16 a[106:107], v44 offset:20480\n\t"
      "ds_read_b64_tr_b16 a[108:109], v44 offset:24576\n\t"
      "ds_read_b64_tr_b16 a[110:111], v44 offset:28672\n\t"
      "ds_read_b64_tr_b16 a[112:113], v44 offset:32768\n\t"
      "ds_read_b64_tr_b16 a[114:115], v44 offset:36864\n\t"
      "ds_read_b64_tr_b16 a[116:117], v44 offset:40960\n\t"
      "ds_read_b64_tr_b16 a[118:119], v44 offset:45056\n\t"
      "ds_read_b64_tr_b16 a[120:121], v44 offset:49152\n\t"
      "ds_read_b64_tr_b16 a[122:123], v44 offset:53248\n\t"
      "ds_read_b64_tr_b16 a[124:125], v44 offset:57344\n\t"
      "ds_read_b64_tr_b16 a[126:127], v44 offset:61440\n\t"
      "ds_read_b64_tr_b16 a[160:161], v45 offset:0\n\t"
      "ds_read_b64_tr_b16 a[162:163], v45 offset:4096\n\t"
      "ds_read_b64_tr_b16 a[164:165], v45 offset:8192\n\t"
      "ds_read_b64_tr_b16 a[166:167], v45 offset:12288\n\t"
      "ds_read_b64_tr_b16 a[168:169], v45 offset:16384\n\t"
      "ds_read_b64_tr_b16 a[170:171], v45 offset:20480\n\t"
      "ds_read_b64_tr_b16 a[172:173], v45 offset:24576\n\t"
      "ds_read_b64_tr_b16 a[174:175], v45 offset:28672\n\t"
      "ds_read_b64_tr_b16 a[176:177], v45 offset:32768\n\t"
      "ds_read_b64_tr_b16 a[178:179], v45 offset:36864\n\t"
      "ds_read_b64_tr_b16 a[180:181], v45 offset:40960\n\t"
      "ds_read_b64_tr_b16 a[182:183], v45 offset:45056\n\t"
      "ds_read_b64_tr_b16 a[184:185], v45 offset:49152\n\t"
      "ds_read_b64_tr_b16 a[186:187], v45 offset:53248\n\t"
      "ds_read_b64_tr_b16 a[188:189], v45 offset:57344\n\t"
      "ds_read_b64_tr_b16 a[190:191], v45 offset:61440\n\t"
      "ds_read_b64_tr_b16 a[224:225], v46 offset:0\n\t"
      "ds_read_b64_tr_b16 a[226:227], v46 offset:4096\n\t"
      "ds_read_b64_tr_b16 a[228:229], v46 offset:8192\n\t"
      "ds_read_b64_tr_b16 a[230:231], v46 offset:12288\n\t"
      "ds_read_b64_tr_b16 a[232:233], v46 offset:16384\n\t"
      "ds_read_b64_tr_b16 a[234:235], v46 offset:20480\n\t"
      "ds_read_b64_tr_b16 a[236:237], v46 offset:24576\n\t"
      "ds_read_b64_tr_b16 a[238:239], v46 offset:28672\n\t"
      "ds_read_b64_tr_b16 a[240:241], v46 offset:32768\n\t"
      "ds_read_b64_tr_b16 a[242:243], v46 offset:36864\n\t"
      "ds_read_b64_tr_b16 a[244:245], v46 offset:40960\n\t"
      "ds_read_b64_tr_b16 a[246:247], v46 offset:45056\n\t"
      "ds_read_b64_tr_b16 a[248:249], v46 offset:49152\n\t"
      "ds_read_b64_tr_b16 a[250:251], v46 offset:53248\n\t"
      "ds_read_b64_tr_b16 a[252:253], v46 offset:57344\n\t"
      "ds_read_b64_tr_b16 a[254:255], v46 offset:61440\n\t"
      "s_waitcnt lgkmcnt(0)\n\t"
      "s_barrier\n\t"
      "v_mov_b32 v172, 0x3f803f80\n\t"
      "v_mov_b32 v173, 0x3f803f80\n\t"
      "v_mov_b32 v174, 0x3f803f80\n\t"
      "v_mov_b32 v175, 0x3f803f80\n\t"
      "s_nop 1\n\t"
      "v_mfma_f32_16x16x32_bf16 v[136:139], a[0:3], v[172:175], 0\n\t"
      "v_mfma_f32_16x16x32_bf16 v[140:143], a[64:67], v[172:175], 0\n\t"
      "v_mfma_f32_16x16x32_bf16 v[144:147], a[128:131], v[172:175], 0\n\t"
      "v_mfma_f32_16x16x32_bf16 v[148:151], a[192:195], v[172:175], 0\n\t"
      "v_mfma_f32_16x16x32_bf16 v[136:139], a[4:7], v[172:175], v[136:139]\n\t"
      "v_mfma_f32_16x16x32_bf16 v[140:143], a[68:71], v[172:175], v[140:143]\n\t"
      "v_mfma_f32_16x16x32_bf16 v[144:147], a[132:135], v[172:175], v[144:147]\n\t"
      "v_mfma_f32_16x16x32_bf16 v[148:151], a[196:199], v[172:175], v[148:151]\n\t"
      "v_mfma_f32_16x16x32_bf16 v[136:139], a[8:11], v[172:175], v[136:139]\n\t"
      "v_mfma_f32_16x16x32_bf16 v[140:143], a[72:75], v[172:175], v[140:143]\n\t"
      "v_mfma_f32_16x16x32_bf16 v[144:147], a[136:139], v[172:175], v[144:147]\n\t"
      "v_mfma_f32_16x16x32_bf16 v[148:151], a[200:203], v[172:175], v[148:151]\n\t"
      "v_mfma_f32_16x16x32_bf16 v[136:139], a[12:15], v[172:175], v[136:139]\n\t"
      "v_mfma_f32_16x16x32_bf16 v[140:143], a[76:79], v[172:175], v[140:143]\n\t"
      "v_mfma_f32_16x16x32_bf16 v[144:147], a[140:143], v[172:175], v[144:147]\n\t"
      "v_mfma_f32_16x16x32_bf16 v[148:151], a[204:207], v[172:175], v[148:151]\n\t"
      "v_mfma_f32_16x16x32_bf16 v[136:139], a[16:19], v[172:175], v[136:139]\n\t"
      "v_mfma_f32_16x16x32_bf16 v[140:143], a[80:83], v[172:175], v[140:143]\n\t"
      "v_mfma_f32_16x16x32_bf16 v[144:147], a[144:147], v[172:175], v[144:147]\n\t"
      "v_mfma_f32_16x16x32_bf16 v[148:151], a[208:211], v[172:175], v[148:151]\n\t"
      "v_mfma_f32_16x16x32_bf16 v[136:139], a[20:23], v[172:175], v[136:139]\n\t"
      "v_mfma_f32_16x16x32_bf16 v[140:143], a[84:87], v[172:175], v[140:143]\n\t"
      "v_mfma_f32_16x16x32_bf16 v[144:147], a[148:151], v[172:175], v[144:147]\n\t"
      "v_mfma_f32_16x16x32_bf16 v[148:151], a[212:215], v[172:175], v[148:151]\n\t"
      "v_mfma_f32_16x16x32_bf16 v[136:139], a[24:27], v[172:175], v[136:139]\n\t"
      "v_mfma_f32_16x16x32_bf16 v[140:143], a[88:91], v[172:175], v[140:143]\n\t"
      "v_mfma_f32_16x16x32_bf16 v[144:147], a[152:155], v[172:175], v[144:147]\n\t"
      "v_mfma_f32_16x16x32_bf16 v[148:151], a[216:219], v[172:175], v[148:151]\n\t"
      "v_mfma_f32_16x16x32_bf16 v[136:139], a[28:31], v[172:175], v[136:139]\n\t"
      "v_mfma_f32_16x16x32_bf16 v[140:143], a[92:95], v[172:175], v[140:143]\n\t"
      "v_mfma_f32_16x16x32_bf16 v[144:147], a[156:159], v[172:175], v[144:147]\n\t"
      "v_mfma_f32_16x16x32_bf16 v[148:151], a[220:223], v[172:175], v[148:151]\n\t"
      "v_mfma_f32_16x16x32_bf16 v[136:139], a[32:35], v[172:175], v[136:139]\n\t"
      "v_mfma_f32_16x16x32_bf16 v[140:143], a[96:99], v[172:175], v[140:143]\n\t"
      "v_mfma_f32_16x16x32_bf16 v[144:147], a[160:163], v[172:175], v[144:147]\n\t"
      "v_mfma_f32_16x16x32_bf16 v[148:151], a[224:227], v[172:175], v[148:151]\n\t"
      "v_mfma_f32_16x16x32_bf16 v[136:139], a[36:39], v[172:175], v[136:139]\n\t"
      "v_mfma_f32_16x16x32_bf16 v[140:143], a[100:103], v[172:175], v[140:143]\n\t"
      "v_mfma_f32_16x16x32_bf16 v[144:147], a[164:167], v[172:175], v[144:147]\n\t"
      "v_mfma_f32_16x16x32_bf16 v[148:151], a[228:231], v[172:175], v[148:151]\n\t"
      "v_mfma_f32_16x16x32_bf16 v[136:139], a[40:43], v[172:175], v[136:139]\n\t"
      "v_mfma_f32_16x16x32_bf16 v[140:143], a[104:107], v[172:175], v[140:143]\n\t"
      "v_mfma_f32_16x16x32_bf16 v[144:147], a[168:171], v[172:175], v[144:147]\n\t"
      "v_mfma_f32_16x16x32_bf16 v[148:151], a[232:235], v[172:175], v[148:151]\n\t"
      "v_mfma_f32_16x16x32_bf16 v[136:139], a[44:47], v[172:175], v[136:139]\n\t"
      "v_mfma_f32_16x16x32_bf16 v[140:143], a[108:111], v[172:175], v[140:143]\n\t"
      "v_mfma_f32_16x16x32_bf16 v[144:147], a[172:175], v[172:175], v[144:147]\n\t"
      "v_mfma_f32_16x16x32_bf16 v[148:151], a[236:239], v[172:175], v[148:151]\n\t"
      "v_mfma_f32_16x16x32_bf16 v[136:139], a[48:51], v[172:175], v[136:139]\n\t"
      "v_mfma_f32_16x16x32_bf16 v[140:143], a[112:115], v[172:175], v[140:143]\n\t"
      "v_mfma_f32_16x16x32_bf16 v[144:147], a[176:179], v[172:175], v[144:147]\n\t"
      "v_mfma_f32_16x16x32_bf16 v[148:151], a[240:243], v[172:175], v[148:151]\n\t"
      "v_mfma_f32_16x16x32_bf16 v[136:139], a[52:55], v[172:175], v[136:139]\n\t"
      "v_mfma_f32_16x16x32_bf16 v[140:143], a[116:119], v[172:175], v[140:143]\n\t"
      "v_mfma_f32_16x16x32_bf16 v[144:147], a[180:183], v[172:175], v[144:147]\n\t"
      "v_mfma_f32_16x16x32_bf16 v[148:151], a[244:247], v[172:175], v[148:151]\n\t"
      "v_mfma_f32_16x16x32_bf16 v[136:139], a[56:59], v[172:175], v[136:139]\n\t"
      "v_mfma_f32_16x16x32_bf16 v[140:143], a[120:123], v[172:175], v[140:143]\n\t"
      "v_mfma_f32_16x16x32_bf16 v[144:147], a[184:187], v[172:175], v[144:147]\n\t"
      "v_mfma_f32_16x16x32_bf16 v[148:151], a[248:251], v[172:175], v[148:151]\n\t"
      "v_mfma_f32_16x16x32_bf16 v[136:139], a[60:63], v[172:175], v[136:139]\n\t"
      "v_mfma_f32_16x16x32_bf16 v[140:143], a[124:127], v[172:175], v[140:143]\n\t"
      "v_mfma_f32_16x16x32_bf16 v[144:147], a[188:191], v[172:175], v[144:147]\n\t"
      "v_mfma_f32_16x16x32_bf16 v[148:151], a[252:255], v[172:175], v[148:151]\n\t"
      "s_mul_i32 s60, s60, 0x9e3779b1\n\t"
      "s_add_u32 s60, s60, 0x7f4a7c15\n\t"
      "s_branch LIN1_%=\n\t"
      "LIN0_%=:\n\t"
      "s_add_u32 s71, s71, 1\n\t"
      "s_cmp_gt_u32 s71, 131072\n\t"
      "s_cbranch_scc1 LFAIL_%=\n\t"
      "s_sleep 4\n\t"
      "s_mov_b32 s69, 188747776\n\t"
      "buffer_load_dwordx4 v[64:67], %[vL], %[rs], s69 offen sc1\n\t"
      "LIN1_%=:\n\t"
      "s_waitcnt vmcnt(0)\n\t"
      "v_cmp_ne_u32 vcc, s60, v64\n\t"
      "v_cmp_ne_u32 s[64:65], s60, v66\n\t"
      "s_or_b64 vcc, vcc, s[64:65]\n\t"
      "s_cbranch_scc1 LIN0_%=\n\t"
      "v_add_u32 v64, v65, v67\n\t"
      "s_nop 1\n\t"
      "v_readlane_b32 s69, v64, 0\n\t"
      "v_readlane_b32 s61, v64, 1\n\t"
      "s_cmp_lt_u32 s61, s69\n\t"
      "s_cselect_b32 s61, 8192, 0\n\t"
      "v_readlane_b32 s69, v64, 2\n\t"
      "v_readlane_b32 s62, v64, 3\n\t"
      "s_cmp_lt_u32 s62, s69\n\t"
      "s_cselect_b32 s62, 8192, 0\n\t"
      "s_mov_b32 s78, s61\n\t"
      "LLAYER_%=:\n\t"
      "s_mov_b32 s68, -1\n\t"
      "s_lshl_b32 s76, s55, 6\n\t"
      "s_sub_u32 s52, s76, 1\n\t"
      "s_mov_b32 s84, 65536\n\t"
      "s_mov_b32 s85, 0\n\t"
      "s_mov_b32 s86, 32768\n\t"
      "s_lshl_b32 s69, s55, 22\n\t"
      "s_add_u32 s88, s69, 174063616\n\t"
      "s_lshl_b32 s69, %[wv], 13\n\t"
      "s_add_u32 s88, s88, s69\n\t"
      "s_lshl_b32 s69, s55, 19\n\t"
      "s_add_u32 s89, s69, 189792256\n\t"
      "s_lshl_b32 s69, %[wv], 10\n\t"
      "s_add_u32 s89, s89, s69\n\t"
      "v_mov_b32 v200, 16\n\t"
      "v_mov_b32 v201, 16\n\t"
      "v_mov_b32 v202, 16\n\t"
      "v_mov_b32 v203, 16\n\t"
      "s_mov_b32 s90, 0x40000000\n\t"
      "s_mov_b32 s91, 0x40000000\n\t"
      "v_add_u32 v196, s85, %[vLd]\n\t"
      "s_lshr_b32 s69, s85, 3\n\t"
      "s_add_u32 s69, s69, 98304\n\t"
      "v_add_u32 v197, s69, %[vLd]\n\t"
      "s_cmp_eq_u32 s55, 0\n\t"
      "s_cbranch_scc1 LENT_%=\n\t"
      "s_cmp_eq_u32 %[wv], 0\n\t"
      "s_cbranch_scc0 LPW_%=\n\t"
      "LPP_%=:\n\t"
      "buffer_load_dword v204, %[vB1], %[rc], s78 offen sc1\n\t"
      "s_add_u32 s71, s71, 1\n\t"
      "s_cmp_gt_u32 s71, 131072\n\t"
      "s_cbranch_scc1 LFAIL_%=\n\t"
      "s_sleep 32\n\t"
      "s_waitcnt vmcnt(0)\n\t"
      "v_cmp_gt_i32 vcc, 0x64, v204\n\t"
      "s_cbranch_vccnz LPP_%=\n\t"
      "s_mov_b64 exec, 63\n\t"
      "LPQ_%=:\n\t"
      "buffer_load_dwordx4 v[200:203], %[vX2], %[rc], s78 offen sc1\n\t"
      "s_add_u32 s71, s71, 1\n\t"
      "s_cmp_gt_u32 s71, 131072\n\t"
      "s_cbranch_scc1 LPV_%=\n\t"
      "s_waitcnt vmcnt(0)\n\t"
      "v_min_i32 v204, v200, v201\n\t"
      "v_min3_i32 v204, v204, v202, v203\n\t"
      "s_add_u32 s94, s68, 1\n\t"
      "s_add_u32 s94, s94, s76\n\t"
      "v_cmp_lt_i32 vcc, s94, v204\n\t"
      "s_and_b32 s94, s94, 15\n\t"
      "s_lshl_b32 s94, s94, 1\n\t"
      "s_lshr_b32 s94, vcc_lo, s94\n\t"
      "s_andn2_b32 s94, 3, s94\n\t"
      "s_cbranch_scc1 LPQ_%=\n\t"
      "LPV_%=:\n\t"
      "s_mov_b64 exec, -1\n\t"
      "s_waitcnt vmcnt(0)\n\t"
      "ds_write_b128 %[vFl], v[200:203]\n\t"
      "s_waitcnt lgkmcnt(0)\n\t"
      "LPW_%=:\n\t"
      "v_add_u32 v204, 110592, %[vLd]\n\t"
      "s_barrier\n\t"
      "ds_read_b128 v[200:203], v204\n\t"
      "s_waitcnt lgkmcnt(0)\n\t"
      "v_min_i32 v204, v200, v201\n\t"
      "v_min3_i32 v204, v204, v202, v203\n\t"
      "s_add_u32 s94, s68, 1\n\t"
      "s_add_u32 s94, s94, s76\n\t"
      "v_cmp_lt_i32 vcc, s94, v204\n\t"
      "s_and_b32 s94, s94, 15\n\t"
      "s_lshl_b32 s94, s94, 1\n\t"
      "s_lshr_b32 s94, vcc_lo, s94\n\t"
      "s_andn2_b32 s94, 3, s94\n\t"
      "s_cbranch_scc1 LFAIL_%=\n\t"
      "LENT_%=:\n\t"
      "s_add_u32 m0, s97, 12288\n\t"
      "s_nop 0\n\t"
      "buffer_load_dwordx4 %[vX2], %[rc], s78 offen sc1 lds\n\t"
      "s_add_u32 s94, s68, 1\n\t"
      "s_lshl_b32 s79, s94, 15\n\t"
      "s_add_u32 s79, s79, s88\n\t"
      "s_lshl_b32 s80, s94, 12\n\t"
      "s_add_u32 s80, s80, s89\n\t"
      "s_add_u32 m0, s85, s96\n\t"
      "s_add_u32 s98, s79, 4096\n\t"
      "buffer_load_dwordx4 %[vL], %[rs], s79 offen offset:0 sc1 lds\n\t"
      "buffer_load_dwordx4 %[vL], %[rs], s79 offen offset:1024 sc1 lds\n\t"
      "buffer_load_dwordx4 %[vL], %[rs], s79 offen offset:2048 sc1 lds\n\t"
      "buffer_load_dwordx4 %[vL], %[rs], s79 offen offset:3072 sc1 lds\n\t"
      "s_add_u32 m0, m0, 4096\n\t"
      "s_lshr_b32 s99, s85, 3\n\t"
      "buffer_load_dwordx4 %[vL], %[rs], s98 offen offset:0 sc1 lds\n\t"
      "buffer_load_dwordx4 %[vL], %[rs], s98 offen offset:1024 sc1 lds\n\t"
      "buffer_load_dwordx4 %[vL], %[rs], s98 offen offset:2048 sc1 lds\n\t"
      "buffer_load_dwordx4 %[vL], %[rs], s98 offen offset:3072 sc1 lds\n\t"
      "s_add_u32 m0, s99, s97\n\t"
      "s_nop 0\n\t"
      "buffer_load_dwordx4 %[vL], %[rs], s80 offen offset:0 sc1 lds\n\t"
      "s_add_u32 s94, s68, 2\n\t"
      "s_lshl_b32 s72, s94, 15\n\t"
      "s_add_u32 s72, s72, s88\n\t"
      "s_lshl_b32 s73, s94, 12\n\t"
      "s_add_u32 s73, s73, s89\n\t"
      "v_min_i32 v204, v200, v201\n\t"
      "v_min3_i32 v204, v204, v202, v203\n\t"
      "s_add_u32 s94, s68, 2\n\t"
      "s_add_u32 s94, s94, s76\n\t"
      "v_cmp_lt_i32 vcc, s94, v204\n\t"
      "s_and_b32 s94, s94, 15\n\t"
      "s_lshl_b32 s94, s94, 1\n\t"
      "s_lshr_b32 s94, vcc_lo, s94\n\t"
      "s_andn2_b32 s94, 3, s94\n\t"
      "s_cselect_b32 s87, 0, 2\n\t"
      "s_cselect_b32 s72, 0x40000000, s72\n\t"
      "s_cselect_b32 s73, 0x40000000, s73\n\t"
      "s_add_u32 m0, s86, s96\n\t"
      "s_add_u32 s98, s72, 4096\n\t"
      "buffer_load_dwordx4 %[vL], %[rs], s72 offen offset:0 sc1 lds\n\t"
      "buffer_load_dwordx4 %[vL], %[rs], s72 offen offset:1024 sc1 lds\n\t"
      "buffer_load_dwordx4 %[vL], %[rs], s72 offen offset:2048 sc1 lds\n\t"
      "buffer_load_dwordx4 %[vL], %[rs], s72 offen offset:3072 sc1 lds\n\t"
      "s_add_u32 m0, m0, 4096\n\t"
      "s_lshr_b32 s99, s86, 3\n\t"
      "buffer_load_dwordx4 %[vL], %[rs], s98 offen offset:0 sc1 lds\n\t"
      "buffer_load_dwordx4 %[vL], %[rs], s98 offen offset:1024 sc1 lds\n\t"
      "buffer_load_dwordx4 %[vL], %[rs], s98 offen offset:2048 sc1 lds\n\t"
      "buffer_load_dwordx4 %[vL], %[rs], s98 offen offset:3072 sc1 lds\n\t"
      "s_add_u32 m0, s99, s97\n\t"
      "s_nop 0\n\t"
      "buffer_load_dwordx4 %[vL], %[rs], s73 offen offset:0 sc1 lds\n\t"
      "s_add_u32 s94, s68, 3\n\t"
      "s_lshl_b32 s72, s94, 15\n\t"
      "s_add_u32 s72, s72, s88\n\t"
      "s_lshl_b32 s73, s94, 12\n\t"
      "s_add_u32 s73, s73, s89\n\t"
      "s_cmp_gt_i32 s68, 60\n\t"
      "s_cselect_b32 s74, 0, 3\n\t"
      "v_min_i32 v204, v200, v201\n\t"
      "v_min3_i32 v204, v204, v202, v203\n\t"
      "s_add_u32 s94, s68, 3\n\t"
      "s_add_u32 s94, s94, s76\n\t"
      "v_cmp_lt_i32 vcc, s94, v204\n\t"
      "s_and_b32 s94, s94, 15\n\t"
      "s_lshl_b32 s94, s94, 1\n\t"
      "s_lshr_b32 s94, vcc_lo, s94\n\t"
      "s_andn2_b32 s94, s74, s94\n\t"
      "s_cselect_b32 s74, 0, 4\n\t"
      "s_cselect_b32 s72, 0x40000000, s72\n\t"
      "s_cselect_b32 s73, 0x40000000, s73\n\t"
      "s_or_b32 s87, s87, s74\n\t"
      "s_lshr_b32 s87, s87, 1\n\t"
      "s_add_u32 m0, s97, 12288\n\t"
      "s_mov_b32 s94, s84\n\t"
      "buffer_load_dwordx4 %[vX2], %[rc], s78 offen sc1 lds\n\t"
      "s_add_u32 m0, s94, s96\n\t"
      "s_add_u32 s98, s72, 4096\n\t"
      "buffer_load_dwordx4 %[vL], %[rs], s72 offen offset:0 sc1 lds\n\t"
      "buffer_load_dwordx4 %[vL], %[rs], s72 offen offset:1024 sc1 lds\n\t"
      "buffer_load_dwordx4 %[vL], %[rs], s72 offen offset:2048 sc1 lds\n\t"
      "buffer_load_dwordx4 %[vL], %[rs], s72 offen offset:3072 sc1 lds\n\t"
      "s_add_u32 m0, m0, 4096\n\t"
      "s_lshr_b32 s99, s94, 3\n\t"
      "buffer_load_dwordx4 %[vL], %[rs], s98 offen offset:0 sc1 lds\n\t"
      "buffer_load_dwordx4 %[vL], %[rs], s98 offen offset:1024 sc1 lds\n\t"
      "buffer_load_dwordx4 %[vL], %[rs], s98 offen offset:2048 sc1 lds\n\t"
      "buffer_load_dwordx4 %[vL], %[rs], s98 offen offset:3072 sc1 lds\n\t"
      "s_add_u32 m0, s99, s97\n\t"
      "s_nop 0\n\t"
      "buffer_load_dwordx4 %[vL], %[rs], s73 offen offset:0 sc1 lds\n\t"
      "s_mov_b32 s84, s85\n\t"
      "s_mov_b32 s85, s86\n\t"
      "s_mov_b32 s86, s94\n\t"
      "s_cmp_eq_u32 s87, 3\n\t"
      "s_cbranch_scc0 LSC_%=\n\t"
      "s_waitcnt vmcnt(19)\n\t"
      "LSR_%=:\n\t"
      "s_barrier\n\t"
      "ds_read_b128 v[40:43], v196 offset:0\n\t"
      "ds_read_b128 v[44:47], v196 offset:1024\n\t"
      "ds_read_b128 v[48:51], v196 offset:2048\n\t"
      "ds_read_b128 v[52:55], v196 offset:3072\n\t"
      "ds_read_b128 v[56:59], v196 offset:4096\n\t"
      "ds_read_b128 v[60:63], v196 offset:5120\n\t"
      "ds_read_b128 v[64:67], v196 offset:6144\n\t"
      "ds_read_b128 v[68:71], v196 offset:7168\n\t"
      "ds_read_b128 v[72:75], v196 offset:8192\n\t"
      "ds_read_b128 v[76:79], v196 offset:9216\n\t"
      "ds_read_b128 v[80:83], v196 offset:10240\n\t"
      "ds_read_b128 v[84:87], v196 offset:11264\n\t"
      "ds_read_b128 v[88:91], v196 offset:12288\n\t"
      "ds_read_b128 v[92:95], v196 offset:13312\n\t"
      "s_mov_b32 s68, 0\n\t"
      "s_waitcnt lgkmcnt(0)\n\t"
      "LLOOP_%=:\n\t"
      "s_waitcnt lgkmcnt(10)\n\t"
      "v_mfma_f32_16x16x32_bf16 v[104:107], a[0:3], v[40:43], 0\n\t"
      "ds_read_b128 v[96:99], v196 offset:14336\n\t"
      "v_mfma_f32_16x16x32_bf16 v[108:111], a[64:67], v[40:43], 0\n\t"
      "ds_read_b128 v[100:103], v196 offset:15360\n\t"
      "ds_read_b128 v[152:155], v197 offset:0\n\t"
      "v_mfma_f32_16x16x32_bf16 v[112:115], a[128:131], v[40:43], 0\n\t"
      "ds_read_b128 v[156:159], v197 offset:1024\n\t"
      "ds_read_b128 v[160:163], v197 offset:2048\n\t"
      "v_mfma_f32_16x16x32_bf16 v[116:119], a[192:195], v[40:43], 0\n\t"
      "ds_read_b128 v[164:167], v197 offset:3072\n\t"
      "v_mul_f32 v120, v120, v170\n\t"
      "v_mfma_f32_16x16x32_bf16 v[104:107], a[4:7], v[44:47], v[104:107]\n\t"
      "ds_read_b128 v[40:43], v196 offset:16384\n\t"
      "v_mul_f32 v121, v121, v170\n\t"
      "v_mfma_f32_16x16x32_bf16 v[108:111], a[68:71], v[44:47], v[108:111]\n\t"
      "v_mul_f32 v122, v122, v170\n\t"
      "v_mul_f32 v123, v123, v170\n\t"
      "v_mfma_f32_16x16x32_bf16 v[112:115], a[132:135], v[44:47], v[112:115]\n\t"
      "v_mul_f32 v124, v124, v170\n\t"
      "v_mul_f32 v125, v125, v170\n\t"
      "v_mfma_f32_16x16x32_bf16 v[116:119], a[196:199], v[44:47], v[116:119]\n\t"
      "v_mul_f32 v126, v126, v170\n\t"
      "v_mul_f32 v127, v127, v170\n\t"
      "v_mfma_f32_16x16x32_bf16 v[104:107], a[8:11], v[48:51], v[104:107]\n\t"
      "ds_read_b128 v[44:47], v196 offset:17408\n\t"
      "v_fmac_f32 v120, v136, v171\n\t"
      "v_mfma_f32_16x16x32_bf16 v[108:111], a[72:75], v[48:51], v[108:111]\n\t"
      "v_fmac_f32 v121, v137, v171\n\t"
      "v_fmac_f32 v122, v138, v171\n\t"
      "v_mfma_f32_16x16x32_bf16 v[112:115], a[136:139], v[48:51], v[112:115]\n\t"
      "v_fmac_f32 v123, v139, v171\n\t"
      "v_fmac_f32 v124, v140, v171\n\t"
      "v_mfma_f32_16x16x32_bf16 v[116:119], a[200:203], v[48:51], v[116:119]\n\t"
      "v_fmac_f32 v125, v141, v171\n\t"
      "v_fmac_f32 v126, v142, v171\n\t"
      "v_mfma_f32_16x16x32_bf16 v[104:107], a[12:15], v[52:55], v[104:107]\n\t"
      "ds_read_b128 v[48:51], v196 offset:18432\n\t"
      "v_fmac_f32 v127, v143, v171\n\t"
      "v_mfma_f32_16x16x32_bf16 v[108:111], a[76:79], v[52:55], v[108:111]\n\t"
      "v_cvt_pk_bf16_f32 v180, v120, v121\n\t"
      "v_cvt_pk_bf16_f32 v181, v122, v123\n\t"
      "v_mfma_f32_16x16x32_bf16 v[112:115], a[140:143], v[52:55], v[112:115]\n\t"
      "v_cvt_pk_bf16_f32 v182, v124, v125\n\t"
      "v_cvt_pk_bf16_f32 v183, v126, v127\n\t"
      "v_mfma_f32_16x16x32_bf16 v[116:119], a[204:207], v[52:55], v[116:119]\n\t"
      "buffer_store_dwordx4 v[180:183], %[vD], %[rs], s91 offen offset:0 sc1\n\t"
      "s_waitcnt lgkmcnt(15)\n\t"
      "v_mfma_f32_16x16x32_bf16 v[104:107], a[16:19], v[56:59], v[104:107]\n\t"
      "ds_read_b128 v[52:55], v196 offset:19456\n\t"
      "v_mfma_f32_16x16x32_bf16 v[108:111], a[80:83], v[56:59], v[108:111]\n\t"
      "v_mul_f32 v128, v128, v170\n\t"
      "v_mul_f32 v129, v129, v170\n\t"
      "v_mfma_f32_16x16x32_bf16 v[112:115], a[144:147], v[56:59], v[112:115]\n\t"
      "v_mul_f32 v130, v130, v170\n\t"
      "v_mul_f32 v131, v131, v170\n\t"
      "v_mfma_f32_16x16x32_bf16 v[116:119], a[208:211], v[56:59], v[116:119]\n\t"
      "v_mul_f32 v132, v132, v170\n\t"
      "v_mul_f32 v133, v133, v170\n\t"
      "v_mfma_f32_16x16x32_bf16 v[104:107], a[20:23], v[60:63], v[104:107]\n\t"
      "ds_read_b128 v[56:59], v196 offset:20480\n\t"
      "v_mul_f32 v134, v134, v170\n\t"
      "v_mfma_f32_16x16x32_bf16 v[108:111], a[84:87], v[60:63], v[108:111]\n\t"
      "v_mul_f32 v135, v135, v170\n\t"
      "v_fmac_f32 v128, v144, v171\n\t"
      "v_mfma_f32_16x16x32_bf16 v[112:115], a[148:151], v[60:63], v[112:115]\n\t"
      "v_fmac_f32 v129, v145, v171\n\t"
      "v_fmac_f32 v130, v146, v171\n\t"
      "v_mfma_f32_16x16x32_bf16 v[116:119], a[212:215], v[60:63], v[116:119]\n\t"
      "v_fmac_f32 v131, v147, v171\n\t"
      "v_fmac_f32 v132, v148, v171\n\t"
      "v_mfma_f32_16x16x32_bf16 v[104:107], a[24:27], v[64:67], v[104:107]\n\t"
      "ds_read_b128 v[60:63], v196 offset:21504\n\t"
      "v_fmac_f32 v133, v149, v171\n\t"
      "v_mfma_f32_16x16x32_bf16 v[108:111], a[88:91], v[64:67], v[108:111]\n\t"
      "v_fmac_f32 v134, v150, v171\n\t"
      "v_fmac_f32 v135, v151, v171\n\t"
      "v_mfma_f32_16x16x32_bf16 v[112:115], a[152:155], v[64:67], v[112:115]\n\t"
      "v_cvt_pk_bf16_f32 v184, v128, v129\n\t"
      "v_cvt_pk_bf16_f32 v185, v130, v131\n\t"
      "v_mfma_f32_16x16x32_bf16 v[116:119], a[216:219], v[64:67], v[116:119]\n\t"
      "v_cvt_pk_bf16_f32 v186, v132, v133\n\t"
      "v_cvt_pk_bf16_f32 v187, v134, v135\n\t"
      "v_mfma_f32_16x16x32_bf16 v[104:107], a[28:31], v[68:71], v[104:107]\n\t"
      "ds_read_b128 v[64:67], v196 offset:22528\n\t"
      "ds_read_b128 v[200:203], %[vFl]\n\t"
      "v_mfma_f32_16x16x32_bf16 v[108:111], a[92:95], v[68:71], v[108:111]\n\t"
      "buffer_store_dwordx4 v[184:187], %[vD], %[rs], s91 offen offset:1024 sc1\n\t"
      "v_mfma_f32_16x16x32_bf16 v[112:115], a[156:159], v[68:71], v[112:115]\n\t"
      "s_lshl_b32 s90, s68, 13\n\t"
      "s_add_u32 s91, s90, 4096\n\t"
      "v_mfma_f32_16x16x32_bf16 v[116:119], a[220:223], v[68:71], v[116:119]\n\t"
      "s_waitcnt lgkmcnt(10)\n\t"
      "v_add_f32 v188, v152, v154\n\t"
      "v_mfma_f32_16x16x32_bf16 v[104:107], a[32:35], v[72:75], v[104:107]\n\t"
      "ds_read_b128 v[68:71], v196 offset:23552\n\t"
      "v_add_f32 v189, v156, v158\n\t"
      "v_mfma_f32_16x16x32_bf16 v[108:111], a[96:99], v[72:75], v[108:111]\n\t"
      "s_waitcnt lgkmcnt(9)\n\t"
      "v_add_f32 v188, v188, v160\n\t"
      "v_mfma_f32_16x16x32_bf16 v[112:115], a[160:163], v[72:75], v[112:115]\n\t"
      "v_add_f32 v189, v189, v162\n\t"
      "v_add_f32 v188, v188, v164\n\t"
      "v_mfma_f32_16x16x32_bf16 v[116:119], a[224:227], v[72:75], v[116:119]\n\t"
      "v_add_f32 v189, v189, v166\n\t"
      "v_add_f32 v188, v188, v189\n\t"
      "v_mfma_f32_16x16x32_bf16 v[104:107], a[36:39], v[76:79], v[104:107]\n\t"
      "ds_read_b128 v[72:75], v196 offset:24576\n\t"
      "v_mov_b32 v193, v188\n\t"
      "v_mfma_f32_16x16x32_bf16 v[108:111], a[100:103], v[76:79], v[108:111]\n\t"
      "v_add_f32 v190, v153, v155\n\t"
      "v_add_f32 v191, v157, v159\n\t"
      "v_mfma_f32_16x16x32_bf16 v[112:115], a[164:167], v[76:79], v[112:115]\n\t"
      "v_permlane16_swap_b32 v188, v193\n\t"
      "v_add_f32 v192, v188, v193\n\t"
      "v_mfma_f32_16x16x32_bf16 v[116:119], a[228:231], v[76:79], v[116:119]\n\t"
      "v_mul_f32 v192, s92, v192\n\t"
      "v_add_f32 v190, v190, v161\n\t"
      "v_mfma_f32_16x16x32_bf16 v[104:107], a[40:43], v[80:83], v[104:107]\n\t"
      "ds_read_b128 v[76:79], v196 offset:25600\n\t"
      "v_add_f32 v191, v191, v163\n\t"
      "v_mfma_f32_16x16x32_bf16 v[108:111], a[104:107], v[80:83], v[108:111]\n\t"
      "v_add_f32 v190, v190, v165\n\t"
      "v_add_f32 v191, v191, v167\n\t"
      "v_mfma_f32_16x16x32_bf16 v[112:115], a[168:171], v[80:83], v[112:115]\n\t"
      "v_mul_f32 v190, s95, v190\n\t"
      "v_mul_f32 v191, s95, v191\n\t"
      "v_mfma_f32_16x16x32_bf16 v[116:119], a[232:235], v[80:83], v[116:119]\n\t"
      "v_sub_f32 v194, v152, v192\n\t"
      "v_sub_f32 v195, v154, v192\n\t"
      "v_mfma_f32_16x16x32_bf16 v[104:107], a[44:47], v[84:87], v[104:107]\n\t"
      "ds_read_b128 v[80:83], v196 offset:26624\n\t"
      "v_fmac_f32 v190, v194, v194\n\t"
      "v_mfma_f32_16x16x32_bf16 v[108:111], a[108:111], v[84:87], v[108:111]\n\t"
      "v_fmac_f32 v191, v195, v195\n\t"
      "v_sub_f32 v194, v156, v192\n\t"
      "v_mfma_f32_16x16x32_bf16 v[112:115], a[172:175], v[84:87], v[112:115]\n\t"
      "v_sub_f32 v195, v158, v192\n\t"
      "v_fmac_f32 v190, v194, v194\n\t"
      "v_mfma_f32_16x16x32_bf16 v[116:119], a[236:239], v[84:87], v[116:119]\n\t"
      "v_fmac_f32 v191, v195, v195\n\t"
      "v_sub_f32 v194, v160, v192\n\t"
      "v_mfma_f32_16x16x32_bf16 v[104:107], a[48:51], v[88:91], v[104:107]\n\t"
      "ds_read_b128 v[84:87], v196 offset:27648\n\t"
      "v_sub_f32 v195, v162, v192\n\t"
      "v_mfma_f32_16x16x32_bf16 v[108:111], a[112:115], v[88:91], v[108:111]\n\t"
      "v_fmac_f32 v190, v194, v194\n\t"
      "v_fmac_f32 v191, v195, v195\n\t"
      "v_mfma_f32_16x16x32_bf16 v[112:115], a[176:179], v[88:91], v[112:115]\n\t"
      "v_sub_f32 v194, v164, v192\n\t"
      "v_sub_f32 v195, v166, v192\n\t"
      "v_mfma_f32_16x16x32_bf16 v[116:119], a[240:243], v[88:91], v[116:119]\n\t"
      "v_fmac_f32 v190, v194, v194\n\t"
      "v_fmac_f32 v191, v195, v195\n\t"
      "v_mfma_f32_16x16x32_bf16 v[104:107], a[52:55], v[92:95], v[104:107]\n\t"
      "ds_read_b128 v[88:91], v196 offset:28672\n\t"
      "v_add_f32 v188, v190, v191\n\t"
      "v_mfma_f32_16x16x32_bf16 v[108:111], a[116:119], v[92:95], v[108:111]\n\t"
      "v_mov_b32 v193, v188\n\t"
      "v_mul_f32 v192, 0xbd000000, v192\n\t"
      "v_mfma_f32_16x16x32_bf16 v[112:115], a[180:183], v[92:95], v[112:115]\n\t"
      "v_permlane16_swap_b32 v188, v193\n\t"
      "v_add_f32 v188, v188, v193\n\t"
      "v_mfma_f32_16x16x32_bf16 v[116:119], a[244:247], v[92:95], v[116:119]\n\t"
      "v_fma_f32 v188, v188, s93, v205\n\t"
      "v_rsq_f32 v168, v188\n\t"
      "v_mfma_f32_16x16x32_bf16 v[104:107], a[56:59], v[96:99], v[104:107]\n\t"
      "ds_read_b128 v[92:95], v196 offset:29696\n\t"
      "v_mfma_f32_16x16x32_bf16 v[108:111], a[120:123], v[96:99], v[108:111]\n\t"
      "v_mul_f32 v169, v192, v168\n\t"
      "v_mov_b32 v170, v168\n\t"
      "v_mfma_f32_16x16x32_bf16 v[112:115], a[184:187], v[96:99], v[112:115]\n\t"
      "v_mov_b32 v171, v169\n\t"
      "v_mfma_f32_16x16x32_bf16 v[116:119], a[248:251], v[96:99], v[116:119]\n\t"
      "v_permlane32_swap_b32 v168, v170\n\t"
      "v_permlane32_swap_b32 v169, v171\n\t"
      "v_mfma_f32_16x16x32_bf16 v[104:107], a[60:63], v[100:103], v[104:107]\n\t"
      "ds_read_b128 v[96:99], v196 offset:30720\n\t"
      "v_mfma_f32_16x16x32_bf16 v[108:111], a[124:127], v[100:103], v[108:111]\n\t"
      "v_mfma_f32_16x16x32_bf16 v[112:115], a[188:191], v[100:103], v[112:115]\n\t"
      "v_mfma_f32_16x16x32_bf16 v[116:119], a[252:255], v[100:103], v[116:119]\n\t"
      "s_waitcnt lgkmcnt(12)\n\t"
      "v_mfma_f32_16x16x32_bf16 v[120:123], a[0:3], v[40:43], 0\n\t"
      "ds_read_b128 v[100:103], v196 offset:31744\n\t"
      "v_mfma_f32_16x16x32_bf16 v[124:127], a[64:67], v[40:43], 0\n\t"
      "v_mfma_f32_16x16x32_bf16 v[128:131], a[128:131], v[40:43], 0\n\t"
      "v_mul_f32 v104, v104, v168\n\t"
      "v_mul_f32 v105, v105, v168\n\t"
      "v_mfma_f32_16x16x32_bf16 v[132:135], a[192:195], v[40:43], 0\n\t"
      "v_mul_f32 v106, v106, v168\n\t"
      "v_mul_f32 v107, v107, v168\n\t"
      "v_mfma_f32_16x16x32_bf16 v[120:123], a[4:7], v[44:47], v[120:123]\n\t"
      "v_mul_f32 v108, v108, v168\n\t"
      "v_mul_f32 v109, v109, v168\n\t"
      "v_mfma_f32_16x16x32_bf16 v[124:127], a[68:71], v[44:47], v[124:127]\n\t"
      "v_mul_f32 v110, v110, v168\n\t"
      "v_mul_f32 v111, v111, v168\n\t"
      "v_mfma_f32_16x16x32_bf16 v[128:131], a[132:135], v[44:47], v[128:131]\n\t"
      "v_fmac_f32 v104, v136, v169\n\t"
      "v_fmac_f32 v105, v137, v169\n\t"
      "v_mfma_f32_16x16x32_bf16 v[132:135], a[196:199], v[44:47], v[132:135]\n\t"
      "v_fmac_f32 v106, v138, v169\n\t"
      "v_fmac_f32 v107, v139, v169\n\t"
      "s_waitcnt lgkmcnt(0)\n\t"
      "s_cmp_lt_i32 s68, 10\n\t"
      "s_cbranch_scc1 LPS_%=\n\t"
      "s_waitcnt vmcnt(15)\n\t"
      "s_add_u32 s69, s68, s52\n\t"
      "LPR_%=:\n\t"
      "v_mov_b32 v198, s69\n\t"
      "s_bitcmp1_b32 s87, 0\n\t"
      "buffer_store_dword v198, %[vFo], %[rc], s78 offen sc1\n\t"
      "s_cbranch_scc0 LBLK_%=\n\t"
      "LBR_%=:\n\t"
      "s_barrier\n\t"
      "v_add_u32 v196, s85, %[vLd]\n\t"
      "s_lshr_b32 s69, s85, 3\n\t"
      "s_add_u32 s69, s69, 98304\n\t"
      "v_add_u32 v197, s69, %[vLd]\n\t"
      "v_mfma_f32_16x16x32_bf16 v[120:123], a[8:11], v[48:51], v[120:123]\n\t"
      "ds_read_b128 v[40:43], v196 offset:0\n\t"
      "ds_read_b128 v[44:47], v196 offset:1024\n\t"
      "v_mfma_f32_16x16x32_bf16 v[124:127], a[72:75], v[48:51], v[124:127]\n\t"
      "s_add_u32 s94, s68, 3\n\t"
      "s_lshl_b32 s72, s94, 15\n\t"
      "v_mfma_f32_16x16x32_bf16 v[128:131], a[136:139], v[48:51], v[128:131]\n\t"
      "s_add_u32 s72, s72, s88\n\t"
      "s_lshl_b32 s73, s94, 12\n\t"
      "v_mfma_f32_16x16x32_bf16 v[132:135], a[200:203], v[48:51], v[132:135]\n\t"
      "s_add_u32 s73, s73, s89\n\t"
      "s_cmp_gt_i32 s68, 60\n\t"
      "s_cselect_b32 s74, 0, 3\n\t"
      "v_mfma_f32_16x16x32_bf16 v[120:123], a[12:15], v[52:55], v[120:123]\n\t"
      "ds_read_b128 v[48:51], v196 offset:2048\n\t"
      "v_min_i32 v204, v200, v201\n\t"
      "v_mfma_f32_16x16x32_bf16 v[124:127], a[76:79], v[52:55], v[124:127]\n\t"
      "v_min3_i32 v204, v204, v202, v203\n\t"
      "s_add_u32 s94, s68, 3\n\t"
      "v_mfma_f32_16x16x32_bf16 v[128:131], a[140:143], v[52:55], v[128:131]\n\t"
      "s_add_u32 s94, s94, s76\n\t"
      "v_cmp_lt_i32 vcc, s94, v204\n\t"
      "v_mfma_f32_16x16x32_bf16 v[132:135], a[204:207], v[52:55], v[132:135]\n\t"
      "s_and_b32 s94, s94, 15\n\t"
      "s_lshl_b32 s94, s94, 1\n\t"
      "v_mfma_f32_16x16x32_bf16 v[120:123], a[16:19], v[56:59], v[120:123]\n\t"
      "ds_read_b128 v[52:55], v196 offset:3072\n\t"
      "s_lshr_b32 s94, vcc_lo, s94\n\t"
      "v_mfma_f32_16x16x32_bf16 v[124:127], a[80:83], v[56:59], v[124:127]\n\t"
      "s_andn2_b32 s94, s74, s94\n\t"
      "s_cselect_b32 s74, 0, 4\n\t"
      "s_cselect_b32 s72, 0x40000000, s72\n\t"
      "s_cselect_b32 s73, 0x40000000, s73\n\t"
      "s_or_b32 s87, s87, s74\n\t"
      "v_mfma_f32_16x16x32_bf16 v[128:131], a[144:147], v[56:59], v[128:131]\n\t"
      "s_lshr_b32 s87, s87, 1\n\t"
      "s_add_u32 m0, s97, 12288\n\t"
      "v_mfma_f32_16x16x32_bf16 v[132:135], a[208:211], v[56:59], v[132:135]\n\t"
      "s_mov_b32 s94, s84\n\t"
      "v_fmac_f32 v108, v140, v169\n\t"
      "v_mfma_f32_16x16x32_bf16 v[120:123], a[20:23], v[60:63], v[120:123]\n\t"
      "ds_read_b128 v[56:59], v196 offset:4096\n\t"
      "v_fmac_f32 v109, v141, v169\n\t"
      "v_mfma_f32_16x16x32_bf16 v[124:127], a[84:87], v[60:63], v[124:127]\n\t"
      "buffer_load_dwordx4 %[vX2], %[rc], s78 offen sc1 lds\n\t"
      "v_mfma_f32_16x16x32_bf16 v[128:131], a[148:151], v[60:63], v[128:131]\n\t"
      "s_add_u32 m0, s94, s96\n\t"
      "s_add_u32 s98, s72, 4096\n\t"
      "v_mfma_f32_16x16x32_bf16 v[132:135], a[212:215], v[60:63], v[132:135]\n\t"
      "buffer_load_dwordx4 %[vL], %[rs], s72 offen offset:0 sc1 lds\n\t"
      "v_mfma_f32_16x16x32_bf16 v[120:123], a[24:27], v[64:67], v[120:123]\n\t"
      "ds_read_b128 v[60:63], v196 offset:5120\n\t"
      "v_fmac_f32 v110, v142, v169\n\t"
      "v_mfma_f32_16x16x32_bf16 v[124:127], a[88:91], v[64:67], v[124:127]\n\t"
      "v_fmac_f32 v111, v143, v169\n\t"
      "v_cvt_pk_bf16_f32 v172, v104, v105\n\t"
      "v_mfma_f32_16x16x32_bf16 v[128:131], a[152:155], v[64:67], v[128:131]\n\t"
      "v_cvt_pk_bf16_f32 v173, v106, v107\n\t"
      "v_cvt_pk_bf16_f32 v174, v108, v109\n\t"
      "v_mfma_f32_16x16x32_bf16 v[132:135], a[216:219], v[64:67], v[132:135]\n\t"
      "buffer_load_dwordx4 %[vL], %[rs], s72 offen offset:1024 sc1 lds\n\t"
      "v_mfma_f32_16x16x32_bf16 v[120:123], a[28:31], v[68:71], v[120:123]\n\t"
      "ds_read_b128 v[64:67], v196 offset:6144\n\t"
      "v_cvt_pk_bf16_f32 v175, v110, v111\n\t"
      "v_mfma_f32_16x16x32_bf16 v[124:127], a[92:95], v[68:71], v[124:127]\n\t"
      "buffer_store_dwordx4 v[172:175], %[vD], %[rs], s90 offen offset:0 sc1\n\t"
      "v_mfma_f32_16x16x32_bf16 v[128:131], a[156:159], v[68:71], v[128:131]\n\t"
      "v_mul_f32 v112, v112, v168\n\t"
      "v_mul_f32 v113, v113, v168\n\t"
      "v_mfma_f32_16x16x32_bf16 v[132:135], a[220:223], v[68:71], v[132:135]\n\t"
      "buffer_load_dwordx4 %[vL], %[rs], s72 offen offset:2048 sc1 lds\n\t"
      "v_mfma_f32_16x16x32_bf16 v[120:123], a[32:35], v[72:75], v[120:123]\n\t"
      "ds_read_b128 v[68:71], v196 offset:7168\n\t"
      "v_mul_f32 v114, v114, v168\n\t"
      "v_mfma_f32_16x16x32_bf16 v[124:127], a[96:99], v[72:75], v[124:127]\n\t"
      "v_mul_f32 v115, v115, v168\n\t"
      "v_mul_f32 v116, v116, v168\n\t"
      "v_mfma_f32_16x16x32_bf16 v[128:131], a[160:163], v[72:75], v[128:131]\n\t"
      "v_mul_f32 v117, v117, v168\n\t"
      "v_mul_f32 v118, v118, v168\n\t"
      "v_mfma_f32_16x16x32_bf16 v[132:135], a[224:227], v[72:75], v[132:135]\n\t"
      "buffer_load_dwordx4 %[vL], %[rs], s72 offen offset:3072 sc1 lds\n\t"
      "v_mfma_f32_16x16x32_bf16 v[120:123], a[36:39], v[76:79], v[120:123]\n\t"
      "ds_read_b128 v[72:75], v196 offset:8192\n\t"
      "s_add_u32 m0, m0, 4096\n\t"
      "v_mfma_f32_16x16x32_bf16 v[124:127], a[100:103], v[76:79], v[124:127]\n\t"
      "s_lshr_b32 s99, s94, 3\n\t"
      "v_mul_f32 v119, v119, v168\n\t"
      "v_mfma_f32_16x16x32_bf16 v[128:131], a[164:167], v[76:79], v[128:131]\n\t"
      "v_fmac_f32 v112, v144, v169\n\t"
      "v_fmac_f32 v113, v145, v169\n\t"
      "v_mfma_f32_16x16x32_bf16 v[132:135], a[228:231], v[76:79], v[132:135]\n\t"
      "buffer_load_dwordx4 %[vL], %[rs], s98 offen offset:0 sc1 lds\n\t"
      "v_mfma_f32_16x16x32_bf16 v[120:123], a[40:43], v[80:83], v[120:123]\n\t"
      "ds_read_b128 v[76:79], v196 offset:9216\n\t"
      "v_fmac_f32 v114, v146, v169\n\t"
      "v_mfma_f32_16x16x32_bf16 v[124:127], a[104:107], v[80:83], v[124:127]\n\t"
      "v_fmac_f32 v115, v147, v169\n\t"
      "v_fmac_f32 v116, v148, v169\n\t"
      "v_mfma_f32_16x16x32_bf16 v[128:131], a[168:171], v[80:83], v[128:131]\n\t"
      "v_fmac_f32 v117, v149, v169\n\t"
      "v_fmac_f32 v118, v150, v169\n\t"
      "v_mfma_f32_16x16x32_bf16 v[132:135], a[232:235], v[80:83], v[132:135]\n\t"
      "buffer_load_dwordx4 %[vL], %[rs], s98 offen offset:1024 sc1 lds\n\t"
      "v_mfma_f32_16x16x32_bf16 v[120:123], a[44:47], v[84:87], v[120:123]\n\t"
      "ds_read_b128 v[80:83], v196 offset:10240\n\t"
      "v_fmac_f32 v119, v151, v169\n\t"
      "v_mfma_f32_16x16x32_bf16 v[124:127], a[108:111], v[84:87], v[124:127]\n\t"
      "v_cvt_pk_bf16_f32 v176, v112, v113\n\t"
      "v_cvt_pk_bf16_f32 v177, v114, v115\n\t"
      "v_mfma_f32_16x16x32_bf16 v[128:131], a[172:175], v[84:87], v[128:131]\n\t"
      "v_cvt_pk_bf16_f32 v178, v116, v117\n\t"
      "v_cvt_pk_bf16_f32 v179, v118, v119\n\t"
      "v_mfma_f32_16x16x32_bf16 v[132:135], a[236:239], v[84:87], v[132:135]\n\t"
      "buffer_load_dwordx4 %[vL], %[rs], s98 offen offset:2048 sc1 lds\n\t"
      "v_mfma_f32_16x16x32_bf16 v[120:123], a[48:51], v[88:91], v[120:123]\n\t"
      "ds_read_b128 v[84:87], v196 offset:11264\n\t"
      "v_mfma_f32_16x16x32_bf16 v[124:127], a[112:115], v[88:91], v[124:127]\n\t"
      "buffer_store_dwordx4 v[176:179], %[vD], %[rs], s90 offen offset:1024 sc1\n\t"
      "v_mfma_f32_16x16x32_bf16 v[128:131], a[176:179], v[88:91], v[128:131]\n\t"
      "v_mfma_f32_16x16x32_bf16 v[132:135], a[240:243], v[88:91], v[132:135]\n\t"
      "buffer_load_dwordx4 %[vL], %[rs], s98 offen offset:3072 sc1 lds\n\t"
      "v_mfma_f32_16x16x32_bf16 v[120:123], a[52:55], v[92:95], v[120:123]\n\t"
      "ds_read_b128 v[88:91], v196 offset:12288\n\t"
      "s_add_u32 m0, s99, s97\n\t"
      "v_mfma_f32_16x16x32_bf16 v[124:127], a[116:119], v[92:95], v[124:127]\n\t"
      "s_nop 0\n\t"
      "v_mfma_f32_16x16x32_bf16 v[128:131], a[180:183], v[92:95], v[128:131]\n\t"
      "v_mfma_f32_16x16x32_bf16 v[132:135], a[244:247], v[92:95], v[132:135]\n\t"
      "buffer_load_dwordx4 %[vL], %[rs], s73 offen offset:0 sc1 lds\n\t"
      "v_mfma_f32_16x16x32_bf16 v[120:123], a[56:59], v[96:99], v[120:123]\n\t"
      "ds_read_b128 v[92:95], v196 offset:13312\n\t"
      "s_mov_b32 s84, s85\n\t"
      "v_mfma_f32_16x16x32_bf16 v[124:127], a[120:123], v[96:99], v[124:127]\n\t"
      "s_mov_b32 s85, s86\n\t"
      "s_mov_b32 s86, s94\n\t"
      "v_mfma_f32_16x16x32_bf16 v[128:131], a[184:187], v[96:99], v[128:131]\n\t"
      "v_mfma_f32_16x16x32_bf16 v[132:135], a[248:251], v[96:99], v[132:135]\n\t"
      "v_mfma_f32_16x16x32_bf16 v[120:123], a[60:63], v[100:103], v[120:123]\n\t"
      "v_mfma_f32_16x16x32_bf16 v[124:127], a[124:127], v[100:103], v[124:127]\n\t"
      "v_mfma_f32_16x16x32_bf16 v[128:131], a[188:191], v[100:103], v[128:131]\n\t"
      "v_mfma_f32_16x16x32_bf16 v[132:135], a[252:255], v[100:103], v[132:135]\n\t"
      "s_add_u32 s68, s68, 1\n\t"
      "s_cmp_lt_i32 s68, 64\n\t"
      "s_cbranch_scc1 LLOOP_%=\n\t"
      "s_nop 7\n\t"
      "v_mul_f32 v120, v120, v170\n\t"
      "v_mul_f32 v121, v121, v170\n\t"
      "v_mul_f32 v122, v122, v170\n\t"
      "v_mul_f32 v123, v123, v170\n\t"
      "v_mul_f32 v124, v124, v170\n\t"
      "v_mul_f32 v125, v125, v170\n\t"
      "v_mul_f32 v126, v126, v170\n\t"
      "v_mul_f32 v127, v127, v170\n\t"
      "v_fmac_f32 v120, v136, v171\n\t"
      "v_fmac_f32 v121, v137, v171\n\t"
      "v_fmac_f32 v122, v138, v171\n\t"
      "v_fmac_f32 v123, v139, v171\n\t"
      "v_fmac_f32 v124, v140, v171\n\t"
      "v_fmac_f32 v125, v141, v171\n\t"
      "v_fmac_f32 v126, v142, v171\n\t"
      "v_fmac_f32 v127, v143, v171\n\t"
      "v_cvt_pk_bf16_f32 v180, v120, v121\n\t"
      "v_cvt_pk_bf16_f32 v181, v122, v123\n\t"
      "v_cvt_pk_bf16_f32 v182, v124, v125\n\t"
      "v_cvt_pk_bf16_f32 v183, v126, v127\n\t"
      "buffer_store_dwordx4 v[180:183], %[vD], %[rs], s91 offen offset:0 sc1\n\t"
      "v_mul_f32 v128, v128, v170\n\t"
      "v_mul_f32 v129, v129, v170\n\t"
      "v_mul_f32 v130, v130, v170\n\t"
      "v_mul_f32 v131, v131, v170\n\t"
      "v_mul_f32 v132, v132, v170\n\t"
      "v_mul_f32 v133, v133, v170\n\t"
      "v_mul_f32 v134, v134, v170\n\t"
      "v_mul_f32 v135, v135, v170\n\t"
      "v_fmac_f32 v128, v144, v171\n\t"
      "v_fmac_f32 v129, v145, v171\n\t"
      "v_fmac_f32 v130, v146, v171\n\t"
      "v_fmac_f32 v131, v147, v171\n\t"
      "v_fmac_f32 v132, v148, v171\n\t"
      "v_fmac_f32 v133, v149, v171\n\t"
      "v_fmac_f32 v134, v150, v171\n\t"
      "v_fmac_f32 v135, v151, v171\n\t"
      "v_cvt_pk_bf16_f32 v184, v128, v129\n\t"
      "v_cvt_pk_bf16_f32 v185, v130, v131\n\t"
      "v_cvt_pk_bf16_f32 v186, v132, v133\n\t"
      "v_cvt_pk_bf16_f32 v187, v134, v135\n\t"
      "buffer_store_dwordx4 v[184:187], %[vD], %[rs], s91 offen offset:1024 sc1\n\t"
      "s_add_u32 s69, s76, 64\n\t"
      "v_mov_b32 v198, s69\n\t"
      "s_waitcnt vmcnt(0) lgkmcnt(0)\n\t"
      "buffer_store_dword v198, %[vFo], %[rc], s78 offen sc1\n\t"
      "s_barrier\n\t"
      "s_cmp_eq_u32 s55, 1\n\t"
      "s_cbranch_scc1 LDONE_%=\n\t"
      "s_mov_b32 s55, 1\n\t"
      "s_branch LLAYER_%=\n\t"
      "LDONE_%=:\n\t"
      "s_mov_b32 %[st], 0\n\t"
      "s_branch LEND_%=\n\t"
      "LSC_%=:\n\t"
      "buffer_load_dwordx4 v[200:203], %[vX2], %[rc], s78 offen sc1\n\t"
      "s_waitcnt vmcnt(0)\n\t"
      "s_bitcmp1_b32 s87, 0\n\t"
      "s_cbranch_scc1 LSD0_%=\n\t"
      "v_min_i32 v204, v200, v201\n\t"
      "v_min3_i32 v204, v204, v202, v203\n\t"
      "s_add_u32 s94, s68, 2\n\t"
      "s_add_u32 s94, s94, s76\n\t"
      "v_cmp_lt_i32 vcc, s94, v204\n\t"
      "s_and_b32 s94, s94, 15\n\t"
      "s_lshl_b32 s94, s94, 1\n\t"
      "s_lshr_b32 s94, vcc_lo, s94\n\t"
      "s_andn2_b32 s94, 3, s94\n\t"
      "s_cbranch_scc1 LSD0_%=\n\t"
      "s_or_b32 s87, s87, 1\n\t"
      "s_add_u32 s94, s68, 2\n\t"
      "s_lshl_b32 s79, s94, 15\n\t"
      "s_add_u32 s79, s79, s88\n\t"
      "s_lshl_b32 s80, s94, 12\n\t"
      "s_add_u32 s80, s80, s89\n\t"
      "s_add_u32 m0, s85, s96\n\t"
      "s_add_u32 s98, s79, 4096\n\t"
      "buffer_load_dwordx4 %[vL], %[rs], s79 offen offset:0 sc1 lds\n\t"
      "buffer_load_dwordx4 %[vL], %[rs], s79 offen offset:1024 sc1 lds\n\t"
      "buffer_load_dwordx4 %[vL], %[rs], s79 offen offset:2048 sc1 lds\n\t"
      "buffer_load_dwordx4 %[vL], %[rs], s79 offen offset:3072 sc1 lds\n\t"
      "s_add_u32 m0, m0, 4096\n\t"
      "s_lshr_b32 s99, s85, 3\n\t"
      "buffer_load_dwordx4 %[vL], %[rs], s98 offen offset:0 sc1 lds\n\t"
      "buffer_load_dwordx4 %[vL], %[rs], s98 offen offset:1024 sc1 lds\n\t"
      "buffer_load_dwordx4 %[vL], %[rs], s98 offen offset:2048 sc1 lds\n\t"
      "buffer_load_dwordx4 %[vL], %[rs], s98 offen offset:3072 sc1 lds\n\t"
      "s_add_u32 m0, s99, s97\n\t"
      "s_nop 0\n\t"
      "buffer_load_dwordx4 %[vL], %[rs], s80 offen offset:0 sc1 lds\n\t"
      "LSD0_%=:\n\t"
      "s_bitcmp1_b32 s87, 1\n\t"
      "s_cbranch_scc1 LSD1_%=\n\t"
      "v_min_i32 v204, v200, v201\n\t"
      "v_min3_i32 v204, v204, v202, v203\n\t"
      "s_add_u32 s94, s68, 3\n\t"
      "s_add_u32 s94, s94, s76\n\t"
      "v_cmp_lt_i32 vcc, s94, v204\n\t"
      "s_and_b32 s94, s94, 15\n\t"
      "s_lshl_b32 s94, s94, 1\n\t"
      "s_lshr_b32 s94, vcc_lo, s94\n\t"
      "s_andn2_b32 s94, 3, s94\n\t"
      "s_cbranch_scc1 LSD1_%=\n\t"
      "s_or_b32 s87, s87, 2\n\t"
      "s_add_u32 s94, s68, 3\n\t"
      "s_lshl_b32 s79, s94, 15\n\t"
      "s_add_u32 s79, s79, s88\n\t"
      "s_lshl_b32 s80, s94, 12\n\t"
      "s_add_u32 s80, s80, s89\n\t"
      "s_add_u32 m0, s86, s96\n\t"
      "s_add_u32 s98, s79, 4096\n\t"
      "buffer_load_dwordx4 %[vL], %[rs], s79 offen offset:0 sc1 lds\n\t"
      "buffer_load_dwordx4 %[vL], %[rs], s79 offen offset:1024 sc1 lds\n\t"
      "buffer_load_dwordx4 %[vL], %[rs], s79 offen offset:2048 sc1 lds\n\t"
      "buffer_load_dwordx4 %[vL], %[rs], s79 offen offset:3072 sc1 lds\n\t"
      "s_add_u32 m0, m0, 4096\n\t"
      "s_lshr_b32 s99, s86, 3\n\t"
      "buffer_load_dwordx4 %[vL], %[rs], s98 offen offset:0 sc1 lds\n\t"
      "buffer_load_dwordx4 %[vL], %[rs], s98 offen offset:1024 sc1 lds\n\t"
      "buffer_load_dwordx4 %[vL], %[rs], s98 offen offset:2048 sc1 lds\n\t"
      "buffer_load_dwordx4 %[vL], %[rs], s98 offen offset:3072 sc1 lds\n\t"
      "s_add_u32 m0, s99, s97\n\t"
      "s_nop 0\n\t"
      "buffer_load_dwordx4 %[vL], %[rs], s80 offen offset:0 sc1 lds\n\t"
      "LSD1_%=:\n\t"
      "s_branch LSR_%=\n\t"
      "LPS_%=:\n\t"
      "s_waitcnt vmcnt(0)\n\t"
      "s_add_u32 s69, s68, s76\n\t"
      "s_branch LPR_%=\n\t"
      "LBLK_%=:\n\t"
      "LBLs_%=:\n\t"
      "buffer_load_dwordx4 v[200:203], %[vX2], %[rc], s78 offen sc1\n\t"
      "s_add_u32 s71, s71, 1\n\t"
      "s_cmp_gt_u32 s71, 131072\n\t"
      "s_cbranch_scc1 LFAIL_%=\n\t"
      "s_waitcnt vmcnt(0)\n\t"
      "v_min_i32 v204, v200, v201\n\t"
      "v_min3_i32 v204, v204, v202, v203\n\t"
      "s_add_u32 s94, s68, 1\n\t"
      "s_add_u32 s94, s94, s76\n\t"
      "v_cmp_lt_i32 vcc, s94, v204\n\t"
      "s_and_b32 s94, s94, 15\n\t"
      "s_lshl_b32 s94, s94, 1\n\t"
      "s_lshr_b32 s94, vcc_lo, s94\n\t"
      "s_andn2_b32 s94, 3, s94\n\t"
      "s_sleep 1\n\t"
      "s_cbranch_scc1 LBLs_%=\n\t"
      "LBGs_%=:\n\t"
      "s_add_u32 s94, s68, 1\n\t"
      "s_lshl_b32 s79, s94, 15\n\t"
      "s_add_u32 s79, s79, s88\n\t"
      "s_lshl_b32 s80, s94, 12\n\t"
      "s_add_u32 s80, s80, s89\n\t"
      "s_add_u32 m0, s85, s96\n\t"
      "s_add_u32 s98, s79, 4096\n\t"
      "buffer_load_dwordx4 %[vL], %[rs], s79 offen offset:0 sc1 lds\n\t"
      "buffer_load_dwordx4 %[vL], %[rs], s79 offen offset:1024 sc1 lds\n\t"
      "buffer_load_dwordx4 %[vL], %[rs], s79 offen offset:2048 sc1 lds\n\t"
      "buffer_load_dwordx4 %[vL], %[rs], s79 offen offset:3072 sc1 lds\n\t"
      "s_add_u32 m0, m0, 4096\n\t"
      "s_lshr_b32 s99, s85, 3\n\t"
      "buffer_load_dwordx4 %[vL], %[rs], s98 offen offset:0 sc1 lds\n\t"
      "buffer_load_dwordx4 %[vL], %[rs], s98 offen offset:1024 sc1 lds\n\t"
      "buffer_load_dwordx4 %[vL], %[rs], s98 offen offset:2048 sc1 lds\n\t"
      "buffer_load_dwordx4 %[vL], %[rs], s98 offen offset:3072 sc1 lds\n\t"
      "s_add_u32 m0, s99, s97\n\t"
      "s_nop 0\n\t"
      "buffer_load_dwordx4 %[vL], %[rs], s80 offen offset:0 sc1 lds\n\t"
      "s_bitcmp1_b32 s87, 1\n\t"
      "s_cbranch_scc1 LBD_%=\n\t"
      "v_min_i32 v204, v200, v201\n\t"
      "v_min3_i32 v204, v204, v202, v203\n\t"
      "s_add_u32 s94, s68, 2\n\t"
      "s_add_u32 s94, s94, s76\n\t"
      "v_cmp_lt_i32 vcc, s94, v204\n\t"
      "s_and_b32 s94, s94, 15\n\t"
      "s_lshl_b32 s94, s94, 1\n\t"
      "s_lshr_b32 s94, vcc_lo, s94\n\t"
      "s_andn2_b32 s94, 3, s94\n\t"
      "s_cbranch_scc1 LBD_%=\n\t"
      "s_or_b32 s87, s87, 2\n\t"
      "s_add_u32 s94, s68, 2\n\t"
      "s_lshl_b32 s79, s94, 15\n\t"
      "s_add_u32 s79, s79, s88\n\t"
      "s_lshl_b32 s80, s94, 12\n\t"
      "s_add_u32 s80, s80, s89\n\t"
      "s_add_u32 m0, s86, s96\n\t"
      "s_add_u32 s98, s79, 4096\n\t"
      "buffer_load_dwordx4 %[vL], %[rs], s79 offen offset:0 sc1 lds\n\t"
      "buffer_load_dwordx4 %[vL], %[rs], s79 offen offset:1024 sc1 lds\n\t"
      "buffer_load_dwordx4 %[vL], %[rs], s79 offen offset:2048 sc1 lds\n\t"
      "buffer_load_dwordx4 %[vL], %[rs], s79 offen offset:3072 sc1 lds\n\t"
      "s_add_u32 m0, m0, 4096\n\t"
      "s_lshr_b32 s99, s86, 3\n\t"
      "buffer_load_dwordx4 %[vL], %[rs], s98 offen offset:0 sc1 lds\n\t"
      "buffer_load_dwordx4 %[vL], %[rs], s98 offen offset:1024 sc1 lds\n\t"
      "buffer_load_dwordx4 %[vL], %[rs], s98 offen offset:2048 sc1 lds\n\t"
      "buffer_load_dwordx4 %[vL], %[rs], s98 offen offset:3072 sc1 lds\n\t"
      "s_add_u32 m0, s99, s97\n\t"
      "s_nop 0\n\t"
      "buffer_load_dwordx4 %[vL], %[rs], s80 offen offset:0 sc1 lds\n\t"
      "LBD_%=:\n\t"
      "s_waitcnt vmcnt(0)\n\t"
      "s_branch LBR_%=\n\t"
      "LFAIL_%=:\n\t"
      "s_mov_b32 %[st], 1\n\t"
      "LEND_%=:\n\t"
      "s_waitcnt vmcnt(0) lgkmcnt(0)\n\t"
      : [st] "=s"(st)
      : PR_OPS, [vFo] "v"(lane == 0 ? L_PR + h * 128 + w * 4 : OOB),
        [vD] "v"(((w >> 1) ? OFF_K : OFF_Q) + h * HQ + 2 * (w & 1) * 1024 + lane * 16),
        [vTr] "v"((w >> 1) * 65536 + (4 * g + (n >> 2)) * 256 + (n & 3) * 8 + (4 * (w & 1) ^ (4 * (g & 1) + (n >> 2))) * 32),
        [rW] "s"(mkrs(Wq + h * 128, (4 << 20) - h * 256)), [rW2] "s"(mkrs(Wk + h * 128, (4 << 20) - h * 256)) TSP_OPS(64 + h)
      :
        "v40", "v41", "v42", "v43", "v44", "v45", "v46", "v47", "v48", "v49", "v50", "v51", "v52", "v53", "v54", "v55", "v56", "v57", "v58", "v59", "v60", "v61", "v62", "v63",
        "v64", "v65", "v66", "v67", "v68", "v69", "v70", "v71", "v72", "v73", "v74", "v75", "v76", "v77", "v78", "v79", "v80", "v81", "v82", "v83", "v84", "v85", "v86", "v87",
        "v88", "v89", "v90", "v91", "v92", "v93", "v94", "v95", "v96", "v97", "v98", "v99", "v100", "v101", "v102", "v103", "v104", "v105", "v106", "v107", "v108", "v109", "v110", "v111",
        "v112", "v113", "v114", "v115", "v116", "v117", "v118", "v119", "v120", "v121", "v122", "v123", "v124", "v125", "v126", "v127", "v128", "v129", "v130", "v131", "v132", "v133", "v134", "v135",
        "v136", "v137", "v138", "v139", "v140", "v141", "v142", "v143", "v144", "v145", "v146", "v147", "v148", "v149", "v150", "v151", "v152", "v153", "v154", "v155", "v156", "v157", "v158", "v159",
        "v160", "v161", "v162", "v163", "v164", "v165", "v166", "v167", "v168", "v169", "v170", "v171", "v172", "v173", "v174", "v175", "v176", "v177", "v178", "v179", "v180", "v181", "v182", "v183",
        "v184", "v185", "v186", "v187", "v188", "v189", "v190", "v191", "v192", "v193", "v194", "v195", "v196", "v197", "v198", "v199", "v200", "v201", "v202", "v203", "v204", "v205", "v206", "v207",
        "a0", "a1", "a2", "a3", "a4", "a5", "a6", "a7", "a8", "a9", "a10", "a11", "a12", "a13", "a14", "a15", "a16", "a17", "a18", "a19", "a20", "a21", "a22", "a23",
        "a24", "a25", "a26", "a27", "a28", "a29", "a30", "a31", "a32", "a33", "a34", "a35", "a36", "a37", "a38", "a39", "a40", "a41", "a42", "a43", "a44", "a45", "a46", "a47",
        "a48", "a49", "a50", "a51", "a52", "a53", "a54", "a55", "a56", "a57", "a58", "a59", "a60", "a61", "a62", "a63", "a64", "a65", "a66", "a67", "a68", "a69", "a70", "a71",
        "a72", "a73", "a74", "a75", "a76", "a77", "a78", "a79", "a80", "a81", "a82", "a83", "a84", "a85", "a86", "a87", "a88", "a89", "a90", "a91", "a92", "a93", "a94", "a95",
        "a96", "a97", "a98", "a99", "a100", "a101", "a102", "a103", "a104", "a105", "a106", "a107", "a108", "a109", "a110", "a111", "a112", "a113", "a114", "a115", "a116", "a117", "a118", "a119",
        "a120", "a121", "a122", "a123", "a124", "a125", "a126", "a127", "a128", "a129", "a130", "a131", "a132", "a133", "a134", "a135", "a136", "a137", "a138", "a139", "a140", "a141", "a142", "a143",
        "a144", "a145", "a146", "a147", "a148", "a149", "a150", "a151", "a152", "a153", "a154", "a155", "a156", "a157", "a158", "a159", "a160", "a161", "a162", "a163", "a164", "a165", "a166", "a167",
        "a168", "a169", "a170", "a171", "a172", "a173", "a174", "a175", "a176", "a177", "a178", "a179", "a180", "a181", "a182", "a183", "a184", "a185", "a186", "a187", "a188", "a189", "a190", "a191",
        "a192", "a193", "a194", "a195", "a196", "a197", "a198", "a199", "a200", "a201", "a202", "a203", "a204", "a205", "a206", "a207", "a208", "a209", "a210", "a211", "a212", "a213", "a214", "a215",
        "a216", "a217", "a218", "a219", "a220", "a221", "a222", "a223", "a224", "a225", "a226", "a227", "a228", "a229", "a230", "a231", "a232", "a233", "a234", "a235", "a236", "a237", "a238", "a239",
        "a240", "a241", "a242", "a243", "a244", "a245", "a246", "a247", "a248", "a249", "a250", "a251", "a252", "a253", "a254", "a255", "s62", "s63", "s64", "s65", "s66", "s67", "s68", "s69",
        "s70", "s71", "s72", "s73", "s74", "s75", "s76", "s77", "s78", "s79", "s80", "s81", "s82", "s83", "s84", "s85", "s86", "s87", "s88", "s89", "s90", "s91", "s92", "s93",
        "s94", "s95", "s96", "s97", "s98", "s99", "s100", "s101", "s52", "s53", "s54", "s55", "s56", "s57", "s58", "s59", "s60", "s61", "vcc", "scc", "m0", "memory"
  );
  return st == 0;
}

DI bool role_vu(u8* __restrict__ ws, const u16* Wv, const u16* Wo, int h, unsigned ldsb) {
  const int tid = threadIdx.x, w = __builtin_amdgcn_readfirstlane(tid >> 6), lane = tid & 63, n = lane & 15, g = lane >> 4;
  const int tr = (4 * g + (n >> 2)) * 256 + (n & 3) * 8, sl = 4 * (g & 1) + (n >> 2);
  int st;
  asm volatile(
      "s_nop 4\n\t"
      "s_mov_b32 s69, 188743808\n\t"
      "s_buffer_load_dword s60, %[rs], s69\n\t"
      "s_mov_b32 s71, 0\n\t"
      "s_mov_b32 s55, 0\n\t"
      "s_mov_b32 s92, 0x3d800000\n\t"
      "s_mov_b32 s93, 0x38800000\n\t"
      "v_mov_b32 v253, 0x3727c5ac\n\t"
      "s_mov_b32 s95, 0x42000000\n\t"
      "s_lshl_b32 s69, %[wv], 13\n\t"
      "s_add_u32 s96, s69, %[ldsb]\n\t"
      "s_lshl_b32 s69, %[wv], 10\n\t"
      "s_add_u32 s97, s69, %[ldsb]\n\t"
      "s_add_u32 s97, s97, 98304\n\t"
      "s_mov_b32 s69, 0\n\t"
      "buffer_load_dwordx4 a[0:3], %[vG], %[rW], s69 offen\n\t"
      "s_mov_b32 s69, 131072\n\t"
      "buffer_load_dwordx4 a[4:7], %[vG], %[rW], s69 offen\n\t"
      "s_mov_b32 s69, 262144\n\t"
      "buffer_load_dwordx4 a[8:11], %[vG], %[rW], s69 offen\n\t"
      "s_mov_b32 s69, 393216\n\t"
      "buffer_load_dwordx4 a[12:15], %[vG], %[rW], s69 offen\n\t"
      "s_mov_b32 s69, 524288\n\t"
      "buffer_load_dwordx4 a[16:19], %[vG], %[rW], s69 offen\n\t"
      "s_mov_b32 s69, 655360\n\t"
      "buffer_load_dwordx4 a[20:23], %[vG], %[rW], s69 offen\n\t"
      "s_mov_b32 s69, 786432\n\t"
      "buffer_load_dwordx4 a[24:27], %[vG], %[rW], s69 offen\n\t"
      "s_mov_b32 s69, 917504\n\t"
      "buffer_load_dwordx4 a[28:31], %[vG], %[rW], s69 offen\n\t"
      "s_mov_b32 s69, 1048576\n\t"
      "buffer_load_dwordx4 a[32:35], %[vG], %[rW], s69 offen\n\t"
      "s_mov_b32 s69, 1179648\n\t"
      "buffer_load_dwordx4 a[36:39], %[vG], %[rW], s69 offen\n\t"
      "s_mov_b32 s69, 1310720\n\t"
      "buffer_load_dwordx4 a[40:43], %[vG], %[rW], s69 offen\n\t"
      "s_mov_b32 s69, 1441792\n\t"
      "buffer_load_dwordx4 a[44:47], %[vG], %[rW], s69 offen\n\t"
      "s_mov_b32 s69, 1572864\n\t"
      "buffer_load_dwordx4 a[48:51], %[vG], %[rW], s69 offen\n\t"
      "s_mov_b32 s69, 1703936\n\t"
      "buffer_load_dwordx4 a[52:55], %[vG], %[rW], s69 offen\n\t"
      "s_mov_b32 s69, 1835008\n\t"
      "buffer_load_dwordx4 a[56:59], %[vG], %[rW], s69 offen\n\t"
      "s_mov_b32 s69, 1966080\n\t"
      "buffer_load_dwordx4 a[60:63], %[vG], %[rW], s69 offen\n\t"
      "s_mov_b32 s69, 2097152\n\t"
      "buffer_load_dwordx4 a[64:67], %[vG], %[rW], s69 offen\n\t"
      "s_mov_b32 s69, 2228224\n\t"
      "buffer_load_dwordx4 a[68:71], %[vG], %[rW], s69 offen\n\t"
      "s_mov_b32 s69, 2359296\n\t"
      "buffer_load_dwordx4 a[72:75], %[vG], %[rW], s69 offen\n\t"
      "s_mov_b32 s69, 2490368\n\t"
      "buffer_load_dwordx4 a[76:79], %[vG], %[rW], s69 offen\n\t"
      "s_mov_b32 s69, 2621440\n\t"
      "buffer_load_dwordx4 a[80:83], %[vG], %[rW], s69 offen\n\t"
      "s_mov_b32 s69, 2752512\n\t"
      "buffer_load_dwordx4 a[84:87], %[vG], %[rW], s69 offen\n\t"
      "s_mov_b32 s69, 2883584\n\t"
      "buffer_load_dwordx4 a[88:91], %[vG], %[rW], s69 offen\n\t"
      "s_mov_b32 s69, 3014656\n\t"
      "buffer_load_dwordx4 a[92:95], %[vG], %[rW], s69 offen\n\t"
      "s_mov_b32 s69, 3145728\n\t"
      "buffer_load_dwordx4 a[96:99], %[vG], %[rW], s69 offen\n\t"
      "s_mov_b32 s69, 3276800\n\t"
      "buffer_load_dwordx4 a[100:103], %[vG], %[rW], s69 offen\n\t"
      "s_mov_b32 s69, 3407872\n\t"
      "buffer_load_dwordx4 a[104:107], %[vG], %[rW], s69 offen\n\t"
      "s_mov_b32 s69, 3538944\n\t"
      "buffer_load_dwordx4 a[108:111], %[vG], %[rW], s69 offen\n\t"
      "s_mov_b32 s69, 3670016\n\t"
      "buffer_load_dwordx4 a[112:115], %[vG], %[rW], s69 offen\n\t"
      "s_mov_b32 s69, 3801088\n\t"
      "buffer_load_dwordx4 a[116:119], %[vG], %[rW], s69 offen\n\t"
      "s_mov_b32 s69, 3932160\n\t"
      "buffer_load_dwordx4 a[120:123], %[vG], %[rW], s69 offen\n\t"
      "s_mov_b32 s69, 4063232\n\t"
      "buffer_load_dwordx4 a[124:127], %[vG], %[rW], s69 offen\n\t"
      "s_mov_b32 s69, 0\n\t"
      "buffer_load_dwordx4 a[128:131], %[vG2], %[rW2], s69 offen\n\t"
      "s_mov_b32 s69, 4096\n\t"
      "buffer_load_dwordx4 a[132:135], %[vG2], %[rW2], s69 offen\n\t"
      "s_mov_b32 s69, 8192\n\t"
      "buffer_load_dwordx4 a[136:139], %[vG2], %[rW2], s69 offen\n\t"
      "s_mov_b32 s69, 12288\n\t"
      "buffer_load_dwordx4 a[140:143], %[vG2], %[rW2], s69 offen\n\t"
      "s_mov_b32 s69, 16384\n\t"
      "buffer_load_dwordx4 a[144:147], %[vG2], %[rW2], s69 offen\n\t"
      "s_mov_b32 s69, 20480\n\t"
      "buffer_load_dwordx4 a[148:151], %[vG2], %[rW2], s69 offen\n\t"
      "s_mov_b32 s69, 24576\n\t"
      "buffer_load_dwordx4 a[152:155], %[vG2], %[rW2], s69 offen\n\t"
      "s_mov_b32 s69, 28672\n\t"
      "buffer_load_dwordx4 a[156:159], %[vG2], %[rW2], s69 offen\n\t"
      "s_mov_b32 s69, 32768\n\t"
      "buffer_load_dwordx4 a[160:163], %[vG2], %[rW2], s69 offen\n\t"
      "s_mov_b32 s69, 36864\n\t"
      "buffer_load_dwordx4 a[164:167], %[vG2], %[rW2], s69 offen\n\t"
      "s_mov_b32 s69, 40960\n\t"
      "buffer_load_dwordx4 a[168:171], %[vG2], %[rW2], s69 offen\n\t"
      "s_mov_b32 s69, 45056\n\t"
      "buffer_load_dwordx4 a[172:175], %[vG2], %[rW2], s69 offen\n\t"
      "s_mov_b32 s69, 49152\n\t"
      "buffer_load_dwordx4 a[176:179], %[vG2], %[rW2], s69 offen\n\t"
      "s_mov_b32 s69, 53248\n\t"
      "buffer_load_dwordx4 a[180:183], %[vG2], %[rW2], s69 offen\n\t"
      "s_mov_b32 s69, 57344\n\t"
      "buffer_load_dwordx4 a[184:187], %[vG2], %[rW2], s69 offen\n\t"
      "s_mov_b32 s69, 61440\n\t"
      "buffer_load_dwordx4 a[188:191], %[vG2], %[rW2], s69 offen\n\t"
      "s_mov_b32 s69, 65536\n\t"
      "buffer_load_dwordx4 a[192:195], %[vG2], %[rW2], s69 offen\n\t"
      "s_mov_b32 s69, 69632\n\t"
      "buffer_load_dwordx4 a[196:199], %[vG2], %[rW2], s69 offen\n\t"
      "s_mov_b32 s69, 73728\n\t"
      "buffer_load_dwordx4 a[200:203], %[vG2], %[rW2], s69 offen\n\t"
      "s_mov_b32 s69, 77824\n\t"
      "buffer_load_dwordx4 a[204:207], %[vG2], %[rW2], s69 offen\n\t"
      "s_mov_b32 s69, 81920\n\t"
      "buffer_load_dwordx4 a[208:211], %[vG2], %[rW2], s69 offen\n\t"
      "s_mov_b32 s69, 86016\n\t"
      "buffer_load_dwordx4 a[212:215], %[vG2], %[rW2], s69 offen\n\t"
      "s_mov_b32 s69, 90112\n\t"
      "buffer_load_dwordx4 a[216:219], %[vG2], %[rW2], s69 offen\n\t"
      "s_mov_b32 s69, 94208\n\t"
      "buffer_load_dwordx4 a[220:223], %[vG2], %[rW2], s69 offen\n\t"
      "s_mov_b32 s69, 98304\n\t"
      "buffer_load_dwordx4 a[224:227], %[vG2], %[rW2], s69 offen\n\t"
      "s_mov_b32 s69, 102400\n\t"
      "buffer_load_dwordx4 a[228:231], %[vG2], %[rW2], s69 offen\n\t"
      "s_mov_b32 s69, 106496\n\t"
      "buffer_load_dwordx4 a[232:235], %[vG2], %[rW2], s69 offen\n\t"
      "s_mov_b32 s69, 110592\n\t"
      "buffer_load_dwordx4 a[236:239], %[vG2], %[rW2], s69 offen\n\t"
      "s_mov_b32 s69, 114688\n\t"
      "buffer_load_dwordx4 a[240:243], %[vG2], %[rW2], s69 offen\n\t"
      "s_mov_b32 s69, 118784\n\t"
      "buffer_load_dwordx4 a[244:247], %[vG2], %[rW2], s69 offen\n\t"
      "s_mov_b32 s69, 122880\n\t"
      "buffer_load_dwordx4 a[248:251], %[vG2], %[rW2], s69 offen\n\t"
      "s_mov_b32 s69, 126976\n\t"
      "buffer_load_dwordx4 a[252:255], %[vG2], %[rW2], s69 offen\n\t"
      "v_add_u32 v41, %[ldsb], %[vSw]\n\t"
      "v_add_u32 v42, 0x10000, v41\n\t"
      "v_xor_b32 v43, 0, %[vTr]\n\t"
      "v_add_u32 v43, %[ldsb], v43\n\t"
      "v_add_u32 v44, 0x10000, v43\n\t"
      "v_xor_b32 v45, 32, %[vTr]\n\t"
      "v_add_u32 v45, %[ldsb], v45\n\t"
      "v_add_u32 v46, 0x10000, v45\n\t"
      "s_waitcnt vmcnt(63)\n\t"
      "ds_write_b128 v41, a[0:3] offset:0\n\t"
      "s_waitcnt vmcnt(62)\n\t"
      "ds_write_b128 v41, a[4:7] offset:4096\n\t"
      "s_waitcnt vmcnt(61)\n\t"
      "ds_write_b128 v41, a[8:11] offset:8192\n\t"
      "s_waitcnt vmcnt(60)\n\t"
      "ds_write_b128 v41, a[12:15] offset:12288\n\t"
      "s_waitcnt vmcnt(59)\n\t"
      "ds_write_b128 v41, a[16:19] offset:16384\n\t"
      "s_waitcnt vmcnt(58)\n\t"
      "ds_write_b128 v41, a[20:23] offset:20480\n\t"
      "s_waitcnt vmcnt(57)\n\t"
      "ds_write_b128 v41, a[24:27] offset:24576\n\t"
      "s_waitcnt vmcnt(56)\n\t"
      "ds_write_b128 v41, a[28:31] offset:28672\n\t"
      "s_waitcnt vmcnt(55)\n\t"
      "ds_write_b128 v41, a[32:35] offset:32768\n\t"
      "s_waitcnt vmcnt(54)\n\t"
      "ds_write_b128 v41, a[36:39] offset:36864\n\t"
      "s_waitcnt vmcnt(53)\n\t"
      "ds_write_b128 v41, a[40:43] offset:40960\n\t"
      "s_waitcnt vmcnt(52)\n\t"
      "ds_write_b128 v41, a[44:47] offset:45056\n\t"
      "s_waitcnt vmcnt(51)\n\t"
      "ds_write_b128 v41, a[48:51] offset:49152\n\t"
      "s_waitcnt vmcnt(50)\n\t"
      "ds_write_b128 v41, a[52:55] offset:53248\n\t"
      "s_waitcnt vmcnt(49)\n\t"
      "ds_write_b128 v41, a[56:59] offset:57344\n\t"
      "s_waitcnt vmcnt(48)\n\t"
      "ds_write_b128 v41, a[60:63] offset:61440\n\t"
      "s_waitcnt vmcnt(47)\n\t"
      "ds_write_b128 v42, a[64:67] offset:0\n\t"
      "s_waitcnt vmcnt(46)\n\t"
      "ds_write_b128 v42, a[68:71] offset:4096\n\t"
      "s_waitcnt vmcnt(45)\n\t"
      "ds_write_b128 v42, a[72:75] offset:8192\n\t"
      "s_waitcnt vmcnt(44)\n\t"
      "ds_write_b128 v42, a[76:79] offset:12288\n\t"
      "s_waitcnt vmcnt(43)\n\t"
      "ds_write_b128 v42, a[80:83] offset:16384\n\t"
      "s_waitcnt vmcnt(42)\n\t"
      "ds_write_b128 v42, a[84:87] offset:20480\n\t"
      "s_waitcnt vmcnt(41)\n\t"
      "ds_write_b128 v42, a[88:91] offset:24576\n\t"
      "s_waitcnt vmcnt(40)\n\t"
      "ds_write_b128 v42, a[92:95] offset:28672\n\t"
      "s_waitcnt vmcnt(39)\n\t"
      "ds_write_b128 v42, a[96:99] offset:32768\n\t"
      "s_waitcnt vmcnt(38)\n\t"
      "ds_write_b128 v42, a[100:103] offset:36864\n\t"
      "s_waitcnt vmcnt(37)\n\t"
      "ds_write_b128 v42, a[104:107] offset:40960\n\t"
      "s_waitcnt vmcnt(36)\n\t"
      "ds_write_b128 v42, a[108:111] offset:45056\n\t"
      "s_waitcnt vmcnt(35)\n\t"
      "ds_write_b128 v42, a[112:115] offset:49152\n\t"
      "s_waitcnt vmcnt(34)\n\t"
      "ds_write_b128 v42, a[116:119] offset:53248\n\t"
      "s_waitcnt vmcnt(33)\n\t"
      "ds_write_b128 v42, a[120:123] offset:57344\n\t"
      "s_waitcnt vmcnt(32)\n\t"
      "ds_write_b128 v42, a[124:127] offset:61440\n\t"
      "s_waitcnt lgkmcnt(0)\n\t"
      "s_barrier\n\t"
      "ds_read_b64_tr_b16 a[0:1], v43 offset:0\n\t"
      "ds_read_b64_tr_b16 a[2:3], v43 offset:4096\n\t"
      "ds_read_b64_tr_b16 a[4:5], v43 offset:8192\n\t"
      "ds_read_b64_tr_b16 a[6:7], v43 offset:12288\n\t"
      "ds_read_b64_tr_b16 a[8:9], v43 offset:16384\n\t"
      "ds_read_b64_tr_b16 a[10:11], v43 offset:20480\n\t"
      "ds_read_b64_tr_b16 a[12:13], v43 offset:24576\n\t"
      "ds_read_b64_tr_b16 a[14:15], v43 offset:28672\n\t"
      "ds_read_b64_tr_b16 a[16:17], v43 offset:32768\n\t"
      "ds_read_b64_tr_b16 a[18:19], v43 offset:36864\n\t"
      "ds_read_b64_tr_b16 a[20:21], v43 offset:40960\n\t"
      "ds_read_b64_tr_b16 a[22:23], v43 offset:45056\n\t"
      "ds_read_b64_tr_b16 a[24:25], v43 offset:49152\n\t"
      "ds_read_b64_tr_b16 a[26:27], v43 offset:53248\n\t"
      "ds_read_b64_tr_b16 a[28:29], v43 offset:57344\n\t"
      "ds_read_b64_tr_b16 a[30:31], v43 offset:61440\n\t"
      "ds_read_b64_tr_b16 a[32:33], v44 offset:0\n\t"
      "ds_read_b64_tr_b16 a[34:35], v44 offset:4096\n\t"
      "ds_read_b64_tr_b16 a[36:37], v44 offset:8192\n\t"
      "ds_read_b64_tr_b16 a[38:39], v44 offset:12288\n\t"
      "ds_read_b64_tr_b16 a[40:41], v44 offset:16384\n\t"
      "ds_read_b64_tr_b16 a[42:43], v44 offset:20480\n\t"
      "ds_read_b64_tr_b16 a[44:45], v44 offset:24576\n\t"
      "ds_read_b64_tr_b16 a[46:47], v44 offset:28672\n\t"
      "ds_read_b64_tr_b16 a[48:49], v44 offset:32768\n\t"
      "ds_read_b64_tr_b16 a[50:51], v44 offset:36864\n\t"
      "ds_read_b64_tr_b16 a[52:53], v44 offset:40960\n\t"
      "ds_read_b64_tr_b16 a[54:55], v44 offset:45056\n\t"
      "ds_read_b64_tr_b16 a[56:57], v44 offset:49152\n\t"
      "ds_read_b64_tr_b16 a[58:59], v44 offset:53248\n\t"
      "ds_read_b64_tr_b16 a[60:61], v44 offset:57344\n\t"
      "ds_read_b64_tr_b16 a[62:63], v44 offset:61440\n\t"
      "ds_read_b64_tr_b16 a[64:65], v45 offset:0\n\t"
      "ds_read_b64_tr_b16 a[66:67], v45 offset:4096\n\t"
      "ds_read_b64_tr_b16 a[68:69], v45 offset:8192\n\t"
      "ds_read_b64_tr_b16 a[70:71], v45 offset:12288\n\t"
      "ds_read_b64_tr_b16 a[72:73], v45 offset:16384\n\t"
      "ds_read_b64_tr_b16 a[74:75], v45 offset:20480\n\t"
      "ds_read_b64_tr_b16 a[76:77], v45 offset:24576\n\t"
      "ds_read_b64_tr_b16 a[78:79], v45 offset:28672\n\t"
      "ds_read_b64_tr_b16 a[80:81], v45 offset:32768\n\t"
      "ds_read_b64_tr_b16 a[82:83], v45 offset:36864\n\t"
      "ds_read_b64_tr_b16 a[84:85], v45 offset:40960\n\t"
      "ds_read_b64_tr_b16 a[86:87], v45 offset:45056\n\t"
      "ds_read_b64_tr_b16 a[88:89], v45 offset:49152\n\t"
      "ds_read_b64_tr_b16 a[90:91], v45 offset:53248\n\t"
      "ds_read_b64_tr_b16 a[92:93], v45 offset:57344\n\t"
      "ds_read_b64_tr_b16 a[94:95], v45 offset:61440\n\t"
      "ds_read_b64_tr_b16 a[96:97], v46 offset:0\n\t"
      "ds_read_b64_tr_b16 a[98:99], v46 offset:4096\n\t"
      "ds_read_b64_tr_b16 a[100:101], v46 offset:8192\n\t"
      "ds_read_b64_tr_b16 a[102:103], v46 offset:12288\n\t"
      "ds_read_b64_tr_b16 a[104:105], v46 offset:16384\n\t"
      "ds_read_b64_tr_b16 a[106:107], v46 offset:20480\n\t"
      "ds_read_b64_tr_b16 a[108:109], v46 offset:24576\n\t"
      "ds_read_b64_tr_b16 a[110:111], v46 offset:28672\n\t"
      "ds_read_b64_tr_b16 a[112:113], v46 offset:32768\n\t"
      "ds_read_b64_tr_b16 a[114:115], v46 offset:36864\n\t"
      "ds_read_b64_tr_b16 a[116:117], v46 offset:40960\n\t"
      "ds_read_b64_tr_b16 a[118:119], v46 offset:45056\n\t"
      "ds_read_b64_tr_b16 a[120:121], v46 offset:49152\n\t"
      "ds_read_b64_tr_b16 a[122:123], v46 offset:53248\n\t"
      "ds_read_b64_tr_b16 a[124:125], v46 offset:57344\n\t"
      "ds_read_b64_tr_b16 a[126:127], v46 offset:61440\n\t"
      "v_mov_b32 v180, 0x3f803f80\n\t"
      "v_mov_b32 v181, 0x3f803f80\n\t"
      "v_mov_b32 v182, 0x3f803f80\n\t"
      "v_mov_b32 v183, 0x3f803f80\n\t"
      "s_waitcnt lgkmcnt(0)\n\t"
      "s_barrier\n\t"
      "v_add_u32 v41, %[ldsb], %[vSw2]\n\t"
      "v_xor_b32 v42, 128, %[vSw2]\n\t"
      "v_add_u32 v42, %[ldsb], v42\n\t"
      "v_xor_b32 v43, 0, %[vTr2]\n\t"
      "v_add_u32 v43, %[ldsb], v43\n\t"
      "v_xor_b32 v44, 32, %[vTr2]\n\t"
      "v_add_u32 v44, %[ldsb], v44\n\t"
      "v_xor_b32 v45, 64, %[vTr2]\n\t"
      "v_add_u32 v45, %[ldsb], v45\n\t"
      "v_xor_b32 v46, 96, %[vTr2]\n\t"
      "v_add_u32 v46, %[ldsb], v46\n\t"
      "v_xor_b32 v47, 128, %[vTr2]\n\t"
      "v_add_u32 v47, %[ldsb], v47\n\t"
      "v_xor_b32 v48, 160, %[vTr2]\n\t"
      "v_add_u32 v48, %[ldsb], v48\n\t"
      "v_xor_b32 v49, 192, %[vTr2]\n\t"
      "v_add_u32 v49, %[ldsb], v49\n\t"
      "v_xor_b32 v50, 224, %[vTr2]\n\t"
      "v_add_u32 v50, %[ldsb], v50\n\t"
      "s_waitcnt vmcnt(31)\n\t"
      "ds_write_b128 v41, a[128:131] offset:0\n\t"
      "v_mfma_f32_16x16x32_bf16 v[152:155], a[0:3], v[180:183], 0\n\t"
      "s_waitcnt vmcnt(30)\n\t"
      "ds_write_b128 v42, a[132:135] offset:1024\n\t"
      "v_mfma_f32_16x16x32_bf16 v[156:159], a[64:67], v[180:183], 0\n\t"
      "s_waitcnt vmcnt(29)\n\t"
      "ds_write_b128 v41, a[136:139] offset:2048\n\t"
      "v_mfma_f32_16x16x32_bf16 v[152:155], a[4:7], v[180:183], v[152:155]\n\t"
      "s_waitcnt vmcnt(28)\n\t"
      "ds_write_b128 v42, a[140:143] offset:3072\n\t"
      "v_mfma_f32_16x16x32_bf16 v[156:159], a[68:71], v[180:183], v[156:159]\n\t"
      "s_waitcnt vmcnt(27)\n\t"
      "ds_write_b128 v41, a[144:147] offset:4096\n\t"
      "v_mfma_f32_16x16x32_bf16 v[152:155], a[8:11], v[180:183], v[152:155]\n\t"
      "s_waitcnt vmcnt(26)\n\t"
      "ds_write_b128 v42, a[148:151] offset:5120\n\t"
      "v_mfma_f32_16x16x32_bf16 v[156:159], a[72:75], v[180:183], v[156:159]\n\t"
      "s_waitcnt vmcnt(25)\n\t"
      "ds_write_b128 v41, a[152:155] offset:6144\n\t"
      "v_mfma_f32_16x16x32_bf16 v[152:155], a[12:15], v[180:183], v[152:155]\n\t"
      "s_waitcnt vmcnt(24)\n\t"
      "ds_write_b128 v42, a[156:159] offset:7168\n\t"
      "v_mfma_f32_16x16x32_bf16 v[156:159], a[76:79], v[180:183], v[156:159]\n\t"
      "s_waitcnt vmcnt(23)\n\t"
      "ds_write_b128 v41, a[160:163] offset:8192\n\t"
      "v_mfma_f32_16x16x32_bf16 v[152:155], a[16:19], v[180:183], v[152:155]\n\t"
      "s_waitcnt vmcnt(22)\n\t"
      "ds_write_b128 v42, a[164:167] offset:9216\n\t"
      "v_mfma_f32_16x16x32_bf16 v[156:159], a[80:83], v[180:183], v[156:159]\n\t"
      "s_waitcnt vmcnt(21)\n\t"
      "ds_write_b128 v41, a[168:171] offset:10240\n\t"
      "v_mfma_f32_16x16x32_bf16 v[152:155], a[20:23], v[180:183], v[152:155]\n\t"
      "s_waitcnt vmcnt(20)\n\t"
      "ds_write_b128 v42, a[172:175] offset:11264\n\t"
      "v_mfma_f32_16x16x32_bf16 v[156:159], a[84:87], v[180:183], v[156:159]\n\t"
      "s_waitcnt vmcnt(19)\n\t"
      "ds_write_b128 v41, a[176:179] offset:12288\n\t"
      "v_mfma_f32_16x16x32_bf16 v[152:155], a[24:27], v[180:183], v[152:155]\n\t"
      "s_waitcnt vmcnt(18)\n\t"
      "ds_write_b128 v42, a[180:183] offset:13312\n\t"
      "v_mfma_f32_16x16x32_bf16 v[156:159], a[88:91], v[180:183], v[156:159]\n\t"
      "s_waitcnt vmcnt(17)\n\t"
      "ds_write_b128 v41, a[184:187] offset:14336\n\t"
      "v_mfma_f32_16x16x32_bf16 v[152:155], a[28:31], v[180:183], v[152:155]\n\t"
      "s_waitcnt vmcnt(16)\n\t"
      "ds_write_b128 v42, a[188:191] offset:15360\n\t"
      "v_mfma_f32_16x16x32_bf16 v[156:159], a[92:95], v[180:183], v[156:159]\n\t"
      "s_waitcnt vmcnt(15)\n\t"
      "ds_write_b128 v41, a[192:195] offset:16384\n\t"
      "v_mfma_f32_16x16x32_bf16 v[152:155], a[32:35], v[180:183], v[152:155]\n\t"
      "s_waitcnt vmcnt(14)\n\t"
      "ds_write_b128 v42, a[196:199] offset:17408\n\t"
      "v_mfma_f32_16x16x32_bf16 v[156:159], a[96:99], v[180:183], v[156:159]\n\t"
      "s_waitcnt vmcnt(13)\n\t"
      "ds_write_b128 v41, a[200:203] offset:18432\n\t"
      "v_mfma_f32_16x16x32_bf16 v[152:155], a[36:39], v[180:183], v[152:155]\n\t"
      "s_waitcnt vmcnt(12)\n\t"
      "ds_write_b128 v42, a[204:207] offset:19456\n\t"
      "v_mfma_f32_16x16x32_bf16 v[156:159], a[100:103], v[180:183], v[156:159]\n\t"
      "s_waitcnt vmcnt(11)\n\t"
      "ds_write_b128 v41, a[208:211] offset:20480\n\t"
      "v_mfma_f32_16x16x32_bf16 v[152:155], a[40:43], v[180:183], v[152:155]\n\t"
      "s_waitcnt vmcnt(10)\n\t"
      "ds_write_b128 v42, a[212:215] offset:21504\n\t"
      "v_mfma_f32_16x16x32_bf16 v[156:159], a[104:107], v[180:183], v[156:159]\n\t"
      "s_waitcnt vmcnt(9)\n\t"
      "ds_write_b128 v41, a[216:219] offset:22528\n\t"
      "v_mfma_f32_16x16x32_bf16 v[152:155], a[44:47], v[180:183], v[152:155]\n\t"
      "s_waitcnt vmcnt(8)\n\t"
      "ds_write_b128 v42, a[220:223] offset:23552\n\t"
      "v_mfma_f32_16x16x32_bf16 v[156:159], a[108:111], v[180:183], v[156:159]\n\t"
      "s_waitcnt vmcnt(7)\n\t"
      "ds_write_b128 v41, a[224:227] offset:24576\n\t"
      "v_mfma_f32_16x16x32_bf16 v[152:155], a[48:51], v[180:183], v[152:155]\n\t"
      "s_waitcnt vmcnt(6)\n\t"
      "ds_write_b128 v42, a[228:231] offset:25600\n\t"
      "v_mfma_f32_16x16x32_bf16 v[156:159], a[112:115], v[180:183], v[156:159]\n\t"
      "s_waitcnt vmcnt(5)\n\t"
      "ds_write_b128 v41, a[232:235] offset:26624\n\t"
      "v_mfma_f32_16x16x32_bf16 v[152:155], a[52:55], v[180:183], v[152:155]\n\t"
      "s_waitcnt vmcnt(4)\n\t"
      "ds_write_b128 v42, a[236:239] offset:27648\n\t"
      "v_mfma_f32_16x16x32_bf16 v[156:159], a[116:119], v[180:183], v[156:159]\n\t"
      "s_waitcnt vmcnt(3)\n\t"
      "ds_write_b128 v41, a[240:243] offset:28672\n\t"
      "v_mfma_f32_16x16x32_bf16 v[152:155], a[56:59], v[180:183], v[152:155]\n\t"
      "s_waitcnt vmcnt(2)\n\t"
      "ds_write_b128 v42, a[244:247] offset:29696\n\t"
      "v_mfma_f32_16x16x32_bf16 v[156:159], a[120:123], v[180:183], v[156:159]\n\t"
      "s_waitcnt vmcnt(1)\n\t"
      "ds_write_b128 v41, a[248:251] offset:30720\n\t"
      "v_mfma_f32_16x16x32_bf16 v[152:155], a[60:63], v[180:183], v[152:155]\n\t"
      "s_waitcnt vmcnt(0)\n\t"
      "ds_write_b128 v42, a[252:255] offset:31744\n\t"
      "v_mfma_f32_16x16x32_bf16 v[156:159], a[124:127], v[180:183], v[156:159]\n\t"
      "s_mov_b32 s69, 188747776\n\t"
      "buffer_load_dwordx4 v[64:67], %[vL], %[rs], s69 offen sc1\n\t"
      "s_waitcnt lgkmcnt(0)\n\t"
      "ds_read_b64_tr_b16 a[128:129], v43 offset:0\n\t"
      "ds_read_b64_tr_b16 a[130:131], v43 offset:4096\n\t"
      "ds_read_b64_tr_b16 a[132:133], v43 offset:8192\n\t"
      "ds_read_b64_tr_b16 a[134:135], v43 offset:12288\n\t"
      "ds_read_b64_tr_b16 a[136:137], v43 offset:16384\n\t"
      "ds_read_b64_tr_b16 a[138:139], v43 offset:20480\n\t"
      "ds_read_b64_tr_b16 a[140:141], v43 offset:24576\n\t"
      "ds_read_b64_tr_b16 a[142:143], v43 offset:28672\n\t"
      "ds_read_b64_tr_b16 a[144:145], v44 offset:0\n\t"
      "ds_read_b64_tr_b16 a[146:147], v44 offset:4096\n\t"
      "ds_read_b64_tr_b16 a[148:149], v44 offset:8192\n\t"
      "ds_read_b64_tr_b16 a[150:151], v44 offset:12288\n\t"
      "ds_read_b64_tr_b16 a[152:153], v44 offset:16384\n\t"
      "ds_read_b64_tr_b16 a[154:155], v44 offset:20480\n\t"
      "ds_read_b64_tr_b16 a[156:157], v44 offset:24576\n\t"
      "ds_read_b64_tr_b16 a[158:159], v44 offset:28672\n\t"
      "ds_read_b64_tr_b16 a[160:161], v45 offset:0\n\t"
      "ds_read_b64_tr_b16 a[162:163], v45 offset:4096\n\t"
      "ds_read_b64_tr_b16 a[164:165], v45 offset:8192\n\t"
      "ds_read_b64_tr_b16 a[166:167], v45 offset:12288\n\t"
      "ds_read_b64_tr_b16 a[168:169], v45 offset:16384\n\t"
      "ds_read_b64_tr_b16 a[170:171], v45 offset:20480\n\t"
      "ds_read_b64_tr_b16 a[172:173], v45 offset:24576\n\t"
      "ds_read_b64_tr_b16 a[174:175], v45 offset:28672\n\t"
      "ds_read_b64_tr_b16 a[176:177], v46 offset:0\n\t"
      "ds_read_b64_tr_b16 a[178:179], v46 offset:4096\n\t"
      "ds_read_b64_tr_b16 a[180:181], v46 offset:8192\n\t"
      "ds_read_b64_tr_b16 a[182:183], v46 offset:12288\n\t"
      "ds_read_b64_tr_b16 a[184:185], v46 offset:16384\n\t"
      "ds_read_b64_tr_b16 a[186:187], v46 offset:20480\n\t"
      "ds_read_b64_tr_b16 a[188:189], v46 offset:24576\n\t"
      "ds_read_b64_tr_b16 a[190:191], v46 offset:28672\n\t"
      "ds_read_b64_tr_b16 a[192:193], v47 offset:0\n\t"
      "ds_read_b64_tr_b16 a[194:195], v47 offset:4096\n\t"
      "ds_read_b64_tr_b16 a[196:197], v47 offset:8192\n\t"
      "ds_read_b64_tr_b16 a[198:199], v47 offset:12288\n\t"
      "ds_read_b64_tr_b16 a[200:201], v47 offset:16384\n\t"
      "ds_read_b64_tr_b16 a[202:203], v47 offset:20480\n\t"
      "ds_read_b64_tr_b16 a[204:205], v47 offset:24576\n\t"
      "ds_read_b64_tr_b16 a[206:207], v47 offset:28672\n\t"
      "ds_read_b64_tr_b16 a[208:209], v48 offset:0\n\t"
      "ds_read_b64_tr_b16 a[210:211], v48 offset:4096\n\t"
      "ds_read_b64_tr_b16 a[212:213], v48 offset:8192\n\t"
      "ds_read_b64_tr_b16 a[214:215], v48 offset:12288\n\t"
      "ds_read_b64_tr_b16 a[216:217], v48 offset:16384\n\t"
      "ds_read_b64_tr_b16 a[218:219], v48 offset:20480\n\t"
      "ds_read_b64_tr_b16 a[220:221], v48 offset:24576\n\t"
      "ds_read_b64_tr_b16 a[222:223], v48 offset:28672\n\t"
      "ds_read_b64_tr_b16 a[224:225], v49 offset:0\n\t"
      "ds_read_b64_tr_b16 a[226:227], v49 offset:4096\n\t"
      "ds_read_b64_tr_b16 a[228:229], v49 offset:8192\n\t"
      "ds_read_b64_tr_b16 a[230:231], v49 offset:12288\n\t"
      "ds_read_b64_tr_b16 a[232:233], v49 offset:16384\n\t"
      "ds_read_b64_tr_b16 a[234:235], v49 offset:20480\n\t"
      "ds_read_b64_tr_b16 a[236:237], v49 offset:24576\n\t"
      "ds_read_b64_tr_b16 a[238:239], v49 offset:28672\n\t"
      "ds_read_b64_tr_b16 a[240:241], v50 offset:0\n\t"
      "ds_read_b64_tr_b16 a[242:243], v50 offset:4096\n\t"
      "ds_read_b64_tr_b16 a[244:245], v50 offset:8192\n\t"
      "ds_read_b64_tr_b16 a[246:247], v50 offset:12288\n\t"
      "ds_read_b64_tr_b16 a[248:249], v50 offset:16384\n\t"
      "ds_read_b64_tr_b16 a[250:251], v50 offset:20480\n\t"
      "ds_read_b64_tr_b16 a[252:253], v50 offset:24576\n\t"
      "ds_read_b64_tr_b16 a[254:255], v50 offset:28672\n\t"
      "s_waitcnt lgkmcnt(0)\n\t"
      "s_barrier\n\t"
      "s_mul_i32 s60, s60, 0x9e3779b1\n\t"
      "s_add_u32 s60, s60, 0x7f4a7c15\n\t"
      "s_branch LIN1_%=\n\t"
      "LIN0_%=:\n\t"
      "s_add_u32 s71, s71, 1\n\t"
      "s_cmp_gt_u32 s71, 131072\n\t"
      "s_cbranch_scc1 LFAIL_%=\n\t"
      "s_sleep 4\n\t"
      "s_mov_b32 s69, 188747776\n\t"
      "buffer_load_dwordx4 v[64:67], %[vL], %[rs], s69 offen sc1\n\t"
      "LIN1_%=:\n\t"
      "s_waitcnt vmcnt(0)\n\t"
      "v_cmp_ne_u32 vcc, s60, v64\n\t"
      "v_cmp_ne_u32 s[64:65], s60, v66\n\t"
      "s_or_b64 vcc, vcc, s[64:65]\n\t"
      "s_cbranch_scc1 LIN0_%=\n\t"
      "v_add_u32 v64, v65, v67\n\t"
      "s_nop 1\n\t"
      "v_readlane_b32 s69, v64, 0\n\t"
      "v_readlane_b32 s61, v64, 1\n\t"
      "s_cmp_lt_u32 s61, s69\n\t"
      "s_cselect_b32 s61, 8192, 0\n\t"
      "v_readlane_b32 s69, v64, 2\n\t"
      "v_readlane_b32 s62, v64, 3\n\t"
      "s_cmp_lt_u32 s62, s69\n\t"
      "s_cselect_b32 s62, 8192, 0\n\t"
      "s_mov_b32 s78, s61\n\t"
      "LLAYER_%=:\n\t"
      "s_mov_b32 s68, -1\n\t"
      "s_lshl_b32 s76, s55, 6\n\t"
      "s_sub_u32 s52, s76, 1\n\t"
      "s_mov_b32 s84, 65536\n\t"
      "s_mov_b32 s85, 0\n\t"
      "s_mov_b32 s86, 32768\n\t"
      "s_lshl_b32 s69, s55, 22\n\t"
      "s_add_u32 s88, s69, 174063616\n\t"
      "s_lshl_b32 s69, %[wv], 13\n\t"
      "s_add_u32 s88, s88, s69\n\t"
      "s_lshl_b32 s69, s55, 19\n\t"
      "s_add_u32 s89, s69, 189792256\n\t"
      "s_lshl_b32 s69, %[wv], 10\n\t"
      "s_add_u32 s89, s89, s69\n\t"
      "v_mov_b32 v248, 16\n\t"
      "v_mov_b32 v249, 16\n\t"
      "v_mov_b32 v250, 16\n\t"
      "v_mov_b32 v251, 16\n\t"
      "s_mov_b32 s90, 0x40000000\n\t"
      "s_mov_b32 s91, 0x40000000\n\t"
      "v_add_u32 v244, s85, %[vLd]\n\t"
      "s_lshr_b32 s69, s85, 3\n\t"
      "s_add_u32 s69, s69, 98304\n\t"
      "v_add_u32 v245, s69, %[vLd]\n\t"
      "s_cmp_eq_u32 s55, 0\n\t"
      "s_cbranch_scc1 LENT_%=\n\t"
      "s_cmp_eq_u32 %[wv], 0\n\t"
      "s_cbranch_scc0 LPW_%=\n\t"
      "LPP_%=:\n\t"
      "buffer_load_dword v252, %[vB1], %[rc], s78 offen sc1\n\t"
      "s_add_u32 s71, s71, 1\n\t"
      "s_cmp_gt_u32 s71, 131072\n\t"
      "s_cbranch_scc1 LFAIL_%=\n\t"
      "s_sleep 32\n\t"
      "s_waitcnt vmcnt(0)\n\t"
      "v_cmp_gt_i32 vcc, 0x64, v252\n\t"
      "s_cbranch_vccnz LPP_%=\n\t"
      "s_mov_b64 exec, 63\n\t"
      "LPQ_%=:\n\t"
      "buffer_load_dwordx4 v[248:251], %[vX2], %[rc], s78 offen sc1\n\t"
      "s_add_u32 s71, s71, 1\n\t"
      "s_cmp_gt_u32 s71, 131072\n\t"
      "s_cbranch_scc1 LPV_%=\n\t"
      "s_waitcnt vmcnt(0)\n\t"
      "v_min_i32 v252, v248, v249\n\t"
      "v_min3_i32 v252, v252, v250, v251\n\t"
      "s_add_u32 s94, s68, 1\n\t"
      "s_add_u32 s94, s94, s76\n\t"
      "v_cmp_lt_i32 vcc, s94, v252\n\t"
      "s_and_b32 s94, s94, 15\n\t"
      "s_lshl_b32 s94, s94, 1\n\t"
      "s_lshr_b32 s94, vcc_lo, s94\n\t"
      "s_andn2_b32 s94, 3, s94\n\t"
      "s_cbranch_scc1 LPQ_%=\n\t"
      "LPV_%=:\n\t"
      "s_mov_b64 exec, -1\n\t"
      "s_waitcnt vmcnt(0)\n\t"
      "ds_write_b128 %[vFl], v[248:251]\n\t"
      "s_waitcnt lgkmcnt(0)\n\t"
      "LPW_%=:\n\t"
      "v_add_u32 v252, 110592, %[vLd]\n\t"
      "s_barrier\n\t"
      "ds_read_b128 v[248:251], v252\n\t"
      "s_waitcnt lgkmcnt(0)\n\t"
      "v_min_i32 v252, v248, v249\n\t"
      "v_min3_i32 v252, v252, v250, v251\n\t"
      "s_add_u32 s94, s68, 1\n\t"
      "s_add_u32 s94, s94, s76\n\t"
      "v_cmp_lt_i32 vcc, s94, v252\n\t"
      "s_and_b32 s94, s94, 15\n\t"
      "s_lshl_b32 s94, s94, 1\n\t"
      "s_lshr_b32 s94, vcc_lo, s94\n\t"
      "s_andn2_b32 s94, 3, s94\n\t"
      "s_cbranch_scc1 LFAIL_%=\n\t"
      "LENT_%=:\n\t"
      "s_add_u32 m0, s97, 12288\n\t"
      "s_nop 0\n\t"
      "buffer_load_dwordx4 %[vX2], %[rc], s78 offen sc1 lds\n\t"
      "s_add_u32 s94, s68, 1\n\t"
      "s_lshl_b32 s79, s94, 15\n\t"
      "s_add_u32 s79, s79, s88\n\t"
      "s_lshl_b32 s80, s94, 12\n\t"
      "s_add_u32 s80, s80, s89\n\t"
      "s_add_u32 m0, s85, s96\n\t"
      "s_add_u32 s98, s79, 4096\n\t"
      "buffer_load_dwordx4 %[vL], %[rs], s79 offen offset:0 sc1 lds\n\t"
      "buffer_load_dwordx4 %[vL], %[rs], s79 offen offset:1024 sc1 lds\n\t"
      "buffer_load_dwordx4 %[vL], %[rs], s79 offen offset:2048 sc1 lds\n\t"
      "buffer_load_dwordx4 %[vL], %[rs], s79 offen offset:3072 sc1 lds\n\t"
      "s_add_u32 m0, m0, 4096\n\t"
      "s_lshr_b32 s99, s85, 3\n\t"
      "buffer_load_dwordx4 %[vL], %[rs], s98 offen offset:0 sc1 lds\n\t"
      "buffer_load_dwordx4 %[vL], %[rs], s98 offen offset:1024 sc1 lds\n\t"
      "buffer_load_dwordx4 %[vL], %[rs], s98 offen offset:2048 sc1 lds\n\t"
      "buffer_load_dwordx4 %[vL], %[rs], s98 offen offset:3072 sc1 lds\n\t"
      "s_add_u32 m0, s99, s97\n\t"
      "s_nop 0\n\t"
      "buffer_load_dwordx4 %[vL], %[rs], s80 offen offset:0 sc1 lds\n\t"
      "s_add_u32 s94, s68, 2\n\t"
      "s_lshl_b32 s72, s94, 15\n\t"
      "s_add_u32 s72, s72, s88\n\t"
      "s_lshl_b32 s73, s94, 12\n\t"
      "s_add_u32 s73, s73, s89\n\t"
      "v_min_i32 v252, v248, v249\n\t"
      "v_min3_i32 v252, v252, v250, v251\n\t"
      "s_add_u32 s94, s68, 2\n\t"
      "s_add_u32 s94, s94, s76\n\t"
      "v_cmp_lt_i32 vcc, s94, v252\n\t"
      "s_and_b32 s94, s94, 15\n\t"
      "s_lshl_b32 s94, s94, 1\n\t"
      "s_lshr_b32 s94, vcc_lo, s94\n\t"
      "s_andn2_b32 s94, 3, s94\n\t"
      "s_cselect_b32 s87, 0, 2\n\t"
      "s_cselect_b32 s72, 0x40000000, s72\n\t"
      "s_cselect_b32 s73, 0x40000000, s73\n\t"
      "s_add_u32 m0, s86, s96\n\t"
      "s_add_u32 s98, s72, 4096\n\t"
      "buffer_load_dwordx4 %[vL], %[rs], s72 offen offset:0 sc1 lds\n\t"
      "buffer_load_dwordx4 %[vL], %[rs], s72 offen offset:1024 sc1 lds\n\t"
      "buffer_load_dwordx4 %[vL], %[rs], s72 offen offset:2048 sc1 lds\n\t"
      "buffer_load_dwordx4 %[vL], %[rs], s72 offen offset:3072 sc1 lds\n\t"
      "s_add_u32 m0, m0, 4096\n\t"
      "s_lshr_b32 s99, s86, 3\n\t"
      "buffer_load_dwordx4 %[vL], %[rs], s98 offen offset:0 sc1 lds\n\t"
      "buffer_load_dwordx4 %[vL], %[rs], s98 offen offset:1024 sc1 lds\n\t"
      "buffer_load_dwordx4 %[vL], %[rs], s98 offen offset:2048 sc1 lds\n\t"
      "buffer_load_dwordx4 %[vL], %[rs], s98 offen offset:3072 sc1 lds\n\t"
      "s_add_u32 m0, s99, s97\n\t"
      "s_nop 0\n\t"
      "buffer_load_dwordx4 %[vL], %[rs], s73 offen offset:0 sc1 lds\n\t"
      "s_add_u32 s94, s68, 3\n\t"
      "s_lshl_b32 s72, s94, 15\n\t"
      "s_add_u32 s72, s72, s88\n\t"
      "s_lshl_b32 s73, s94, 12\n\t"
      "s_add_u32 s73, s73, s89\n\t"
      "s_cmp_gt_i32 s68, 60\n\t"
      "s_cselect_b32 s74, 0, 3\n\t"
      "v_min_i32 v252, v248, v249\n\t"
      "v_min3_i32 v252, v252, v250, v251\n\t"
      "s_add_u32 s94, s68, 3\n\t"
      "s_add_u32 s94, s94, s76\n\t"
      "v_cmp_lt_i32 vcc, s94, v252\n\t"
      "s_and_b32 s94, s94, 15\n\t"
      "s_lshl_b32 s94, s94, 1\n\t"
      "s_lshr_b32 s94, vcc_lo, s94\n\t"
      "s_andn2_b32 s94, s74, s94\n\t"
      "s_cselect_b32 s74, 0, 4\n\t"
      "s_cselect_b32 s72, 0x40000000, s72\n\t"
      "s_cselect_b32 s73, 0x40000000, s73\n\t"
      "s_or_b32 s87, s87, s74\n\t"
      "s_lshr_b32 s87, s87, 1\n\t"
      "s_add_u32 m0, s97, 12288\n\t"
      "s_mov_b32 s94, s84\n\t"
      "buffer_load_dwordx4 %[vX2], %[rc], s78 offen sc1 lds\n\t"
      "s_add_u32 m0, s94, s96\n\t"
      "s_add_u32 s98, s72, 4096\n\t"
      "buffer_load_dwordx4 %[vL], %[rs], s72 offen offset:0 sc1 lds\n\t"
      "buffer_load_dwordx4 %[vL], %[rs], s72 offen offset:1024 sc1 lds\n\t"
      "buffer_load_dwordx4 %[vL], %[rs], s72 offen offset:2048 sc1 lds\n\t"
      "buffer_load_dwordx4 %[vL], %[rs], s72 offen offset:3072 sc1 lds\n\t"
      "s_add_u32 m0, m0, 4096\n\t"
      "s_lshr_b32 s99, s94, 3\n\t"
      "buffer_load_dwordx4 %[vL], %[rs], s98 offen offset:0 sc1 lds\n\t"
      "buffer_load_dwordx4 %[vL], %[rs], s98 offen offset:1024 sc1 lds\n\t"
      "buffer_load_dwordx4 %[vL], %[rs], s98 offen offset:2048 sc1 lds\n\t"
      "buffer_load_dwordx4 %[vL], %[rs], s98 offen offset:3072 sc1 lds\n\t"
      "s_add_u32 m0, s99, s97\n\t"
      "s_nop 0\n\t"
      "buffer_load_dwordx4 %[vL], %[rs], s73 offen offset:0 sc1 lds\n\t"
      "s_mov_b32 s84, s85\n\t"
      "s_mov_b32 s85, s86\n\t"
      "s_mov_b32 s86, s94\n\t"
      "s_cmp_eq_u32 s87, 3\n\t"
      "s_cbranch_scc0 LSC_%=\n\t"
      "s_waitcnt vmcnt(19)\n\t"
      "LSR_%=:\n\t"
      "s_barrier\n\t"
      "ds_read_b128 v[160:163], v245 offset:0\n\t"
      "ds_read_b128 v[164:167], v245 offset:1024\n\t"
      "ds_read_b128 v[168:171], v245 offset:2048\n\t"
      "ds_read_b128 v[172:175], v245 offset:3072\n\t"
      "ds_read_b128 v[40:43], v244 offset:0\n\t"
      "ds_read_b128 v[44:47], v244 offset:1024\n\t"
      "ds_read_b128 v[48:51], v244 offset:2048\n\t"
      "ds_read_b128 v[52:55], v244 offset:3072\n\t"
      "ds_read_b128 v[56:59], v244 offset:4096\n\t"
      "ds_read_b128 v[60:63], v244 offset:5120\n\t"
      "ds_read_b128 v[64:67], v244 offset:6144\n\t"
      "ds_read_b128 v[68:71], v244 offset:7168\n\t"
      "ds_read_b128 v[72:75], v244 offset:8192\n\t"
      "ds_read_b128 v[76:79], v244 offset:9216\n\t"
      "ds_read_b128 v[80:83], v244 offset:10240\n\t"
      "ds_read_b128 v[84:87], v244 offset:11264\n\t"
      "ds_read_b128 v[88:91], v244 offset:12288\n\t"
      "ds_read_b128 v[92:95], v244 offset:13312\n\t"
      "ds_read_b128 v[96:99], v244 offset:14336\n\t"
      "ds_read_b128 v[100:103], v244 offset:15360\n\t"
      "s_waitcnt lgkmcnt(15)\n\t"
      "v_add_f32 v236, v160, v162\n\t"
      "v_add_f32 v237, v164, v166\n\t"
      "v_add_f32 v236, v236, v168\n\t"
      "v_add_f32 v237, v237, v170\n\t"
      "v_add_f32 v236, v236, v172\n\t"
      "v_add_f32 v237, v237, v174\n\t"
      "v_add_f32 v236, v236, v237\n\t"
      "v_mov_b32 v241, v236\n\t"
      "v_add_f32 v238, v161, v163\n\t"
      "v_add_f32 v239, v165, v167\n\t"
      "v_permlane16_swap_b32 v236, v241\n\t"
      "v_add_f32 v240, v236, v241\n\t"
      "v_mul_f32 v240, s92, v240\n\t"
      "v_add_f32 v238, v238, v169\n\t"
      "v_add_f32 v239, v239, v171\n\t"
      "v_add_f32 v238, v238, v173\n\t"
      "v_add_f32 v239, v239, v175\n\t"
      "v_mul_f32 v238, s95, v238\n\t"
      "v_mul_f32 v239, s95, v239\n\t"
      "s_mov_b32 s68, 0\n\t"
      "s_waitcnt lgkmcnt(0)\n\t"
      "LLOOP_%=:\n\t"
      "v_mfma_f32_16x16x32_bf16 v[104:107], a[0:3], v[40:43], 0\n\t"
      "ds_read_b128 v[248:251], %[vFl]\n\t"
      "s_add_u32 s94, s68, 3\n\t"
      "v_mfma_f32_16x16x32_bf16 v[112:115], a[64:67], v[40:43], 0\n\t"
      "s_lshl_b32 s72, s94, 15\n\t"
      "s_add_u32 s72, s72, s88\n\t"
      "v_mfma_f32_16x16x32_bf16 v[104:107], a[4:7], v[44:47], v[104:107]\n\t"
      "ds_read_b128 v[40:43], v244 offset:16384\n\t"
      "s_lshl_b32 s73, s94, 12\n\t"
      "v_mfma_f32_16x16x32_bf16 v[112:115], a[68:71], v[44:47], v[112:115]\n\t"
      "v_cvt_pk_bf16_f32 v192, v144, v145\n\t"
      "v_cvt_pk_bf16_f32 v193, v148, v149\n\t"
      "v_mfma_f32_16x16x32_bf16 v[104:107], a[8:11], v[48:51], v[104:107]\n\t"
      "ds_read_b128 v[44:47], v244 offset:17408\n\t"
      "v_cvt_pk_bf16_f32 v194, v146, v147\n\t"
      "v_mfma_f32_16x16x32_bf16 v[112:115], a[72:75], v[48:51], v[112:115]\n\t"
      "v_cvt_pk_bf16_f32 v195, v150, v151\n\t"
      "s_add_u32 s73, s73, s89\n\t"
      "v_mfma_f32_16x16x32_bf16 v[104:107], a[12:15], v[52:55], v[104:107]\n\t"
      "ds_read_b128 v[48:51], v244 offset:18432\n\t"
      "s_cmp_gt_i32 s68, 60\n\t"
      "s_cselect_b32 s74, 0, 3\n\t"
      "v_mfma_f32_16x16x32_bf16 v[112:115], a[76:79], v[52:55], v[112:115]\n\t"
      "buffer_store_dwordx4 v[192:195], %[vD], %[rs], s91 offen offset:2304 sc1\n\t"
      "v_mfma_f32_16x16x32_bf16 v[104:107], a[16:19], v[56:59], v[104:107]\n\t"
      "ds_read_b128 v[52:55], v244 offset:19456\n\t"
      "v_sub_f32 v242, v160, v240\n\t"
      "v_mfma_f32_16x16x32_bf16 v[112:115], a[80:83], v[56:59], v[112:115]\n\t"
      "v_sub_f32 v243, v162, v240\n\t"
      "v_fmac_f32 v238, v242, v242\n\t"
      "v_mfma_f32_16x16x32_bf16 v[104:107], a[20:23], v[60:63], v[104:107]\n\t"
      "ds_read_b128 v[56:59], v244 offset:20480\n\t"
      "v_fmac_f32 v239, v243, v243\n\t"
      "v_mfma_f32_16x16x32_bf16 v[112:115], a[84:87], v[60:63], v[112:115]\n\t"
      "v_sub_f32 v242, v164, v240\n\t"
      "v_sub_f32 v243, v166, v240\n\t"
      "v_mfma_f32_16x16x32_bf16 v[104:107], a[24:27], v[64:67], v[104:107]\n\t"
      "ds_read_b128 v[60:63], v244 offset:21504\n\t"
      "v_fmac_f32 v238, v242, v242\n\t"
      "v_mfma_f32_16x16x32_bf16 v[112:115], a[88:91], v[64:67], v[112:115]\n\t"
      "v_fmac_f32 v239, v243, v243\n\t"
      "v_sub_f32 v242, v168, v240\n\t"
      "v_mfma_f32_16x16x32_bf16 v[104:107], a[28:31], v[68:71], v[104:107]\n\t"
      "ds_read_b128 v[64:67], v244 offset:22528\n\t"
      "v_sub_f32 v243, v170, v240\n\t"
      "v_mfma_f32_16x16x32_bf16 v[112:115], a[92:95], v[68:71], v[112:115]\n\t"
      "v_fmac_f32 v238, v242, v242\n\t"
      "v_fmac_f32 v239, v243, v243\n\t"
      "s_waitcnt lgkmcnt(12)\n\t"
      "v_mfma_f32_16x16x32_bf16 v[104:107], a[32:35], v[72:75], v[104:107]\n\t"
      "ds_read_b128 v[68:71], v244 offset:23552\n\t"
      "v_mfma_f32_16x16x32_bf16 v[112:115], a[96:99], v[72:75], v[112:115]\n\t"
      "v_sub_f32 v242, v172, v240\n\t"
      "v_sub_f32 v243, v174, v240\n\t"
      "v_mfma_f32_16x16x32_bf16 v[104:107], a[36:39], v[76:79], v[104:107]\n\t"
      "ds_read_b128 v[72:75], v244 offset:24576\n\t"
      "v_fmac_f32 v238, v242, v242\n\t"
      "v_mfma_f32_16x16x32_bf16 v[112:115], a[100:103], v[76:79], v[112:115]\n\t"
      "v_fmac_f32 v239, v243, v243\n\t"
      "v_add_f32 v236, v238, v239\n\t"
      "v_mfma_f32_16x16x32_bf16 v[104:107], a[40:43], v[80:83], v[104:107]\n\t"
      "ds_read_b128 v[76:79], v244 offset:25600\n\t"
      "v_mov_b32 v241, v236\n\t"
      "v_mfma_f32_16x16x32_bf16 v[112:115], a[104:107], v[80:83], v[112:115]\n\t"
      "v_mul_f32 v240, 0xbd000000, v240\n\t"
      "s_waitcnt lgkmcnt(10)\n\t"
      "v_min_i32 v252, v248, v249\n\t"
      "v_mfma_f32_16x16x32_bf16 v[104:107], a[44:47], v[84:87], v[104:107]\n\t"
      "ds_read_b128 v[80:83], v244 offset:26624\n\t"
      "v_mfma_f32_16x16x32_bf16 v[112:115], a[108:111], v[84:87], v[112:115]\n\t"
      "v_permlane16_swap_b32 v236, v241\n\t"
      "v_add_f32 v236, v236, v241\n\t"
      "v_mfma_f32_16x16x32_bf16 v[104:107], a[48:51], v[88:91], v[104:107]\n\t"
      "ds_read_b128 v[84:87], v244 offset:27648\n\t"
      "v_fma_f32 v236, v236, s93, v253\n\t"
      "v_mfma_f32_16x16x32_bf16 v[112:115], a[112:115], v[88:91], v[112:115]\n\t"
      "v_rsq_f32 v176, v236\n\t"
      "v_mfma_f32_16x16x32_bf16 v[104:107], a[52:55], v[92:95], v[104:107]\n\t"
      "ds_read_b128 v[88:91], v244 offset:28672\n\t"
      "v_mul_f32 v177, v240, v176\n\t"
      "v_mfma_f32_16x16x32_bf16 v[112:115], a[116:119], v[92:95], v[112:115]\n\t"
      "v_mov_b32 v178, v176\n\t"
      "v_mov_b32 v179, v177\n\t"
      "v_mfma_f32_16x16x32_bf16 v[104:107], a[56:59], v[96:99], v[104:107]\n\t"
      "ds_read_b128 v[92:95], v244 offset:29696\n\t"
      "v_permlane32_swap_b32 v176, v178\n\t"
      "v_mfma_f32_16x16x32_bf16 v[112:115], a[120:123], v[96:99], v[112:115]\n\t"
      "v_permlane32_swap_b32 v177, v179\n\t"
      "s_lshl_b32 s90, s68, 15\n\t"
      "v_mfma_f32_16x16x32_bf16 v[104:107], a[60:63], v[100:103], v[104:107]\n\t"
      "ds_read_b128 v[96:99], v244 offset:30720\n\t"
      "s_add_u32 s91, s90, 4096\n\t"
      "v_mfma_f32_16x16x32_bf16 v[112:115], a[124:127], v[100:103], v[112:115]\n\t"
      "v_min3_i32 v252, v252, v250, v251\n\t"
      "s_add_u32 s94, s68, 3\n\t"
      "s_waitcnt lgkmcnt(11)\n\t"
      "v_mfma_f32_16x16x32_bf16 v[108:111], a[0:3], v[40:43], 0\n\t"
      "ds_read_b128 v[100:103], v244 offset:31744\n\t"
      "v_mfma_f32_16x16x32_bf16 v[116:119], a[64:67], v[40:43], 0\n\t"
      "s_add_u32 s94, s94, s76\n\t"
      "v_cmp_lt_i32 vcc, s94, v252\n\t"
      "v_mfma_f32_16x16x32_bf16 v[108:111], a[4:7], v[44:47], v[108:111]\n\t"
      "s_and_b32 s94, s94, 15\n\t"
      "s_lshl_b32 s94, s94, 1\n\t"
      "v_mfma_f32_16x16x32_bf16 v[116:119], a[68:71], v[44:47], v[116:119]\n\t"
      "v_mul_f32 v104, v104, v176\n\t"
      "v_mul_f32 v105, v105, v176\n\t"
      "v_mfma_f32_16x16x32_bf16 v[108:111], a[8:11], v[48:51], v[108:111]\n\t"
      "v_mul_f32 v106, v106, v176\n\t"
      "v_mul_f32 v107, v107, v176\n\t"
      "v_mfma_f32_16x16x32_bf16 v[116:119], a[72:75], v[48:51], v[116:119]\n\t"
      "v_mul_f32 v112, v112, v176\n\t"
      "v_mul_f32 v113, v113, v176\n\t"
      "v_mfma_f32_16x16x32_bf16 v[108:111], a[12:15], v[52:55], v[108:111]\n\t"
      "v_mul_f32 v114, v114, v176\n\t"
      "v_mul_f32 v115, v115, v176\n\t"
      "v_mfma_f32_16x16x32_bf16 v[116:119], a[76:79], v[52:55], v[116:119]\n\t"
      "v_fmac_f32 v104, v152, v177\n\t"
      "v_fmac_f32 v105, v153, v177\n\t"
      "s_waitcnt lgkmcnt(8)\n\t"
      "v_mfma_f32_16x16x32_bf16 v[108:111], a[16:19], v[56:59], v[108:111]\n\t"
      "v_fmac_f32 v106, v154, v177\n\t"
      "v_mfma_f32_16x16x32_bf16 v[116:119], a[80:83], v[56:59], v[116:119]\n\t"
      "v_fmac_f32 v107, v155, v177\n\t"
      "v_fmac_f32 v112, v156, v177\n\t"
      "v_mfma_f32_16x16x32_bf16 v[108:111], a[20:23], v[60:63], v[108:111]\n\t"
      "v_fmac_f32 v113, v157, v177\n\t"
      "v_fmac_f32 v114, v158, v177\n\t"
      "v_mfma_f32_16x16x32_bf16 v[116:119], a[84:87], v[60:63], v[116:119]\n\t"
      "v_fmac_f32 v115, v159, v177\n\t"
      "v_cvt_pk_bf16_f32 v228, v104, v105\n\t"
      "v_mfma_f32_16x16x32_bf16 v[108:111], a[24:27], v[64:67], v[108:111]\n\t"
      "v_cvt_pk_bf16_f32 v229, v106, v107\n\t"
      "v_cvt_pk_bf16_f32 v230, v112, v113\n\t"
      "v_mfma_f32_16x16x32_bf16 v[116:119], a[88:91], v[64:67], v[116:119]\n\t"
      "v_cvt_pk_bf16_f32 v231, v114, v115\n\t"
      "ds_write_b128 %[vVx], v[228:231] offset:0\n\t"
      "v_mfma_f32_16x16x32_bf16 v[108:111], a[28:31], v[68:71], v[108:111]\n\t"
      "s_lshr_b32 s94, vcc_lo, s94\n\t"
      "s_andn2_b32 s94, s74, s94\n\t"
      "s_cselect_b32 s74, 0, 4\n\t"
      "s_cselect_b32 s72, 0x40000000, s72\n\t"
      "s_cselect_b32 s73, 0x40000000, s73\n\t"
      "v_mfma_f32_16x16x32_bf16 v[116:119], a[92:95], v[68:71], v[116:119]\n\t"
      "s_waitcnt lgkmcnt(5)\n\t"
      "v_mfma_f32_16x16x32_bf16 v[108:111], a[32:35], v[72:75], v[108:111]\n\t"
      "v_mfma_f32_16x16x32_bf16 v[116:119], a[96:99], v[72:75], v[116:119]\n\t"
      "s_waitcnt lgkmcnt(0)\n\t"
      "s_cmp_lt_i32 s68, 10\n\t"
      "s_cbranch_scc1 LPS_%=\n\t"
      "s_waitcnt vmcnt(19)\n\t"
      "s_add_u32 s69, s68, s52\n\t"
      "LPR_%=:\n\t"
      "v_mov_b32 v246, s69\n\t"
      "s_bitcmp1_b32 s87, 0\n\t"
      "buffer_store_dword v246, %[vFo], %[rc], s78 offen sc1\n\t"
      "s_cbranch_scc0 LBLK_%=\n\t"
      "LBR_%=:\n\t"
      "s_barrier\n\t"
      "v_add_u32 v244, s85, %[vLd]\n\t"
      "s_lshr_b32 s69, s85, 3\n\t"
      "s_add_u32 s69, s69, 98304\n\t"
      "v_add_u32 v245, s69, %[vLd]\n\t"
      "v_mfma_f32_16x16x32_bf16 v[108:111], a[36:39], v[76:79], v[108:111]\n\t"
      "ds_read_b128 v[196:199], %[vVr] offset:0\n\t"
      "ds_read_b128 v[200:203], %[vVr] offset:1024\n\t"
      "v_mfma_f32_16x16x32_bf16 v[116:119], a[100:103], v[76:79], v[116:119]\n\t"
      "ds_read_b128 v[204:207], %[vVr] offset:2048\n\t"
      "ds_read_b128 v[208:211], %[vVr] offset:3072\n\t"
      "v_mfma_f32_16x16x32_bf16 v[108:111], a[40:43], v[80:83], v[108:111]\n\t"
      "s_or_b32 s87, s87, s74\n\t"
      "s_lshr_b32 s87, s87, 1\n\t"
      "v_mfma_f32_16x16x32_bf16 v[116:119], a[104:107], v[80:83], v[116:119]\n\t"
      "s_add_u32 m0, s97, 12288\n\t"
      "s_mov_b32 s94, s84\n\t"
      "v_mfma_f32_16x16x32_bf16 v[108:111], a[44:47], v[84:87], v[108:111]\n\t"
      "buffer_load_dwordx4 %[vX2], %[rc], s78 offen sc1 lds\n\t"
      "v_mfma_f32_16x16x32_bf16 v[116:119], a[108:111], v[84:87], v[116:119]\n\t"
      "s_add_u32 m0, s94, s96\n\t"
      "s_add_u32 s98, s72, 4096\n\t"
      "v_mfma_f32_16x16x32_bf16 v[108:111], a[48:51], v[88:91], v[108:111]\n\t"
      "buffer_load_dwordx4 %[vL], %[rs], s72 offen offset:0 sc1 lds\n\t"
      "v_mfma_f32_16x16x32_bf16 v[116:119], a[112:115], v[88:91], v[116:119]\n\t"
      "ds_read_b128 v[160:163], v245 offset:0\n\t"
      "ds_read_b128 v[164:167], v245 offset:1024\n\t"
      "v_mfma_f32_16x16x32_bf16 v[108:111], a[52:55], v[92:95], v[108:111]\n\t"
      "ds_read_b128 v[168:171], v245 offset:2048\n\t"
      "ds_read_b128 v[172:175], v245 offset:3072\n\t"
      "v_mfma_f32_16x16x32_bf16 v[116:119], a[116:119], v[92:95], v[116:119]\n\t"
      "ds_read_b128 v[40:43], v244 offset:0\n\t"
      "ds_read_b128 v[44:47], v244 offset:1024\n\t"
      "v_mfma_f32_16x16x32_bf16 v[108:111], a[56:59], v[96:99], v[108:111]\n\t"
      "buffer_load_dwordx4 %[vL], %[rs], s72 offen offset:1024 sc1 lds\n\t"
      "v_mfma_f32_16x16x32_bf16 v[116:119], a[120:123], v[96:99], v[116:119]\n\t"
      "ds_read_b128 v[48:51], v244 offset:2048\n\t"
      "ds_read_b128 v[52:55], v244 offset:3072\n\t"
      "v_mfma_f32_16x16x32_bf16 v[108:111], a[60:63], v[100:103], v[108:111]\n\t"
      "ds_read_b128 v[56:59], v244 offset:4096\n\t"
      "ds_read_b128 v[60:63], v244 offset:5120\n\t"
      "v_mfma_f32_16x16x32_bf16 v[116:119], a[124:127], v[100:103], v[116:119]\n\t"
      "ds_read_b128 v[64:67], v244 offset:6144\n\t"
      "ds_read_b128 v[68:71], v244 offset:7168\n\t"
      "s_waitcnt lgkmcnt(14)\n\t"
      "v_mfma_f32_16x16x32_bf16 v[120:123], v[196:199], a[128:131], 0\n\t"
      "ds_read_b128 v[72:75], v244 offset:8192\n\t"
      "v_mfma_f32_16x16x32_bf16 v[124:127], v[196:199], a[144:147], 0\n\t"
      "buffer_load_dwordx4 %[vL], %[rs], s72 offen offset:2048 sc1 lds\n\t"
      "v_mfma_f32_16x16x32_bf16 v[120:123], v[200:203], a[132:135], v[120:123]\n\t"
      "ds_read_b128 v[76:79], v244 offset:9216\n\t"
      "ds_read_b128 v[80:83], v244 offset:10240\n\t"
      "v_mfma_f32_16x16x32_bf16 v[124:127], v[200:203], a[148:151], v[124:127]\n\t"
      "v_mul_f32 v108, v108, v178\n\t"
      "v_mul_f32 v109, v109, v178\n\t"
      "s_waitcnt lgkmcnt(15)\n\t"
      "v_mfma_f32_16x16x32_bf16 v[120:123], v[204:207], a[136:139], v[120:123]\n\t"
      "v_mul_f32 v110, v110, v178\n\t"
      "v_mfma_f32_16x16x32_bf16 v[124:127], v[204:207], a[152:155], v[124:127]\n\t"
      "v_mul_f32 v111, v111, v178\n\t"
      "v_mul_f32 v116, v116, v178\n\t"
      "v_mfma_f32_16x16x32_bf16 v[120:123], v[208:211], a[140:143], v[120:123]\n\t"
      "v_mul_f32 v117, v117, v178\n\t"
      "v_mul_f32 v118, v118, v178\n\t"
      "v_mfma_f32_16x16x32_bf16 v[124:127], v[208:211], a[156:159], v[124:127]\n\t"
      "v_mul_f32 v119, v119, v178\n\t"
      "v_fmac_f32 v108, v152, v179\n\t"
      "v_mfma_f32_16x16x32_bf16 v[128:131], v[196:199], a[160:163], 0\n\t"
      "v_fmac_f32 v109, v153, v179\n\t"
      "v_fmac_f32 v110, v154, v179\n\t"
      "v_mfma_f32_16x16x32_bf16 v[132:135], v[196:199], a[176:179], 0\n\t"
      "v_fmac_f32 v111, v155, v179\n\t"
      "v_fmac_f32 v116, v156, v179\n\t"
      "v_mfma_f32_16x16x32_bf16 v[128:131], v[200:203], a[164:167], v[128:131]\n\t"
      "v_fmac_f32 v117, v157, v179\n\t"
      "v_fmac_f32 v118, v158, v179\n\t"
      "v_mfma_f32_16x16x32_bf16 v[132:135], v[200:203], a[180:183], v[132:135]\n\t"
      "v_fmac_f32 v119, v159, v179\n\t"
      "v_cvt_pk_bf16_f32 v232, v108, v109\n\t"
      "v_mfma_f32_16x16x32_bf16 v[128:131], v[204:207], a[168:171], v[128:131]\n\t"
      "v_cvt_pk_bf16_f32 v233, v110, v111\n\t"
      "v_cvt_pk_bf16_f32 v234, v116, v117\n\t"
      "v_mfma_f32_16x16x32_bf16 v[132:135], v[204:207], a[184:187], v[132:135]\n\t"
      "v_cvt_pk_bf16_f32 v235, v118, v119\n\t"
      "ds_write_b128 %[vVx], v[232:235] offset:4096\n\t"
      "v_mfma_f32_16x16x32_bf16 v[128:131], v[208:211], a[172:175], v[128:131]\n\t"
      "v_cvt_pk_bf16_f32 v180, v120, v121\n\t"
      "v_cvt_pk_bf16_f32 v181, v124, v125\n\t"
      "v_mfma_f32_16x16x32_bf16 v[132:135], v[208:211], a[188:191], v[132:135]\n\t"
      "v_cvt_pk_bf16_f32 v182, v122, v123\n\t"
      "v_cvt_pk_bf16_f32 v183, v126, v127\n\t"
      "v_mfma_f32_16x16x32_bf16 v[136:139], v[196:199], a[192:195], 0\n\t"
      "buffer_store_dwordx4 v[180:183], %[vD], %[rs], s90 offen offset:0 sc1\n\t"
      "v_mfma_f32_16x16x32_bf16 v[140:143], v[196:199], a[208:211], 0\n\t"
      "buffer_load_dwordx4 %[vL], %[rs], s72 offen offset:3072 sc1 lds\n\t"
      "v_mfma_f32_16x16x32_bf16 v[136:139], v[200:203], a[196:199], v[136:139]\n\t"
      "s_add_u32 m0, m0, 4096\n\t"
      "s_lshr_b32 s99, s94, 3\n\t"
      "v_mfma_f32_16x16x32_bf16 v[140:143], v[200:203], a[212:215], v[140:143]\n\t"
      "v_cvt_pk_bf16_f32 v184, v128, v129\n\t"
      "v_cvt_pk_bf16_f32 v185, v132, v133\n\t"
      "s_waitcnt lgkmcnt(0)\n\t"
      "s_barrier\n\t"
      "v_mfma_f32_16x16x32_bf16 v[136:139], v[204:207], a[200:203], v[136:139]\n\t"
      "ds_read_b128 v[212:215], %[vVr] offset:4096\n\t"
      "ds_read_b128 v[216:219], %[vVr] offset:5120\n\t"
      "v_mfma_f32_16x16x32_bf16 v[140:143], v[204:207], a[216:219], v[140:143]\n\t"
      "ds_read_b128 v[220:223], %[vVr] offset:6144\n\t"
      "ds_read_b128 v[224:227], %[vVr] offset:7168\n\t"
      "v_mfma_f32_16x16x32_bf16 v[136:139], v[208:211], a[204:207], v[136:139]\n\t"
      "v_cvt_pk_bf16_f32 v186, v130, v131\n\t"
      "v_cvt_pk_bf16_f32 v187, v134, v135\n\t"
      "v_mfma_f32_16x16x32_bf16 v[140:143], v[208:211], a[220:223], v[140:143]\n\t"
      "buffer_store_dwordx4 v[184:187], %[vD], %[rs], s90 offen offset:2048 sc1\n\t"
      "v_mfma_f32_16x16x32_bf16 v[144:147], v[196:199], a[224:227], 0\n\t"
      "buffer_load_dwordx4 %[vL], %[rs], s98 offen offset:0 sc1 lds\n\t"
      "v_mfma_f32_16x16x32_bf16 v[148:151], v[196:199], a[240:243], 0\n\t"
      "ds_read_b128 v[84:87], v244 offset:11264\n\t"
      "ds_read_b128 v[88:91], v244 offset:12288\n\t"
      "v_mfma_f32_16x16x32_bf16 v[144:147], v[200:203], a[228:231], v[144:147]\n\t"
      "ds_read_b128 v[92:95], v244 offset:13312\n\t"
      "ds_read_b128 v[96:99], v244 offset:14336\n\t"
      "v_mfma_f32_16x16x32_bf16 v[148:151], v[200:203], a[244:247], v[148:151]\n\t"
      "v_cvt_pk_bf16_f32 v188, v136, v137\n\t"
      "v_cvt_pk_bf16_f32 v189, v140, v141\n\t"
      "v_mfma_f32_16x16x32_bf16 v[144:147], v[204:207], a[232:235], v[144:147]\n\t"
      "v_cvt_pk_bf16_f32 v190, v138, v139\n\t"
      "v_cvt_pk_bf16_f32 v191, v142, v143\n\t"
      "v_mfma_f32_16x16x32_bf16 v[148:151], v[204:207], a[248:251], v[148:151]\n\t"
      "buffer_store_dwordx4 v[188:191], %[vD], %[rs], s91 offen offset:0 sc1\n\t"
      "v_mfma_f32_16x16x32_bf16 v[144:147], v[208:211], a[236:239], v[144:147]\n\t"
      "buffer_load_dwordx4 %[vL], %[rs], s98 offen offset:1024 sc1 lds\n\t"
      "v_mfma_f32_16x16x32_bf16 v[148:151], v[208:211], a[252:255], v[148:151]\n\t"
      "ds_read_b128 v[100:103], v244 offset:15360\n\t"
      "v_add_f32 v236, v160, v162\n\t"
      "s_waitcnt lgkmcnt(7)\n\t"
      "v_mfma_f32_16x16x32_bf16 v[120:123], v[212:215], a[128:131], 0\n\t"
      "v_add_f32 v237, v164, v166\n\t"
      "v_mfma_f32_16x16x32_bf16 v[124:127], v[212:215], a[144:147], 0\n\t"
      "v_add_f32 v236, v236, v168\n\t"
      "v_add_f32 v237, v237, v170\n\t"
      "v_mfma_f32_16x16x32_bf16 v[120:123], v[216:219], a[132:135], v[120:123]\n\t"
      "buffer_load_dwordx4 %[vL], %[rs], s98 offen offset:2048 sc1 lds\n\t"
      "v_mfma_f32_16x16x32_bf16 v[124:127], v[216:219], a[148:151], v[124:127]\n\t"
      "v_cvt_pk_bf16_f32 v192, v144, v145\n\t"
      "v_cvt_pk_bf16_f32 v193, v148, v149\n\t"
      "s_waitcnt lgkmcnt(5)\n\t"
      "v_mfma_f32_16x16x32_bf16 v[120:123], v[220:223], a[136:139], v[120:123]\n\t"
      "v_cvt_pk_bf16_f32 v194, v146, v147\n\t"
      "v_mfma_f32_16x16x32_bf16 v[124:127], v[220:223], a[152:155], v[124:127]\n\t"
      "v_cvt_pk_bf16_f32 v195, v150, v151\n\t"
      "v_add_f32 v236, v236, v172\n\t"
      "v_mfma_f32_16x16x32_bf16 v[120:123], v[224:227], a[140:143], v[120:123]\n\t"
      "buffer_store_dwordx4 v[192:195], %[vD], %[rs], s91 offen offset:2048 sc1\n\t"
      "v_mfma_f32_16x16x32_bf16 v[124:127], v[224:227], a[156:159], v[124:127]\n\t"
      "buffer_load_dwordx4 %[vL], %[rs], s98 offen offset:3072 sc1 lds\n\t"
      "v_mfma_f32_16x16x32_bf16 v[128:131], v[212:215], a[160:163], 0\n\t"
      "s_add_u32 m0, s99, s97\n\t"
      "s_nop 0\n\t"
      "v_mfma_f32_16x16x32_bf16 v[132:135], v[212:215], a[176:179], 0\n\t"
      "v_add_f32 v237, v237, v174\n\t"
      "v_add_f32 v236, v236, v237\n\t"
      "v_mfma_f32_16x16x32_bf16 v[128:131], v[216:219], a[164:167], v[128:131]\n\t"
      "v_mov_b32 v241, v236\n\t"
      "v_add_f32 v238, v161, v163\n\t"
      "v_mfma_f32_16x16x32_bf16 v[132:135], v[216:219], a[180:183], v[132:135]\n\t"
      "v_cvt_pk_bf16_f32 v180, v120, v121\n\t"
      "v_cvt_pk_bf16_f32 v181, v124, v125\n\t"
      "v_mfma_f32_16x16x32_bf16 v[128:131], v[220:223], a[168:171], v[128:131]\n\t"
      "v_cvt_pk_bf16_f32 v182, v122, v123\n\t"
      "v_cvt_pk_bf16_f32 v183, v126, v127\n\t"
      "v_mfma_f32_16x16x32_bf16 v[132:135], v[220:223], a[184:187], v[132:135]\n\t"
      "buffer_store_dwordx4 v[180:183], %[vD], %[rs], s90 offen offset:256 sc1\n\t"
      "v_mfma_f32_16x16x32_bf16 v[128:131], v[224:227], a[172:175], v[128:131]\n\t"
      "buffer_load_dwordx4 %[vL], %[rs], s73 offen offset:0 sc1 lds\n\t"
      "v_mfma_f32_16x16x32_bf16 v[132:135], v[224:227], a[188:191], v[132:135]\n\t"
      "s_mov_b32 s84, s85\n\t"
      "s_mov_b32 s85, s86\n\t"
      "v_mfma_f32_16x16x32_bf16 v[136:139], v[212:215], a[192:195], 0\n\t"
      "s_mov_b32 s86, s94\n\t"
      "v_add_f32 v239, v165, v167\n\t"
      "v_mfma_f32_16x16x32_bf16 v[140:143], v[212:215], a[208:211], 0\n\t"
      "v_permlane16_swap_b32 v236, v241\n\t"
      "v_add_f32 v240, v236, v241\n\t"
      "v_mfma_f32_16x16x32_bf16 v[136:139], v[216:219], a[196:199], v[136:139]\n\t"
      "v_mul_f32 v240, s92, v240\n\t"
      "v_add_f32 v238, v238, v169\n\t"
      "v_mfma_f32_16x16x32_bf16 v[140:143], v[216:219], a[212:215], v[140:143]\n\t"
      "v_cvt_pk_bf16_f32 v184, v128, v129\n\t"
      "v_cvt_pk_bf16_f32 v185, v132, v133\n\t"
      "v_mfma_f32_16x16x32_bf16 v[136:139], v[220:223], a[200:203], v[136:139]\n\t"
      "v_cvt_pk_bf16_f32 v186, v130, v131\n\t"
      "v_cvt_pk_bf16_f32 v187, v134, v135\n\t"
      "v_mfma_f32_16x16x32_bf16 v[140:143], v[220:223], a[216:219], v[140:143]\n\t"
      "buffer_store_dwordx4 v[184:187], %[vD], %[rs], s90 offen offset:2304 sc1\n\t"
      "v_mfma_f32_16x16x32_bf16 v[136:139], v[224:227], a[204:207], v[136:139]\n\t"
      "v_add_f32 v239, v239, v171\n\t"
      "v_add_f32 v238, v238, v173\n\t"
      "v_mfma_f32_16x16x32_bf16 v[140:143], v[224:227], a[220:223], v[140:143]\n\t"
      "v_add_f32 v239, v239, v175\n\t"
      "v_mul_f32 v238, s95, v238\n\t"
      "v_mfma_f32_16x16x32_bf16 v[144:147], v[212:215], a[224:227], 0\n\t"
      "v_mul_f32 v239, s95, v239\n\t"
      "v_mfma_f32_16x16x32_bf16 v[148:151], v[212:215], a[240:243], 0\n\t"
      "v_mfma_f32_16x16x32_bf16 v[144:147], v[216:219], a[228:231], v[144:147]\n\t"
      "v_mfma_f32_16x16x32_bf16 v[148:151], v[216:219], a[244:247], v[148:151]\n\t"
      "v_cvt_pk_bf16_f32 v188, v136, v137\n\t"
      "v_cvt_pk_bf16_f32 v189, v140, v141\n\t"
      "v_mfma_f32_16x16x32_bf16 v[144:147], v[220:223], a[232:235], v[144:147]\n\t"
      "v_cvt_pk_bf16_f32 v190, v138, v139\n\t"
      "v_cvt_pk_bf16_f32 v191, v142, v143\n\t"
      "v_mfma_f32_16x16x32_bf16 v[148:151], v[220:223], a[248:251], v[148:151]\n\t"
      "buffer_store_dwordx4 v[188:191], %[vD], %[rs], s91 offen offset:256 sc1\n\t"
      "v_mfma_f32_16x16x32_bf16 v[144:147], v[224:227], a[236:239], v[144:147]\n\t"
      "v_mfma_f32_16x16x32_bf16 v[148:151], v[224:227], a[252:255], v[148:151]\n\t"
      "s_add_u32 s68, s68, 1\n\t"
      "s_cmp_lt_i32 s68, 64\n\t"
      "s_cbranch_scc1 LLOOP_%=\n\t"
      "s_nop 7\n\t"
      "v_cvt_pk_bf16_f32 v192, v144, v145\n\t"
      "v_cvt_pk_bf16_f32 v193, v148, v149\n\t"
      "v_cvt_pk_bf16_f32 v194, v146, v147\n\t"
      "v_cvt_pk_bf16_f32 v195, v150, v151\n\t"
      "buffer_store_dwordx4 v[192:195], %[vD], %[rs], s91 offen offset:2304 sc1\n\t"
      "s_add_u32 s69, s76, 64\n\t"
      "v_mov_b32 v246, s69\n\t"
      "s_waitcnt vmcnt(0) lgkmcnt(0)\n\t"
      "buffer_store_dword v246, %[vFo], %[rc], s78 offen sc1\n\t"
      "s_barrier\n\t"
      "s_cmp_eq_u32 s55, 1\n\t"
      "s_cbranch_scc1 LDONE_%=\n\t"
      "s_mov_b32 s55, 1\n\t"
      "s_branch LLAYER_%=\n\t"
      "LDONE_%=:\n\t"
      "s_mov_b32 %[st], 0\n\t"
      "s_branch LEND_%=\n\t"
      "LSC_%=:\n\t"
      "buffer_load_dwordx4 v[248:251], %[vX2], %[rc], s78 offen sc1\n\t"
      "s_waitcnt vmcnt(0)\n\t"
      "s_bitcmp1_b32 s87, 0\n\t"
      "s_cbranch_scc1 LSD0_%=\n\t"
      "v_min_i32 v252, v248, v249\n\t"
      "v_min3_i32 v252, v252, v250, v251\n\t"
      "s_add_u32 s94, s68, 2\n\t"
      "s_add_u32 s94, s94, s76\n\t"
      "v_cmp_lt_i32 vcc, s94, v252\n\t"
      "s_and_b32 s94, s94, 15\n\t"
      "s_lshl_b32 s94, s94, 1\n\t"
      "s_lshr_b32 s94, vcc_lo, s94\n\t"
      "s_andn2_b32 s94, 3, s94\n\t"
      "s_cbranch_scc1 LSD0_%=\n\t"
      "s_or_b32 s87, s87, 1\n\t"
      "s_add_u32 s94, s68, 2\n\t"
      "s_lshl_b32 s79, s94, 15\n\t"
      "s_add_u32 s79, s79, s88\n\t"
      "s_lshl_b32 s80, s94, 12\n\t"
      "s_add_u32 s80, s80, s89\n\t"
      "s_add_u32 m0, s85, s96\n\t"
      "s_add_u32 s98, s79, 4096\n\t"
      "buffer_load_dwordx4 %[vL], %[rs], s79 offen offset:0 sc1 lds\n\t"
      "buffer_load_dwordx4 %[vL], %[rs], s79 offen offset:1024 sc1 lds\n\t"
      "buffer_load_dwordx4 %[vL], %[rs], s79 offen offset:2048 sc1 lds\n\t"
      "buffer_load_dwordx4 %[vL], %[rs], s79 offen offset:3072 sc1 lds\n\t"
      "s_add_u32 m0, m0, 4096\n\t"
      "s_lshr_b32 s99, s85, 3\n\t"
      "buffer_load_dwordx4 %[vL], %[rs], s98 offen offset:0 sc1 lds\n\t"
      "buffer_load_dwordx4 %[vL], %[rs], s98 offen offset:1024 sc1 lds\n\t"
      "buffer_load_dwordx4 %[vL], %[rs], s98 offen offset:2048 sc1 lds\n\t"
      "buffer_load_dwordx4 %[vL], %[rs], s98 offen offset:3072 sc1 lds\n\t"
      "s_add_u32 m0, s99, s97\n\t"
      "s_nop 0\n\t"
      "buffer_load_dwordx4 %[vL], %[rs], s80 offen offset:0 sc1 lds\n\t"
      "LSD0_%=:\n\t"
      "s_bitcmp1_b32 s87, 1\n\t"
      "s_cbranch_scc1 LSD1_%=\n\t"
      "v_min_i32 v252, v248, v249\n\t"
      "v_min3_i32 v252, v252, v250, v251\n\t"
      "s_add_u32 s94, s68, 3\n\t"
      "s_add_u32 s94, s94, s76\n\t"
      "v_cmp_lt_i32 vcc, s94, v252\n\t"
      "s_and_b32 s94, s94, 15\n\t"
      "s_lshl_b32 s94, s94, 1\n\t"
      "s_lshr_b32 s94, vcc_lo, s94\n\t"
      "s_andn2_b32 s94, 3, s94\n\t"
      "s_cbranch_scc1 LSD1_%=\n\t"
      "s_or_b32 s87, s87, 2\n\t"
      "s_add_u32 s94, s68, 3\n\t"
      "s_lshl_b32 s79, s94, 15\n\t"
      "s_add_u32 s79, s79, s88\n\t"
      "s_lshl_b32 s80, s94, 12\n\t"
      "s_add_u32 s80, s80, s89\n\t"
      "s_add_u32 m0, s86, s96\n\t"
      "s_add_u32 s98, s79, 4096\n\t"
      "buffer_load_dwordx4 %[vL], %[rs], s79 offen offset:0 sc1 lds\n\t"
      "buffer_load_dwordx4 %[vL], %[rs], s79 offen offset:1024 sc1 lds\n\t"
      "buffer_load_dwordx4 %[vL], %[rs], s79 offen offset:2048 sc1 lds\n\t"
      "buffer_load_dwordx4 %[vL], %[rs], s79 offen offset:3072 sc1 lds\n\t"
      "s_add_u32 m0, m0, 4096\n\t"
      "s_lshr_b32 s99, s86, 3\n\t"
      "buffer_load_dwordx4 %[vL], %[rs], s98 offen offset:0 sc1 lds\n\t"
      "buffer_load_dwordx4 %[vL], %[rs], s98 offen offset:1024 sc1 lds\n\t"
      "buffer_load_dwordx4 %[vL], %[rs], s98 offen offset:2048 sc1 lds\n\t"
      "buffer_load_dwordx4 %[vL], %[rs], s98 offen offset:3072 sc1 lds\n\t"
      "s_add_u32 m0, s99, s97\n\t"
      "s_nop 0\n\t"
      "buffer_load_dwordx4 %[vL], %[rs], s80 offen offset:0 sc1 lds\n\t"
      "LSD1_%=:\n\t"
      "s_branch LSR_%=\n\t"
      "LPS_%=:\n\t"
      "s_waitcnt vmcnt(0)\n\t"
      "s_add_u32 s69, s68, s76\n\t"
      "s_branch LPR_%=\n\t"
      "LBLK_%=:\n\t"
      "LBLs_%=:\n\t"
      "buffer_load_dwordx4 v[248:251], %[vX2], %[rc], s78 offen sc1\n\t"
      "s_add_u32 s71, s71, 1\n\t"
      "s_cmp_gt_u32 s71, 131072\n\t"
      "s_cbranch_scc1 LFAIL_%=\n\t"
      "s_waitcnt vmcnt(0)\n\t"
      "v_min_i32 v252, v248, v249\n\t"
      "v_min3_i32 v252, v252, v250, v251\n\t"
      "s_add_u32 s94, s68, 1\n\t"
      "s_add_u32 s94, s94, s76\n\t"
      "v_cmp_lt_i32 vcc, s94, v252\n\t"
      "s_and_b32 s94, s94, 15\n\t"
      "s_lshl_b32 s94, s94, 1\n\t"
      "s_lshr_b32 s94, vcc_lo, s94\n\t"
      "s_andn2_b32 s94, 3, s94\n\t"
      "s_sleep 1\n\t"
      "s_cbranch_scc1 LBLs_%=\n\t"
      "LBGs_%=:\n\t"
      "s_add_u32 s94, s68, 1\n\t"
      "s_lshl_b32 s79, s94, 15\n\t"
      "s_add_u32 s79, s79, s88\n\t"
      "s_lshl_b32 s80, s94, 12\n\t"
      "s_add_u32 s80, s80, s89\n\t"
      "s_add_u32 m0, s85, s96\n\t"
      "s_add_u32 s98, s79, 4096\n\t"
      "buffer_load_dwordx4 %[vL], %[rs], s79 offen offset:0 sc1 lds\n\t"
      "buffer_load_dwordx4 %[vL], %[rs], s79 offen offset:1024 sc1 lds\n\t"
      "buffer_load_dwordx4 %[vL], %[rs], s79 offen offset:2048 sc1 lds\n\t"
      "buffer_load_dwordx4 %[vL], %[rs], s79 offen offset:3072 sc1 lds\n\t"
      "s_add_u32 m0, m0, 4096\n\t"
      "s_lshr_b32 s99, s85, 3\n\t"
      "buffer_load_dwordx4 %[vL], %[rs], s98 offen offset:0 sc1 lds\n\t"
      "buffer_load_dwordx4 %[vL], %[rs], s98 offen offset:1024 sc1 lds\n\t"
      "buffer_load_dwordx4 %[vL], %[rs], s98 offen offset:2048 sc1 lds\n\t"
      "buffer_load_dwordx4 %[vL], %[rs], s98 offen offset:3072 sc1 lds\n\t"
      "s_add_u32 m0, s99, s97\n\t"
      "s_nop 0\n\t"
      "buffer_load_dwordx4 %[vL], %[rs], s80 offen offset:0 sc1 lds\n\t"
      "s_bitcmp1_b32 s87, 1\n\t"
      "s_cbranch_scc1 LBD_%=\n\t"
      "v_min_i32 v252, v248, v249\n\t"
      "v_min3_i32 v252, v252, v250, v251\n\t"
      "s_add_u32 s94, s68, 2\n\t"
      "s_add_u32 s94, s94, s76\n\t"
      "v_cmp_lt_i32 vcc, s94, v252\n\t"
      "s_and_b32 s94, s94, 15\n\t"
      "s_lshl_b32 s94, s94, 1\n\t"
      "s_lshr_b32 s94, vcc_lo, s94\n\t"
      "s_andn2_b32 s94, 3, s94\n\t"
      "s_cbranch_scc1 LBD_%=\n\t"
      "s_or_b32 s87, s87, 2\n\t"
      "s_add_u32 s94, s68, 2\n\t"
      "s_lshl_b32 s79, s94, 15\n\t"
      "s_add_u32 s79, s79, s88\n\t"
      "s_lshl_b32 s80, s94, 12\n\t"
      "s_add_u32 s80, s80, s89\n\t"
      "s_add_u32 m0, s86, s96\n\t"
      "s_add_u32 s98, s79, 4096\n\t"
      "buffer_load_dwordx4 %[vL], %[rs], s79 offen offset:0 sc1 lds\n\t"
      "buffer_load_dwordx4 %[vL], %[rs], s79 offen offset:1024 sc1 lds\n\t"
      "buffer_load_dwordx4 %[vL], %[rs], s79 offen offset:2048 sc1 lds\n\t"
      "buffer_load_dwordx4 %[vL], %[rs], s79 offen offset:3072 sc1 lds\n\t"
      "s_add_u32 m0, m0, 4096\n\t"
      "s_lshr_b32 s99, s86, 3\n\t"
      "buffer_load_dwordx4 %[vL], %[rs], s98 offen offset:0 sc1 lds\n\t"
      "buffer_load_dwordx4 %[vL], %[rs], s98 offen offset:1024 sc1 lds\n\t"
      "buffer_load_dwordx4 %[vL], %[rs], s98 offen offset:2048 sc1 lds\n\t"
      "buffer_load_dwordx4 %[vL], %[rs], s98 offen offset:3072 sc1 lds\n\t"
      "s_add_u32 m0, s99, s97\n\t"
      "s_nop 0\n\t"
      "buffer_load_dwordx4 %[vL], %[rs], s80 offen offset:0 sc1 lds\n\t"
      "LBD_%=:\n\t"
      "s_waitcnt vmcnt(0)\n\t"
      "s_branch LBR_%=\n\t"
      "LFAIL_%=:\n\t"
      "s_mov_b32 %[st], 1\n\t"
      "LEND_%=:\n\t"
      "s_waitcnt vmcnt(0) lgkmcnt(0)\n\t"
      : [st] "=s"(st)
      : PR_OPS, [vFo] "v"(lane == 0 ? L_PR + (32 + h) * 128 + w * 4 : OOB),
        [vD] "v"(OFF_UT + h * HU + w * 8192 + (g >> 1) * 1024 + ((g & 1) * 32 + n) * 16),
        [vTr] "v"(tr + (sl ^ (2 * w)) * 32), [vG2] "v"(g * 1024 + w * 256 + n * 16),
        [vSw2] "v"(w * 32768 + g * 256 + (n & 1) * 16 + ((n >> 1) ^ g) * 32), [vTr2] "v"(w * 32768 + tr + sl * 32),
        [vVx] "v"(ldsb + 114688 + w * 1024 + lane * 16), [vVr] "v"(ldsb + 114688 + lane * 16),
        [rW] "s"(mkrs(Wv + h * 128, (4 << 20) - h * 256)), [rW2] "s"(mkrs(Wo + h * 65536, 131072)) TSP_OPS(96 + h)
      :
        "v40", "v41", "v42", "v43", "v44", "v45", "v46", "v47", "v48", "v49", "v50", "v51", "v52", "v53", "v54", "v55", "v56", "v57", "v58", "v59", "v60", "v61", "v62", "v63",
        "v64", "v65", "v66", "v67", "v68", "v69", "v70", "v71", "v72", "v73", "v74", "v75", "v76", "v77", "v78", "v79", "v80", "v81", "v82", "v83", "v84", "v85", "v86", "v87",
        "v88", "v89", "v90", "v91", "v92", "v93", "v94", "v95", "v96", "v97", "v98", "v99", "v100", "v101", "v102", "v103", "v104", "v105", "v106", "v107", "v108", "v109", "v110", "v111",
        "v112", "v113", "v114", "v115", "v116", "v117", "v118", "v119", "v120", "v121", "v122", "v123", "v124", "v125", "v126", "v127", "v128", "v129", "v130", "v131", "v132", "v133", "v134", "v135",
        "v136", "v137", "v138", "v139", "v140", "v141", "v142", "v143", "v144", "v145", "v146", "v147", "v148", "v149", "v150", "v151", "v152", "v153", "v154", "v155", "v156", "v157", "v158", "v159",
        "v160", "v161", "v162", "v163", "v164", "v165", "v166", "v167", "v168", "v169", "v170", "v171", "v172", "v173", "v174", "v175", "v176", "v177", "v178", "v179", "v180", "v181", "v182", "v183",
        "v184", "v185", "v186", "v187", "v188", "v189", "v190", "v191", "v192", "v193", "v194", "v195", "v196", "v197", "v198", "v199", "v200", "v201", "v202", "v203", "v204", "v205", "v206", "v207",
        "v208", "v209", "v210", "v211", "v212", "v213", "v214", "v215", "v216", "v217", "v218", "v219", "v220", "v221", "v222", "v223", "v224", "v225", "v226", "v227", "v228", "v229", "v230", "v231",
        "v232", "v233", "v234", "v235", "v236", "v237", "v238", "v239", "v240", "v241", "v242", "v243", "v244", "v245", "v246", "v247", "v248", "v249", "v250", "v251", "v252", "v253", "v254", "v255",
        "a0", "a1", "a2", "a3", "a4", "a5", "a6", "a7", "a8", "a9", "a10", "a11", "a12", "a13", "a14", "a15", "a16", "a17", "a18", "a19", "a20", "a21", "a22", "a23",
        "a24", "a25", "a26", "a27", "a28", "a29", "a30", "a31", "a32", "a33", "a34", "a35", "a36", "a37", "a38", "a39", "a40", "a41", "a42", "a43", "a44", "a45", "a46", "a47",
        "a48", "a49", "a50", "a51", "a52", "a53", "a54", "a55", "a56", "a57", "a58", "a59", "a60", "a61", "a62", "a63", "a64", "a65", "a66", "a67", "a68", "a69", "a70", "a71",
        "a72", "a73", "a74", "a75", "a76", "a77", "a78", "a79", "a80", "a81", "a82", "a83", "a84", "a85", "a86", "a87", "a88", "a89", "a90", "a91", "a92", "a93", "a94", "a95",
        "a96", "a97", "a98", "a99", "a100", "a101", "a102", "a103", "a104", "a105", "a106", "a107", "a108", "a109", "a110", "a111", "a112", "a113", "a114", "a115", "a116", "a117", "a118", "a119",
        "a120", "a121", "a122", "a123", "a124", "a125", "a126", "a127", "a128", "a129", "a130", "a131", "a132", "a133", "a134", "a135", "a136", "a137", "a138", "a139", "a140", "a141", "a142", "a143",
        "a144", "a145", "a146", "a147", "a148", "a149", "a150", "a151", "a152", "a153", "a154", "a155", "a156", "a157", "a158", "a159", "a160", "a161", "a162", "a163", "a164", "a165", "a166", "a167",
        "a168", "a169", "a170", "a171", "a172", "a173", "a174", "a175", "a176", "a177", "a178", "a179", "a180", "a181", "a182", "a183", "a184", "a185", "a186", "a187", "a188", "a189", "a190", "a191",
        "a192", "a193", "a194", "a195", "a196", "a197", "a198", "a199", "a200", "a201", "a202", "a203", "a204", "a205", "a206", "a207", "a208", "a209", "a210", "a211", "a212", "a213", "a214", "a215",
        "a216", "a217", "a218", "a219", "a220", "a221", "a222", "a223", "a224", "a225", "a226", "a227", "a228", "a229", "a230", "a231", "a232", "a233", "a234", "a235", "a236", "a237", "a238", "a239",
        "a240", "a241", "a242", "a243", "a244", "a245", "a246", "a247", "a248", "a249", "a250", "a251", "a252", "a253", "a254", "a255", "s62", "s63", "s64", "s65", "s66", "s67", "s68", "s69",
        "s70", "s71", "s72", "s73", "s74", "s75", "s76", "s77", "s78", "s79", "s80", "s81", "s82", "s83", "s84", "s85", "s86", "s87", "s88", "s89", "s90", "s91", "s92", "s93",
        "s94", "s95", "s96", "s97", "s98", "s99", "s100", "s101", "s52", "s53", "s54", "s55", "s56", "s57", "s58", "s59", "s60", "s61", "vcc", "scc", "m0", "memory"
  );
  return st == 0;
}

constexpr float SCL = 0.08838834764831845f * 1.4426950408889634f;
#define TS_OPS(role)

#define WS_G ((4 * w + g) * 8192 + n * 16)
#define WS_SW ((4 * w + g) * 256 + ((n >> 1) ^ ((g & 1) + 2 * w)) * 32 + (n & 1) * 16)
#define WS_TR (w * 32768 + (16 * (g >> 1) + 4 * (g & 1) + ((n >> 2) & 1) + 8 * (n >> 3)) * 256 + ((2 * w) ^ (((n >> 2) & 1) + 2 * (g & 1) + 4 * (n >> 3))) * 32 + (n & 3) * 8)
constexpr bool W8 = PLACE == 5;
DI unsigned fl(unsigned k) { return W8 ? (k & 4) << 11 : 0u; }
DI bool role_a(u8* __restrict__ ws, const u16* Wq, const u16* Wk, u8* comm, int h, unsigned ldsb) {
  const int tid = threadIdx.x, w = __builtin_amdgcn_readfirstlane(tid >> 6), lane = tid & 63, n = lane & 15, g = lane >> 4;
  constexpr unsigned parA = 0, parB = 0;
  unsigned dR = 0x200000u;
  unsigned xo[2];
#pragma unroll
  for (int i = 0; i < 2; ++i) {
    const unsigned col = 128 * w + 64 * i + 32 * (g & 1) + 16 * (g >> 1) + n;
    xo[i] = C_PX + (parA ^ fl(col >> 4)) + cmap(((col >> 3) * 32 + ((h + (col >> 3)) & 31)) * 128 + (col & 7) * 16);
  }
  const float ninf = (g < 2) ? -INFINITY : 0.f;
  const v4f cinit = {ninf, ninf, ninf, ninf};
  const unsigned img = ldsb + 16384, rdF = img + (4 * g + (n >> 2)) * 32 + (n & 3) * 8, lb = ldsb + lane * 16;
  const v2u sent = {0xffffffffu, 0xffffffffu};
  int st;
  const unsigned rp = W8 ? h & 7 : h & 3, ring = C_RING + (parA ^ fl(rp));
  unsigned oM = ring + rp * 16384u, oAn = ring + 0x80000u + rp * 128u, tag = 0x5a5a0000u;
  unsigned po = g >= 2 && n < 8 ? C_PP + cmap((2 * w + (g & 1)) * 4096u) + h * 128u + n * 16 : OOB;
  unsigned vFk = L_PR + (lane & 4) * 1024u + h * 128u + (lane & 3) * 4u, vFc = tid == 192 ? L_A + h * 128u : OOB;
  asm volatile(
      "s_nop 4\n\t"
      "s_mov_b32 s71, 0\n\t"
      "s_mov_b32 s69, 188743808\n\t"
      "s_buffer_load_dword s60, %[rs], s69\n\t"
      "s_mov_b32 s55, 0\n\t"
      "s_mov_b32 s69, 0\n\t"
      "buffer_load_dwordx4 a[0:3], %[vG], %[rW], s69 offen\n\t"
      "s_mov_b32 s69, 131072\n\t"
      "buffer_load_dwordx4 a[4:7], %[vG], %[rW], s69 offen\n\t"
      "s_mov_b32 s69, 262144\n\t"
      "buffer_load_dwordx4 a[16:19], %[vG], %[rW], s69 offen\n\t"
      "s_mov_b32 s69, 393216\n\t"
      "buffer_load_dwordx4 a[20:23], %[vG], %[rW], s69 offen\n\t"
      "s_mov_b32 s69, 524288\n\t"
      "buffer_load_dwordx4 a[32:35], %[vG], %[rW], s69 offen\n\t"
      "s_mov_b32 s69, 655360\n\t"
      "buffer_load_dwordx4 a[36:39], %[vG], %[rW], s69 offen\n\t"
      "s_mov_b32 s69, 786432\n\t"
      "buffer_load_dwordx4 a[48:51], %[vG], %[rW], s69 offen\n\t"
      "s_mov_b32 s69, 917504\n\t"
      "buffer_load_dwordx4 a[52:55], %[vG], %[rW], s69 offen\n\t"
      "s_mov_b32 s69, 1048576\n\t"
      "buffer_load_dwordx4 a[64:67], %[vG], %[rW], s69 offen\n\t"
      "s_mov_b32 s69, 1179648\n\t"
      "buffer_load_dwordx4 a[68:71], %[vG], %[rW], s69 offen\n\t"
      "s_mov_b32 s69, 1310720\n\t"
      "buffer_load_dwordx4 a[80:83], %[vG], %[rW], s69 offen\n\t"
      "s_mov_b32 s69, 1441792\n\t"
      "buffer_load_dwordx4 a[84:87], %[vG], %[rW], s69 offen\n\t"
      "s_mov_b32 s69, 1572864\n\t"
      "buffer_load_dwordx4 a[96:99], %[vG], %[rW], s69 offen\n\t"
      "s_mov_b32 s69, 1703936\n\t"
      "buffer_load_dwordx4 a[100:103], %[vG], %[rW], s69 offen\n\t"
      "s_mov_b32 s69, 1835008\n\t"
      "buffer_load_dwordx4 a[112:115], %[vG], %[rW], s69 offen\n\t"
      "s_mov_b32 s69, 1966080\n\t"
      "buffer_load_dwordx4 a[116:119], %[vG], %[rW], s69 offen\n\t"
      "s_mov_b32 s69, 2097152\n\t"
      "buffer_load_dwordx4 a[128:131], %[vG], %[rW], s69 offen\n\t"
      "s_mov_b32 s69, 2228224\n\t"
      "buffer_load_dwordx4 a[132:135], %[vG], %[rW], s69 offen\n\t"
      "s_mov_b32 s69, 2359296\n\t"
      "buffer_load_dwordx4 a[144:147], %[vG], %[rW], s69 offen\n\t"
      "s_mov_b32 s69, 2490368\n\t"
      "buffer_load_dwordx4 a[148:151], %[vG], %[rW], s69 offen\n\t"
      "s_mov_b32 s69, 2621440\n\t"
      "buffer_load_dwordx4 a[160:163], %[vG], %[rW], s69 offen\n\t"
      "s_mov_b32 s69, 2752512\n\t"
      "buffer_load_dwordx4 a[164:167], %[vG], %[rW], s69 offen\n\t"
      "s_mov_b32 s69, 2883584\n\t"
      "buffer_load_dwordx4 a[176:179], %[vG], %[rW], s69 offen\n\t"
      "s_mov_b32 s69, 3014656\n\t"
      "buffer_load_dwordx4 a[180:183], %[vG], %[rW], s69 offen\n\t"
      "s_mov_b32 s69, 3145728\n\t"
      "buffer_load_dwordx4 a[192:195], %[vG], %[rW], s69 offen\n\t"
      "s_mov_b32 s69, 3276800\n\t"
      "buffer_load_dwordx4 a[196:199], %[vG], %[rW], s69 offen\n\t"
      "s_mov_b32 s69, 3407872\n\t"
      "buffer_load_dwordx4 a[208:211], %[vG], %[rW], s69 offen\n\t"
      "s_mov_b32 s69, 3538944\n\t"
      "buffer_load_dwordx4 a[212:215], %[vG], %[rW], s69 offen\n\t"
      "s_mov_b32 s69, 3670016\n\t"
      "buffer_load_dwordx4 a[224:227], %[vG], %[rW], s69 offen\n\t"
      "s_mov_b32 s69, 3801088\n\t"
      "buffer_load_dwordx4 a[228:231], %[vG], %[rW], s69 offen\n\t"
      "s_mov_b32 s69, 3932160\n\t"
      "buffer_load_dwordx4 a[240:243], %[vG], %[rW], s69 offen\n\t"
      "s_mov_b32 s69, 4063232\n\t"
      "buffer_load_dwordx4 a[244:247], %[vG], %[rW], s69 offen\n\t"
      "s_mov_b32 s69, 0\n\t"
      "buffer_load_dwordx4 a[8:11], %[vG], %[rW2], s69 offen\n\t"
      "s_mov_b32 s69, 131072\n\t"
      "buffer_load_dwordx4 a[12:15], %[vG], %[rW2], s69 offen\n\t"
      "s_mov_b32 s69, 262144\n\t"
      "buffer_load_dwordx4 a[24:27], %[vG], %[rW2], s69 offen\n\t"
      "s_mov_b32 s69, 393216\n\t"
      "buffer_load_dwordx4 a[28:31], %[vG], %[rW2], s69 offen\n\t"
      "s_mov_b32 s69, 524288\n\t"
      "buffer_load_dwordx4 a[40:43], %[vG], %[rW2], s69 offen\n\t"
      "s_mov_b32 s69, 655360\n\t"
      "buffer_load_dwordx4 a[44:47], %[vG], %[rW2], s69 offen\n\t"
      "s_mov_b32 s69, 786432\n\t"
      "buffer_load_dwordx4 a[56:59], %[vG], %[rW2], s69 offen\n\t"
      "s_mov_b32 s69, 917504\n\t"
      "buffer_load_dwordx4 a[60:63], %[vG], %[rW2], s69 offen\n\t"
      "s_mov_b32 s69, 1048576\n\t"
      "buffer_load_dwordx4 a[72:75], %[vG], %[rW2], s69 offen\n\t"
      "s_mov_b32 s69, 1179648\n\t"
      "buffer_load_dwordx4 a[76:79], %[vG], %[rW2], s69 offen\n\t"
      "s_mov_b32 s69, 1310720\n\t"
      "buffer_load_dwordx4 a[88:91], %[vG], %[rW2], s69 offen\n\t"
      "s_mov_b32 s69, 1441792\n\t"
      "buffer_load_dwordx4 a[92:95], %[vG], %[rW2], s69 offen\n\t"
      "s_mov_b32 s69, 1572864\n\t"
      "buffer_load_dwordx4 a[104:107], %[vG], %[rW2], s69 offen\n\t"
      "s_mov_b32 s69, 1703936\n\t"
      "buffer_load_dwordx4 a[108:111], %[vG], %[rW2], s69 offen\n\t"
      "s_mov_b32 s69, 1835008\n\t"
      "buffer_load_dwordx4 a[120:123], %[vG], %[rW2], s69 offen\n\t"
      "s_mov_b32 s69, 1966080\n\t"
      "buffer_load_dwordx4 a[124:127], %[vG], %[rW2], s69 offen\n\t"
      "s_mov_b32 s69, 2097152\n\t"
      "buffer_load_dwordx4 a[136:139], %[vG], %[rW2], s69 offen\n\t"
      "s_mov_b32 s69, 2228224\n\t"
      "buffer_load_dwordx4 a[140:143], %[vG], %[rW2], s69 offen\n\t"
      "s_mov_b32 s69, 2359296\n\t"
      "buffer_load_dwordx4 a[152:155], %[vG], %[rW2], s69 offen\n\t"
      "s_mov_b32 s69, 2490368\n\t"
      "buffer_load_dwordx4 a[156:159], %[vG], %[rW2], s69 offen\n\t"
      "s_mov_b32 s69, 2621440\n\t"
      "buffer_load_dwordx4 a[168:171], %[vG], %[rW2], s69 offen\n\t"
      "s_mov_b32 s69, 2752512\n\t"
      "buffer_load_dwordx4 a[172:175], %[vG], %[rW2], s69 offen\n\t"
      "s_mov_b32 s69, 2883584\n\t"
      "buffer_load_dwordx4 a[184:187], %[vG], %[rW2], s69 offen\n\t"
      "s_mov_b32 s69, 3014656\n\t"
      "buffer_load_dwordx4 a[188:191], %[vG], %[rW2], s69 offen\n\t"
      "s_mov_b32 s69, 3145728\n\t"
      "buffer_load_dwordx4 a[200:203], %[vG], %[rW2], s69 offen\n\t"
      "s_mov_b32 s69, 3276800\n\t"
      "buffer_load_dwordx4 a[204:207], %[vG], %[rW2], s69 offen\n\t"
      "s_mov_b32 s69, 3407872\n\t"
      "buffer_load_dwordx4 a[216:219], %[vG], %[rW2], s69 offen\n\t"
      "s_mov_b32 s69, 3538944\n\t"
      "buffer_load_dwordx4 a[220:223], %[vG], %[rW2], s69 offen\n\t"
      "s_mov_b32 s69, 3670016\n\t"
      "buffer_load_dwordx4 a[232:235], %[vG], %[rW2], s69 offen\n\t"
      "s_mov_b32 s69, 3801088\n\t"
      "buffer_load_dwordx4 a[236:239], %[vG], %[rW2], s69 offen\n\t"
      "s_mov_b32 s69, 3932160\n\t"
      "buffer_load_dwordx4 a[248:251], %[vG], %[rW2], s69 offen\n\t"
      "s_mov_b32 s69, 4063232\n\t"
      "buffer_load_dwordx4 a[252:255], %[vG], %[rW2], s69 offen\n\t"
      "v_sub_u32 v40, %[vWm], %[vMw]\n\t"
      "v_add_u32 v41, %[vSw], v40\n\t"
      "v_add_u32 v42, 0x10000, v41\n\t"
      "v_xor_b32 v43, 0, %[vTr]\n\t"
      "v_add_u32 v43, v43, v40\n\t"
      "v_xor_b32 v44, 32768, %[vTr]\n\t"
      "v_add_u32 v44, v44, v40\n\t"
      "v_xor_b32 v45, 65536, %[vTr]\n\t"
      "v_add_u32 v45, v45, v40\n\t"
      "v_xor_b32 v46, 98304, %[vTr]\n\t"
      "v_add_u32 v46, v46, v40\n\t"
      "v_xor_b32 v47, 32, %[vTr]\n\t"
      "v_add_u32 v47, v47, v40\n\t"
      "v_xor_b32 v48, 32800, %[vTr]\n\t"
      "v_add_u32 v48, v48, v40\n\t"
      "v_xor_b32 v49, 65568, %[vTr]\n\t"
      "v_add_u32 v49, v49, v40\n\t"
      "v_xor_b32 v50, 98336, %[vTr]\n\t"
      "v_add_u32 v50, v50, v40\n\t"
      "s_waitcnt vmcnt(63)\n\t"
      "ds_write_b128 v41, a[0:3] offset:0\n\t"
      "s_waitcnt vmcnt(62)\n\t"
      "ds_write_b128 v41, a[4:7] offset:4096\n\t"
      "s_waitcnt vmcnt(61)\n\t"
      "ds_write_b128 v41, a[16:19] offset:8192\n\t"
      "s_waitcnt vmcnt(60)\n\t"
      "ds_write_b128 v41, a[20:23] offset:12288\n\t"
      "s_waitcnt vmcnt(59)\n\t"
      "ds_write_b128 v41, a[32:35] offset:16384\n\t"
      "s_waitcnt vmcnt(58)\n\t"
      "ds_write_b128 v41, a[36:39] offset:20480\n\t"
      "s_waitcnt vmcnt(57)\n\t"
      "ds_write_b128 v41, a[48:51] offset:24576\n\t"
      "s_waitcnt vmcnt(56)\n\t"
      "ds_write_b128 v41, a[52:55] offset:28672\n\t"
      "s_waitcnt vmcnt(55)\n\t"
      "ds_write_b128 v41, a[64:67] offset:32768\n\t"
      "s_waitcnt vmcnt(54)\n\t"
      "ds_write_b128 v41, a[68:71] offset:36864\n\t"
      "s_waitcnt vmcnt(53)\n\t"
      "ds_write_b128 v41, a[80:83] offset:40960\n\t"
      "s_waitcnt vmcnt(52)\n\t"
      "ds_write_b128 v41, a[84:87] offset:45056\n\t"
      "s_waitcnt vmcnt(51)\n\t"
      "ds_write_b128 v41, a[96:99] offset:49152\n\t"
      "s_waitcnt vmcnt(50)\n\t"
      "ds_write_b128 v41, a[100:103] offset:53248\n\t"
      "s_waitcnt vmcnt(49)\n\t"
      "ds_write_b128 v41, a[112:115] offset:57344\n\t"
      "s_waitcnt vmcnt(48)\n\t"
      "ds_write_b128 v41, a[116:119] offset:61440\n\t"
      "s_waitcnt vmcnt(47)\n\t"
      "ds_write_b128 v42, a[128:131] offset:0\n\t"
      "s_waitcnt vmcnt(46)\n\t"
      "ds_write_b128 v42, a[132:135] offset:4096\n\t"
      "s_waitcnt vmcnt(45)\n\t"
      "ds_write_b128 v42, a[144:147] offset:8192\n\t"
      "s_waitcnt vmcnt(44)\n\t"
      "ds_write_b128 v42, a[148:151] offset:12288\n\t"
      "s_waitcnt vmcnt(43)\n\t"
      "ds_write_b128 v42, a[160:163] offset:16384\n\t"
      "s_waitcnt vmcnt(42)\n\t"
      "ds_write_b128 v42, a[164:167] offset:20480\n\t"
      "s_waitcnt vmcnt(41)\n\t"
      "ds_write_b128 v42, a[176:179] offset:24576\n\t"
      "s_waitcnt vmcnt(40)\n\t"
      "ds_write_b128 v42, a[180:183] offset:28672\n\t"
      "s_waitcnt vmcnt(39)\n\t"
      "ds_write_b128 v42, a[192:195] offset:32768\n\t"
      "s_waitcnt vmcnt(38)\n\t"
      "ds_write_b128 v42, a[196:199] offset:36864\n\t"
      "s_waitcnt vmcnt(37)\n\t"
      "ds_write_b128 v42, a[208:211] offset:40960\n\t"
      "s_waitcnt vmcnt(36)\n\t"
      "ds_write_b128 v42, a[212:215] offset:45056\n\t"
      "s_waitcnt vmcnt(35)\n\t"
      "ds_write_b128 v42, a[224:227] offset:49152\n\t"
      "s_waitcnt vmcnt(34)\n\t"
      "ds_write_b128 v42, a[228:231] offset:53248\n\t"
      "s_waitcnt vmcnt(33)\n\t"
      "ds_write_b128 v42, a[240:243] offset:57344\n\t"
      "s_waitcnt vmcnt(32)\n\t"
      "ds_write_b128 v42, a[244:247] offset:61440\n\t"
      "s_waitcnt lgkmcnt(0)\n\t"
      "s_barrier\n\t"
      "ds_read_b64_tr_b16 a[0:1], v43 offset:0\n\t"
      "ds_read_b64_tr_b16 a[2:3], v43 offset:512\n\t"
      "ds_read_b64_tr_b16 a[4:5], v43 offset:8192\n\t"
      "ds_read_b64_tr_b16 a[6:7], v43 offset:8704\n\t"
      "ds_read_b64_tr_b16 a[16:17], v43 offset:16384\n\t"
      "ds_read_b64_tr_b16 a[18:19], v43 offset:16896\n\t"
      "ds_read_b64_tr_b16 a[20:21], v43 offset:24576\n\t"
      "ds_read_b64_tr_b16 a[22:23], v43 offset:25088\n\t"
      "ds_read_b64_tr_b16 a[32:33], v44 offset:0\n\t"
      "ds_read_b64_tr_b16 a[34:35], v44 offset:512\n\t"
      "ds_read_b64_tr_b16 a[36:37], v44 offset:8192\n\t"
      "ds_read_b64_tr_b16 a[38:39], v44 offset:8704\n\t"
      "ds_read_b64_tr_b16 a[48:49], v44 offset:16384\n\t"
      "ds_read_b64_tr_b16 a[50:51], v44 offset:16896\n\t"
      "ds_read_b64_tr_b16 a[52:53], v44 offset:24576\n\t"
      "ds_read_b64_tr_b16 a[54:55], v44 offset:25088\n\t"
      "ds_read_b64_tr_b16 a[64:65], v45 offset:0\n\t"
      "ds_read_b64_tr_b16 a[66:67], v45 offset:512\n\t"
      "ds_read_b64_tr_b16 a[68:69], v45 offset:8192\n\t"
      "ds_read_b64_tr_b16 a[70:71], v45 offset:8704\n\t"
      "ds_read_b64_tr_b16 a[80:81], v45 offset:16384\n\t"
      "ds_read_b64_tr_b16 a[82:83], v45 offset:16896\n\t"
      "ds_read_b64_tr_b16 a[84:85], v45 offset:24576\n\t"
      "ds_read_b64_tr_b16 a[86:87], v45 offset:25088\n\t"
      "ds_read_b64_tr_b16 a[96:97], v46 offset:0\n\t"
      "ds_read_b64_tr_b16 a[98:99], v46 offset:512\n\t"
      "ds_read_b64_tr_b16 a[100:101], v46 offset:8192\n\t"
      "ds_read_b64_tr_b16 a[102:103], v46 offset:8704\n\t"
      "ds_read_b64_tr_b16 a[112:113], v46 offset:16384\n\t"
      "ds_read_b64_tr_b16 a[114:115], v46 offset:16896\n\t"
      "ds_read_b64_tr_b16 a[116:117], v46 offset:24576\n\t"
      "ds_read_b64_tr_b16 a[118:119], v46 offset:25088\n\t"
      "ds_read_b64_tr_b16 a[128:129], v47 offset:0\n\t"
      "ds_read_b64_tr_b16 a[130:131], v47 offset:512\n\t"
      "ds_read_b64_tr_b16 a[132:133], v47 offset:8192\n\t"
      "ds_read_b64_tr_b16 a[134:135], v47 offset:8704\n\t"
      "ds_read_b64_tr_b16 a[144:145], v47 offset:16384\n\t"
      "ds_read_b64_tr_b16 a[146:147], v47 offset:16896\n\t"
      "ds_read_b64_tr_b16 a[148:149], v47 offset:24576\n\t"
      "ds_read_b64_tr_b16 a[150:151], v47 offset:25088\n\t"
      "ds_read_b64_tr_b16 a[160:161], v48 offset:0\n\t"
      "ds_read_b64_tr_b16 a[162:163], v48 offset:512\n\t"
      "ds_read_b64_tr_b16 a[164:165], v48 offset:8192\n\t"
      "ds_read_b64_tr_b16 a[166:167], v48 offset:8704\n\t"
      "ds_read_b64_tr_b16 a[176:177], v48 offset:16384\n\t"
      "ds_read_b64_tr_b16 a[178:179], v48 offset:16896\n\t"
      "ds_read_b64_tr_b16 a[180:181], v48 offset:24576\n\t"
      "ds_read_b64_tr_b16 a[182:183], v48 offset:25088\n\t"
      "ds_read_b64_tr_b16 a[192:193], v49 offset:0\n\t"
      "ds_read_b64_tr_b16 a[194:195], v49 offset:512\n\t"
      "ds_read_b64_tr_b16 a[196:197], v49 offset:8192\n\t"
      "ds_read_b64_tr_b16 a[198:199], v49 offset:8704\n\t"
      "ds_read_b64_tr_b16 a[208:209], v49 offset:16384\n\t"
      "ds_read_b64_tr_b16 a[210:211], v49 offset:16896\n\t"
      "ds_read_b64_tr_b16 a[212:213], v49 offset:24576\n\t"
      "ds_read_b64_tr_b16 a[214:215], v49 offset:25088\n\t"
      "ds_read_b64_tr_b16 a[224:225], v50 offset:0\n\t"
      "ds_read_b64_tr_b16 a[226:227], v50 offset:512\n\t"
      "ds_read_b64_tr_b16 a[228:229], v50 offset:8192\n\t"
      "ds_read_b64_tr_b16 a[230:231], v50 offset:8704\n\t"
      "ds_read_b64_tr_b16 a[240:241], v50 offset:16384\n\t"
      "ds_read_b64_tr_b16 a[242:243], v50 offset:16896\n\t"
      "ds_read_b64_tr_b16 a[244:245], v50 offset:24576\n\t"
      "ds_read_b64_tr_b16 a[246:247], v50 offset:25088\n\t"
      "s_waitcnt lgkmcnt(0)\n\t"
      "s_barrier\n\t"
      "s_waitcnt vmcnt(31)\n\t"
      "ds_write_b128 v41, a[8:11] offset:0\n\t"
      "s_waitcnt vmcnt(30)\n\t"
      "ds_write_b128 v41, a[12:15] offset:4096\n\t"
      "s_waitcnt vmcnt(29)\n\t"
      "ds_write_b128 v41, a[24:27] offset:8192\n\t"
      "s_waitcnt vmcnt(28)\n\t"
      "ds_write_b128 v41, a[28:31] offset:12288\n\t"
      "s_waitcnt vmcnt(27)\n\t"
      "ds_write_b128 v41, a[40:43] offset:16384\n\t"
      "s_waitcnt vmcnt(26)\n\t"
      "ds_write_b128 v41, a[44:47] offset:20480\n\t"
      "s_waitcnt vmcnt(25)\n\t"
      "ds_write_b128 v41, a[56:59] offset:24576\n\t"
      "s_waitcnt vmcnt(24)\n\t"
      "ds_write_b128 v41, a[60:63] offset:28672\n\t"
      "s_waitcnt vmcnt(23)\n\t"
      "ds_write_b128 v41, a[72:75] offset:32768\n\t"
      "s_waitcnt vmcnt(22)\n\t"
      "ds_write_b128 v41, a[76:79] offset:36864\n\t"
      "s_waitcnt vmcnt(21)\n\t"
      "ds_write_b128 v41, a[88:91] offset:40960\n\t"
      "s_waitcnt vmcnt(20)\n\t"
      "ds_write_b128 v41, a[92:95] offset:45056\n\t"
      "s_waitcnt vmcnt(19)\n\t"
      "ds_write_b128 v41, a[104:107] offset:49152\n\t"
      "s_waitcnt vmcnt(18)\n\t"
      "ds_write_b128 v41, a[108:111] offset:53248\n\t"
      "s_waitcnt vmcnt(17)\n\t"
      "ds_write_b128 v41, a[120:123] offset:57344\n\t"
      "s_waitcnt vmcnt(16)\n\t"
      "ds_write_b128 v41, a[124:127] offset:61440\n\t"
      "s_waitcnt vmcnt(15)\n\t"
      "ds_write_b128 v42, a[136:139] offset:0\n\t"
      "s_waitcnt vmcnt(14)\n\t"
      "ds_write_b128 v42, a[140:143] offset:4096\n\t"
      "s_waitcnt vmcnt(13)\n\t"
      "ds_write_b128 v42, a[152:155] offset:8192\n\t"
      "s_waitcnt vmcnt(12)\n\t"
      "ds_write_b128 v42, a[156:159] offset:12288\n\t"
      "s_waitcnt vmcnt(11)\n\t"
      "ds_write_b128 v42, a[168:171] offset:16384\n\t"
      "s_waitcnt vmcnt(10)\n\t"
      "ds_write_b128 v42, a[172:175] offset:20480\n\t"
      "s_waitcnt vmcnt(9)\n\t"
      "ds_write_b128 v42, a[184:187] offset:24576\n\t"
      "s_waitcnt vmcnt(8)\n\t"
      "ds_write_b128 v42, a[188:191] offset:28672\n\t"
      "s_waitcnt vmcnt(7)\n\t"
      "ds_write_b128 v42, a[200:203] offset:32768\n\t"
      "s_waitcnt vmcnt(6)\n\t"
      "ds_write_b128 v42, a[204:207] offset:36864\n\t"
      "s_waitcnt vmcnt(5)\n\t"
      "ds_write_b128 v42, a[216:219] offset:40960\n\t"
      "s_waitcnt vmcnt(4)\n\t"
      "ds_write_b128 v42, a[220:223] offset:45056\n\t"
      "s_waitcnt vmcnt(3)\n\t"
      "ds_write_b128 v42, a[232:235] offset:49152\n\t"
      "s_waitcnt vmcnt(2)\n\t"
      "ds_write_b128 v42, a[236:239] offset:53248\n\t"
      "s_waitcnt vmcnt(1)\n\t"
      "ds_write_b128 v42, a[248:251] offset:57344\n\t"
      "s_waitcnt vmcnt(0)\n\t"
      "ds_write_b128 v42, a[252:255] offset:61440\n\t"
      "s_mov_b32 s69, 188747776\n\t"
      "buffer_load_dwordx4 v[64:67], %[vL], %[rs], s69 offen sc1\n\t"
      "s_waitcnt lgkmcnt(0)\n\t"
      "s_barrier\n\t"
      "ds_read_b64_tr_b16 a[8:9], v43 offset:0\n\t"
      "ds_read_b64_tr_b16 a[10:11], v43 offset:512\n\t"
      "ds_read_b64_tr_b16 a[12:13], v43 offset:8192\n\t"
      "ds_read_b64_tr_b16 a[14:15], v43 offset:8704\n\t"
      "ds_read_b64_tr_b16 a[24:25], v43 offset:16384\n\t"
      "ds_read_b64_tr_b16 a[26:27], v43 offset:16896\n\t"
      "ds_read_b64_tr_b16 a[28:29], v43 offset:24576\n\t"
      "ds_read_b64_tr_b16 a[30:31], v43 offset:25088\n\t"
      "ds_read_b64_tr_b16 a[40:41], v44 offset:0\n\t"
      "ds_read_b64_tr_b16 a[42:43], v44 offset:512\n\t"
      "ds_read_b64_tr_b16 a[44:45], v44 offset:8192\n\t"
      "ds_read_b64_tr_b16 a[46:47], v44 offset:8704\n\t"
      "ds_read_b64_tr_b16 a[56:57], v44 offset:16384\n\t"
      "ds_read_b64_tr_b16 a[58:59], v44 offset:16896\n\t"
      "ds_read_b64_tr_b16 a[60:61], v44 offset:24576\n\t"
      "ds_read_b64_tr_b16 a[62:63], v44 offset:25088\n\t"
      "ds_read_b64_tr_b16 a[72:73], v45 offset:0\n\t"
      "ds_read_b64_tr_b16 a[74:75], v45 offset:512\n\t"
      "ds_read_b64_tr_b16 a[76:77], v45 offset:8192\n\t"
      "ds_read_b64_tr_b16 a[78:79], v45 offset:8704\n\t"
      "ds_read_b64_tr_b16 a[88:89], v45 offset:16384\n\t"
      "ds_read_b64_tr_b16 a[90:91], v45 offset:16896\n\t"
      "ds_read_b64_tr_b16 a[92:93], v45 offset:24576\n\t"
      "ds_read_b64_tr_b16 a[94:95], v45 offset:25088\n\t"
      "ds_read_b64_tr_b16 a[104:105], v46 offset:0\n\t"
      "ds_read_b64_tr_b16 a[106:107], v46 offset:512\n\t"
      "ds_read_b64_tr_b16 a[108:109], v46 offset:8192\n\t"
      "ds_read_b64_tr_b16 a[110:111], v46 offset:8704\n\t"
      "ds_read_b64_tr_b16 a[120:121], v46 offset:16384\n\t"
      "ds_read_b64_tr_b16 a[122:123], v46 offset:16896\n\t"
      "ds_read_b64_tr_b16 a[124:125], v46 offset:24576\n\t"
      "ds_read_b64_tr_b16 a[126:127], v46 offset:25088\n\t"
      "ds_read_b64_tr_b16 a[136:137], v47 offset:0\n\t"
      "ds_read_b64_tr_b16 a[138:139], v47 offset:512\n\t"
      "ds_read_b64_tr_b16 a[140:141], v47 offset:8192\n\t"
      "ds_read_b64_tr_b16 a[142:143], v47 offset:8704\n\t"
      "ds_read_b64_tr_b16 a[152:153], v47 offset:16384\n\t"
      "ds_read_b64_tr_b16 a[154:155], v47 offset:16896\n\t"
      "ds_read_b64_tr_b16 a[156:157], v47 offset:24576\n\t"
      "ds_read_b64_tr_b16 a[158:159], v47 offset:25088\n\t"
      "ds_read_b64_tr_b16 a[168:169], v48 offset:0\n\t"
      "ds_read_b64_tr_b16 a[170:171], v48 offset:512\n\t"
      "ds_read_b64_tr_b16 a[172:173], v48 offset:8192\n\t"
      "ds_read_b64_tr_b16 a[174:175], v48 offset:8704\n\t"
      "ds_read_b64_tr_b16 a[184:185], v48 offset:16384\n\t"
      "ds_read_b64_tr_b16 a[186:187], v48 offset:16896\n\t"
      "ds_read_b64_tr_b16 a[188:189], v48 offset:24576\n\t"
      "ds_read_b64_tr_b16 a[190:191], v48 offset:25088\n\t"
      "ds_read_b64_tr_b16 a[200:201], v49 offset:0\n\t"
      "ds_read_b64_tr_b16 a[202:203], v49 offset:512\n\t"
      "ds_read_b64_tr_b16 a[204:205], v49 offset:8192\n\t"
      "ds_read_b64_tr_b16 a[206:207], v49 offset:8704\n\t"
      "ds_read_b64_tr_b16 a[216:217], v49 offset:16384\n\t"
      "ds_read_b64_tr_b16 a[218:219], v49 offset:16896\n\t"
      "ds_read_b64_tr_b16 a[220:221], v49 offset:24576\n\t"
      "ds_read_b64_tr_b16 a[222:223], v49 offset:25088\n\t"
      "ds_read_b64_tr_b16 a[232:233], v50 offset:0\n\t"
      "ds_read_b64_tr_b16 a[234:235], v50 offset:512\n\t"
      "ds_read_b64_tr_b16 a[236:237], v50 offset:8192\n\t"
      "ds_read_b64_tr_b16 a[238:239], v50 offset:8704\n\t"
      "ds_read_b64_tr_b16 a[248:249], v50 offset:16384\n\t"
      "ds_read_b64_tr_b16 a[250:251], v50 offset:16896\n\t"
      "ds_read_b64_tr_b16 a[252:253], v50 offset:24576\n\t"
      "ds_read_b64_tr_b16 a[254:255], v50 offset:25088\n\t"
      "s_waitcnt lgkmcnt(0)\n\t"
      "s_barrier\n\t"
      "v_accvgpr_read_b32 v72, a1\n\t"
      "v_accvgpr_mov_b32 a1, a9\n\t"
      "v_accvgpr_write_b32 a9, v72\n\t"
      "v_accvgpr_read_b32 v73, a3\n\t"
      "v_accvgpr_mov_b32 a3, a11\n\t"
      "v_accvgpr_write_b32 a11, v73\n\t"
      "v_accvgpr_read_b32 v74, a5\n\t"
      "v_accvgpr_mov_b32 a5, a13\n\t"
      "v_accvgpr_write_b32 a13, v74\n\t"
      "v_accvgpr_read_b32 v75, a7\n\t"
      "v_accvgpr_mov_b32 a7, a15\n\t"
      "v_accvgpr_write_b32 a15, v75\n\t"
      "v_accvgpr_read_b32 v76, a17\n\t"
      "v_accvgpr_mov_b32 a17, a25\n\t"
      "v_accvgpr_write_b32 a25, v76\n\t"
      "v_accvgpr_read_b32 v77, a19\n\t"
      "v_accvgpr_mov_b32 a19, a27\n\t"
      "v_accvgpr_write_b32 a27, v77\n\t"
      "v_accvgpr_read_b32 v78, a21\n\t"
      "v_accvgpr_mov_b32 a21, a29\n\t"
      "v_accvgpr_write_b32 a29, v78\n\t"
      "v_accvgpr_read_b32 v79, a23\n\t"
      "v_accvgpr_mov_b32 a23, a31\n\t"
      "v_accvgpr_write_b32 a31, v79\n\t"
      "v_accvgpr_read_b32 v72, a33\n\t"
      "v_accvgpr_mov_b32 a33, a41\n\t"
      "v_accvgpr_write_b32 a41, v72\n\t"
      "v_accvgpr_read_b32 v73, a35\n\t"
      "v_accvgpr_mov_b32 a35, a43\n\t"
      "v_accvgpr_write_b32 a43, v73\n\t"
      "v_accvgpr_read_b32 v74, a37\n\t"
      "v_accvgpr_mov_b32 a37, a45\n\t"
      "v_accvgpr_write_b32 a45, v74\n\t"
      "v_accvgpr_read_b32 v75, a39\n\t"
      "v_accvgpr_mov_b32 a39, a47\n\t"
      "v_accvgpr_write_b32 a47, v75\n\t"
      "v_accvgpr_read_b32 v76, a49\n\t"
      "v_accvgpr_mov_b32 a49, a57\n\t"
      "v_accvgpr_write_b32 a57, v76\n\t"
      "v_accvgpr_read_b32 v77, a51\n\t"
      "v_accvgpr_mov_b32 a51, a59\n\t"
      "v_accvgpr_write_b32 a59, v77\n\t"
      "v_accvgpr_read_b32 v78, a53\n\t"
      "v_accvgpr_mov_b32 a53, a61\n\t"
      "v_accvgpr_write_b32 a61, v78\n\t"
      "v_accvgpr_read_b32 v79, a55\n\t"
      "v_accvgpr_mov_b32 a55, a63\n\t"
      "v_accvgpr_write_b32 a63, v79\n\t"
      "v_accvgpr_read_b32 v72, a65\n\t"
      "v_accvgpr_mov_b32 a65, a73\n\t"
      "v_accvgpr_write_b32 a73, v72\n\t"
      "v_accvgpr_read_b32 v73, a67\n\t"
      "v_accvgpr_mov_b32 a67, a75\n\t"
      "v_accvgpr_write_b32 a75, v73\n\t"
      "v_accvgpr_read_b32 v74, a69\n\t"
      "v_accvgpr_mov_b32 a69, a77\n\t"
      "v_accvgpr_write_b32 a77, v74\n\t"
      "v_accvgpr_read_b32 v75, a71\n\t"
      "v_accvgpr_mov_b32 a71, a79\n\t"
      "v_accvgpr_write_b32 a79, v75\n\t"
      "v_accvgpr_read_b32 v76, a81\n\t"
      "v_accvgpr_mov_b32 a81, a89\n\t"
      "v_accvgpr_write_b32 a89, v76\n\t"
      "v_accvgpr_read_b32 v77, a83\n\t"
      "v_accvgpr_mov_b32 a83, a91\n\t"
      "v_accvgpr_write_b32 a91, v77\n\t"
      "v_accvgpr_read_b32 v78, a85\n\t"
      "v_accvgpr_mov_b32 a85, a93\n\t"
      "v_accvgpr_write_b32 a93, v78\n\t"
      "v_accvgpr_read_b32 v79, a87\n\t"
      "v_accvgpr_mov_b32 a87, a95\n\t"
      "v_accvgpr_write_b32 a95, v79\n\t"
      "v_accvgpr_read_b32 v72, a97\n\t"
      "v_accvgpr_mov_b32 a97, a105\n\t"
      "v_accvgpr_write_b32 a105, v72\n\t"
      "v_accvgpr_read_b32 v73, a99\n\t"
      "v_accvgpr_mov_b32 a99, a107\n\t"
      "v_accvgpr_write_b32 a107, v73\n\t"
      "v_accvgpr_read_b32 v74, a101\n\t"
      "v_accvgpr_mov_b32 a101, a109\n\t"
      "v_accvgpr_write_b32 a109, v74\n\t"
      "v_accvgpr_read_b32 v75, a103\n\t"
      "v_accvgpr_mov_b32 a103, a111\n\t"
      "v_accvgpr_write_b32 a111, v75\n\t"
      "v_accvgpr_read_b32 v76, a113\n\t"
      "v_accvgpr_mov_b32 a113, a121\n\t"
      "v_accvgpr_write_b32 a121, v76\n\t"
      "v_accvgpr_read_b32 v77, a115\n\t"
      "v_accvgpr_mov_b32 a115, a123\n\t"
      "v_accvgpr_write_b32 a123, v77\n\t"
      "v_accvgpr_read_b32 v78, a117\n\t"
      "v_accvgpr_mov_b32 a117, a125\n\t"
      "v_accvgpr_write_b32 a125, v78\n\t"
      "v_accvgpr_read_b32 v79, a119\n\t"
      "v_accvgpr_mov_b32 a119, a127\n\t"
      "v_accvgpr_write_b32 a127, v79\n\t"
      "v_accvgpr_read_b32 v72, a129\n\t"
      "v_accvgpr_mov_b32 a129, a137\n\t"
      "v_accvgpr_write_b32 a137, v72\n\t"
      "v_accvgpr_read_b32 v73, a131\n\t"
      "v_accvgpr_mov_b32 a131, a139\n\t"
      "v_accvgpr_write_b32 a139, v73\n\t"
      "v_accvgpr_read_b32 v74, a133\n\t"
      "v_accvgpr_mov_b32 a133, a141\n\t"
      "v_accvgpr_write_b32 a141, v74\n\t"
      "v_accvgpr_read_b32 v75, a135\n\t"
      "v_accvgpr_mov_b32 a135, a143\n\t"
      "v_accvgpr_write_b32 a143, v75\n\t"
      "v_accvgpr_read_b32 v76, a145\n\t"
      "v_accvgpr_mov_b32 a145, a153\n\t"
      "v_accvgpr_write_b32 a153, v76\n\t"
      "v_accvgpr_read_b32 v77, a147\n\t"
      "v_accvgpr_mov_b32 a147, a155\n\t"
      "v_accvgpr_write_b32 a155, v77\n\t"
      "v_accvgpr_read_b32 v78, a149\n\t"
      "v_accvgpr_mov_b32 a149, a157\n\t"
      "v_accvgpr_write_b32 a157, v78\n\t"
      "v_accvgpr_read_b32 v79, a151\n\t"
      "v_accvgpr_mov_b32 a151, a159\n\t"
      "v_accvgpr_write_b32 a159, v79\n\t"
      "v_accvgpr_read_b32 v72, a161\n\t"
      "v_accvgpr_mov_b32 a161, a169\n\t"
      "v_accvgpr_write_b32 a169, v72\n\t"
      "v_accvgpr_read_b32 v73, a163\n\t"
      "v_accvgpr_mov_b32 a163, a171\n\t"
      "v_accvgpr_write_b32 a171, v73\n\t"
      "v_accvgpr_read_b32 v74, a165\n\t"
      "v_accvgpr_mov_b32 a165, a173\n\t"
      "v_accvgpr_write_b32 a173, v74\n\t"
      "v_accvgpr_read_b32 v75, a167\n\t"
      "v_accvgpr_mov_b32 a167, a175\n\t"
      "v_accvgpr_write_b32 a175, v75\n\t"
      "v_accvgpr_read_b32 v76, a177\n\t"
      "v_accvgpr_mov_b32 a177, a185\n\t"
      "v_accvgpr_write_b32 a185, v76\n\t"
      "v_accvgpr_read_b32 v77, a179\n\t"
      "v_accvgpr_mov_b32 a179, a187\n\t"
      "v_accvgpr_write_b32 a187, v77\n\t"
      "v_accvgpr_read_b32 v78, a181\n\t"
      "v_accvgpr_mov_b32 a181, a189\n\t"
      "v_accvgpr_write_b32 a189, v78\n\t"
      "v_accvgpr_read_b32 v79, a183\n\t"
      "v_accvgpr_mov_b32 a183, a191\n\t"
      "v_accvgpr_write_b32 a191, v79\n\t"
      "v_accvgpr_read_b32 v72, a193\n\t"
      "v_accvgpr_mov_b32 a193, a201\n\t"
      "v_accvgpr_write_b32 a201, v72\n\t"
      "v_accvgpr_read_b32 v73, a195\n\t"
      "v_accvgpr_mov_b32 a195, a203\n\t"
      "v_accvgpr_write_b32 a203, v73\n\t"
      "v_accvgpr_read_b32 v74, a197\n\t"
      "v_accvgpr_mov_b32 a197, a205\n\t"
      "v_accvgpr_write_b32 a205, v74\n\t"
      "v_accvgpr_read_b32 v75, a199\n\t"
      "v_accvgpr_mov_b32 a199, a207\n\t"
      "v_accvgpr_write_b32 a207, v75\n\t"
      "v_accvgpr_read_b32 v76, a209\n\t"
      "v_accvgpr_mov_b32 a209, a217\n\t"
      "v_accvgpr_write_b32 a217, v76\n\t"
      "v_accvgpr_read_b32 v77, a211\n\t"
      "v_accvgpr_mov_b32 a211, a219\n\t"
      "v_accvgpr_write_b32 a219, v77\n\t"
      "v_accvgpr_read_b32 v78, a213\n\t"
      "v_accvgpr_mov_b32 a213, a221\n\t"
      "v_accvgpr_write_b32 a221, v78\n\t"
      "v_accvgpr_read_b32 v79, a215\n\t"
      "v_accvgpr_mov_b32 a215, a223\n\t"
      "v_accvgpr_write_b32 a223, v79\n\t"
      "v_accvgpr_read_b32 v72, a225\n\t"
      "v_accvgpr_mov_b32 a225, a233\n\t"
      "v_accvgpr_write_b32 a233, v72\n\t"
      "v_accvgpr_read_b32 v73, a227\n\t"
      "v_accvgpr_mov_b32 a227, a235\n\t"
      "v_accvgpr_write_b32 a235, v73\n\t"
      "v_accvgpr_read_b32 v74, a229\n\t"
      "v_accvgpr_mov_b32 a229, a237\n\t"
      "v_accvgpr_write_b32 a237, v74\n\t"
      "v_accvgpr_read_b32 v75, a231\n\t"
      "v_accvgpr_mov_b32 a231, a239\n\t"
      "v_accvgpr_write_b32 a239, v75\n\t"
      "v_accvgpr_read_b32 v76, a241\n\t"
      "v_accvgpr_mov_b32 a241, a249\n\t"
      "v_accvgpr_write_b32 a249, v76\n\t"
      "v_accvgpr_read_b32 v77, a243\n\t"
      "v_accvgpr_mov_b32 a243, a251\n\t"
      "v_accvgpr_write_b32 a251, v77\n\t"
      "v_accvgpr_read_b32 v78, a245\n\t"
      "v_accvgpr_mov_b32 a245, a253\n\t"
      "v_accvgpr_write_b32 a253, v78\n\t"
      "v_accvgpr_read_b32 v79, a247\n\t"
      "v_accvgpr_mov_b32 a247, a255\n\t"
      "v_accvgpr_write_b32 a255, v79\n\t"
      "v_xor_b32 v52, 128, %[vRm1]\n\t"
      "v_xor_b32 v53, 128, %[vRm2]\n\t"
      "v_xor_b32 v54, 128, %[vRm3]\n\t"
      "v_xor_b32 v55, 0xaaaa, %[idx]\n\t"
      "v_xor_b32 v60, 128, %[vWm]\n\t"
      "s_mul_i32 s60, s60, 0x9e3779b1\n\t"
      "s_add_u32 s60, s60, 0x7f4a7c15\n\t"
      "s_branch LIN1_%=\n\t"
      "LIN0_%=:\n\t"
      "s_add_u32 s71, s71, 1\n\t"
      "s_cmp_gt_u32 s71, 131072\n\t"
      "s_cbranch_scc1 LFAIL_%=\n\t"
      "s_sleep 4\n\t"
      "s_mov_b32 s69, 188747776\n\t"
      "buffer_load_dwordx4 v[64:67], %[vL], %[rs], s69 offen sc1\n\t"
      "LIN1_%=:\n\t"
      "s_waitcnt vmcnt(0)\n\t"
      "v_cmp_ne_u32 vcc, s60, v64\n\t"
      "v_cmp_ne_u32 s[64:65], s60, v66\n\t"
      "s_or_b64 vcc, vcc, s[64:65]\n\t"
      "s_cbranch_scc1 LIN0_%=\n\t"
      "v_add_u32 v64, v65, v67\n\t"
      "s_nop 1\n\t"
      "v_readlane_b32 s69, v64, 0\n\t"
      "v_readlane_b32 s61, v64, 1\n\t"
      "s_cmp_lt_u32 s61, s69\n\t"
      "s_cselect_b32 s61, 8192, 0\n\t"
      "v_readlane_b32 s69, v64, 2\n\t"
      "v_readlane_b32 s62, v64, 3\n\t"
      "s_cmp_lt_u32 s62, s69\n\t"
      "s_cselect_b32 s62, 8192, 0\n\t"
      "v_add_u32 %[xo0], s61, %[xo0]\n\t"
      "v_add_u32 %[xo1], s61, %[xo1]\n\t"
      "v_add_u32 %[po], s61, %[po]\n\t"
      "v_add_u32 %[vFk], s61, %[vFk]\n\t"
      "v_add_u32 %[vFc], s61, %[vFc]\n\t"
      "s_add_u32 %[oM], %[oM], s61\n\t"
      "s_add_u32 %[oAn], %[oAn], s61\n\t"
      "s_xor_b32 s69, s61, s62\n\t"
      "s_or_b32 %[dR], %[dR], s69\n\t"
      "s_mov_b32 s60, %[tm0]\n\t"
      "LLAYER_%=:\n\t"
      "s_mov_b32 s68, 0\n\t"
      "s_mov_b32 s72, 0\n\t"
      "s_mov_b32 s73, 0\n\t"
      "s_mov_b32 s74, 4096\n\t"
      "s_lshl_b32 s76, s55, 6\n\t"
      "s_mov_b32 s77, 524288\n\t"
      "s_mov_b32 s79, 65536\n\t"
      "LF0_%=:\n\t"
      "buffer_load_dword v150, %[vFk], %[rc], 0 offen sc1\n\t"
      "s_add_u32 s71, s71, 1\n\t"
      "s_cmp_gt_u32 s71, 131072\n\t"
      "s_cbranch_scc1 LFAIL_%=\n\t"
      "s_sleep 4\n\t"
      "s_waitcnt vmcnt(0)\n\t"
      "v_cmp_ge_i32 vcc, s76, v150\n\t"
      "s_cbranch_vccnz LF0_%=\n\t"
      "buffer_load_dwordx4 v[152:155], %[vMw], %[rK], s72 offen offset:0 sc1\n\t"
      "buffer_load_dwordx4 v[156:159], %[vMw], %[rK], s72 offen offset:1024 sc1\n\t"
      "buffer_load_dwordx4 v[192:195], %[vU], %[rU], s73 offen offset:0 sc1\n\t"
      "buffer_load_dwordx4 v[196:199], %[vU], %[rU], s73 offen offset:1024 sc1\n\t"
      "buffer_load_dwordx4 v[200:203], %[vU], %[rU], s73 offen offset:2048 sc1\n\t"
      "buffer_load_dwordx4 v[204:207], %[vU], %[rU], s73 offen offset:3072 sc1\n\t"
      "buffer_load_dwordx4 v[208:211], %[vU], %[rU], s74 offen offset:0 sc1\n\t"
      "buffer_load_dwordx4 v[212:215], %[vU], %[rU], s74 offen offset:1024 sc1\n\t"
      "buffer_load_dwordx4 v[216:219], %[vU], %[rU], s74 offen offset:2048 sc1\n\t"
      "buffer_load_dwordx4 v[220:223], %[vU], %[rU], s74 offen offset:3072 sc1\n\t"
      "v_mov_b64 v[104:105], 0\n\t"
      "v_mov_b64 v[106:107], 0\n\t"
      "v_mov_b64 v[108:109], 0\n\t"
      "v_mov_b64 v[110:111], 0\n\t"
      "v_mov_b64 v[112:113], 0\n\t"
      "v_mov_b64 v[114:115], 0\n\t"
      "v_mov_b64 v[116:117], 0\n\t"
      "v_mov_b64 v[118:119], 0\n\t"
      "s_waitcnt vmcnt(8)\n\t"
      "ds_write_b128 %[vWm], v[152:155] offset:8192\n\t"
      "ds_write_b128 %[vWm], v[156:159] offset:9216\n\t"
      "s_waitcnt lgkmcnt(0)\n\t"
      "s_barrier\n\t"
      "ds_read_b128 v[160:163], %[vLd] offset:8192\n\t"
      "ds_read_b128 v[164:167], %[vLd] offset:9216\n\t"
      "ds_read_b128 v[168:171], %[vLd] offset:10240\n\t"
      "ds_read_b128 v[172:175], %[vLd] offset:11264\n\t"
      "ds_read_b128 v[176:179], %[vLd] offset:12288\n\t"
      "ds_read_b128 v[180:183], %[vLd] offset:13312\n\t"
      "ds_read_b128 v[184:187], %[vLd] offset:14336\n\t"
      "ds_read_b128 v[188:191], %[vLd] offset:15360\n\t"
      "s_add_u32 s52, s76, 1\n\t"
      "s_mov_b32 s82, 1\n\t"
      "LLOOP_%=:\n\t"
      "s_and_b32 s69, s68, 3\n\t"
      "s_lshl_b32 s69, s69, 17\n\t"
      "s_add_u32 s70, %[oM], s69\n\t"
      "LWA0_%=:\n\t"
      "s_memrealtime s[56:57]\n\t"
      "s_waitcnt lgkmcnt(0)\n\t"
      "s_sub_u32 s69, s60, s56\n\t"
      "s_cmp_gt_i32 s69, 0\n\t"
      "s_cbranch_scc1 LWA0_%=\n\t"
      "buffer_load_dwordx4 v[40:43], %[vMw], %[rc], s70 offen sc1\n\t"
      "buffer_load_dwordx4 v[44:47], %[vMw], %[rc], s70 offen offset:1024 sc1\n\t"
      "s_add_u32 s69, s68, 1\n\t"
      "s_lshl_b32 s72, s69, 13\n\t"
      "s_lshl_b32 s73, s69, 15\n\t"
      "v_cmp_ge_i32 vcc, s52, v150\n\t"
      "s_cmp_lg_u64 vcc, 0\n\t"
      "s_cselect_b32 s72, 0x40000000, s72\n\t"
      "s_cselect_b32 s73, 0x40000000, s73\n\t"
      "s_add_u32 s74, s73, 4096\n\t"
      "s_lshr_b32 s69, s68, 3\n\t"
      "s_lshl_b32 s69, s69, 14\n\t"
      "s_and_b32 s62, s68, 7\n\t"
      "s_lshl_b32 s62, s62, 10\n\t"
      "s_add_u32 s62, s62, s69\n\t"
      "s_add_u32 s63, s62, %[oAn]\n\t"
      "s_add_u32 s69, s68, 1\n\t"
      "s_lshl_b32 s78, s55, 2\n\t"
      "s_cmp_gt_u32 s68, 60\n\t"
      "s_cselect_b32 s78, s78, 0\n\t"
      "s_add_u32 s78, s78, s68\n\t"
      "s_lshl_b32 s78, s78, 16\n\t"
      "s_sub_u32 s80, s78, 65536\n\t"
      "s_min_u32 s52, s69, 62\n\t"
      "s_add_u32 s52, s52, s76\n\t"
      "s_add_u32 s52, s52, 1\n\t"
      "s_add_u32 s69, s68, s76\n\t"
      "v_mov_b32 v151, s69\n\t"
      "s_waitcnt vmcnt(1)\n\t"
      "v_max3_u32 v144, v40, v41, v42\n\t"
      "v_max_u32 v144, v144, v43\n\t"
      "v_cmp_eq_u32 s[96:97], -1, v144\n\t"
      "s_waitcnt vmcnt(0)\n\t"
      "v_max3_u32 v147, v44, v45, v46\n\t"
      "v_max_u32 v147, v147, v47\n\t"
      "v_cmp_eq_u32 s[98:99], -1, v147\n\t"
      "s_or_b64 s[100:101], s[96:97], s[98:99]\n\t"
      "s_cbranch_scc1 LFA0_%=\n\t"
      "LGA0_%=:\n\t"
      "ds_write_b128 %[vWm], v[40:43]\n\t"
      "ds_write_b128 %[vWm], v[44:47] offset:1024\n\t"
      "ds_read_b128 v[136:139], v60 offset:0\n\t"
      "ds_read_b128 v[140:143], v60 offset:1024\n\t"
      "v_smfmac_f32_16x16x64_bf16 v[104:107], v[40:43], a[0:7], %[idx]\n\t"
      "v_smfmac_f32_16x16x64_bf16 v[108:111], v[40:43], a[128:135], %[idx]\n\t"
      "v_smfmac_f32_16x16x64_bf16 v[104:107], v[44:47], a[16:23], %[idx]\n\t"
      "buffer_load_dwordx4 v[152:155], %[vMw], %[rK], s72 offen offset:0 sc1\n\t"
      "v_smfmac_f32_16x16x64_bf16 v[108:111], v[44:47], a[144:151], %[idx]\n\t"
      "buffer_load_dwordx4 v[156:159], %[vMw], %[rK], s72 offen offset:1024 sc1\n\t"
      "s_waitcnt lgkmcnt(2)\n\t"
      "s_barrier\n\t"
      "ds_read_b128 v[132:135], %[vRm1] offset:0\n\t"
      "ds_read_b128 v[128:131], v52 offset:0\n\t"
      "s_waitcnt lgkmcnt(2)\n\t"
      "v_smfmac_f32_16x16x64_bf16 v[104:107], v[136:139], a[8:15], v55\n\t"
      "ds_read_b128 v[124:127], %[vRm1] offset:1024\n\t"
      "v_smfmac_f32_16x16x64_bf16 v[108:111], v[136:139], a[136:143], v55\n\t"
      "ds_read_b128 v[120:123], v52 offset:1024\n\t"
      "buffer_load_dwordx4 v[224:227], %[vU], %[rU], s73 offen offset:0 sc1\n\t"
      "v_smfmac_f32_16x16x64_bf16 v[104:107], v[140:143], a[24:31], v55\n\t"
      "ds_read_b128 v[72:75], %[vRm2] offset:0\n\t"
      "v_smfmac_f32_16x16x64_bf16 v[108:111], v[140:143], a[152:159], v55\n\t"
      "ds_read_b128 v[76:79], v53 offset:0\n\t"
      "s_waitcnt lgkmcnt(5)\n\t"
      "v_smfmac_f32_16x16x64_bf16 v[104:107], v[132:135], a[32:39], %[idx]\n\t"
      "ds_read_b128 v[80:83], %[vRm2] offset:1024\n\t"
      "buffer_load_dwordx4 v[228:231], %[vU], %[rU], s73 offen offset:1024 sc1\n\t"
      "v_smfmac_f32_16x16x64_bf16 v[108:111], v[132:135], a[160:167], %[idx]\n\t"
      "ds_read_b128 v[84:87], v53 offset:1024\n\t"
      "s_waitcnt lgkmcnt(6)\n\t"
      "v_smfmac_f32_16x16x64_bf16 v[104:107], v[128:131], a[40:47], v55\n\t"
      "ds_read_b128 v[88:91], %[vRm3] offset:0\n\t"
      "v_smfmac_f32_16x16x64_bf16 v[108:111], v[128:131], a[168:175], v55\n\t"
      "ds_read_b128 v[92:95], v54 offset:0\n\t"
      "buffer_load_dwordx4 v[232:235], %[vU], %[rU], s73 offen offset:2048 sc1\n\t"
      "s_waitcnt lgkmcnt(7)\n\t"
      "v_smfmac_f32_16x16x64_bf16 v[104:107], v[124:127], a[48:55], %[idx]\n\t"
      "ds_read_b128 v[96:99], %[vRm3] offset:1024\n\t"
      "v_smfmac_f32_16x16x64_bf16 v[108:111], v[124:127], a[176:183], %[idx]\n\t"
      "ds_read_b128 v[100:103], v54 offset:1024\n\t"
      "s_waitcnt lgkmcnt(8)\n\t"
      "v_smfmac_f32_16x16x64_bf16 v[104:107], v[120:123], a[56:63], v55\n\t"
      "buffer_load_dwordx4 v[236:239], %[vU], %[rU], s73 offen offset:3072 sc1\n\t"
      "v_smfmac_f32_16x16x64_bf16 v[108:111], v[120:123], a[184:191], v55\n\t"
      "s_waitcnt lgkmcnt(7)\n\t"
      "v_smfmac_f32_16x16x64_bf16 v[104:107], v[72:75], a[64:71], %[idx]\n\t"
      "s_bitcmp1_b32 s72, 30\n\t"
      "s_cbranch_scc1 LFD0_%=\n\t"
      "LFE0_%=:\n\t"
      "v_smfmac_f32_16x16x64_bf16 v[108:111], v[72:75], a[192:199], %[idx]\n\t"
      "buffer_store_dwordx4 v[48:51], %[kmo], %[rKM], s80 offen sc1\n\t"
      "s_waitcnt lgkmcnt(6)\n\t"
      "v_smfmac_f32_16x16x64_bf16 v[104:107], v[76:79], a[72:79], v55\n\t"
      "v_smfmac_f32_16x16x64_bf16 v[108:111], v[76:79], a[200:207], v55\n\t"
      "s_waitcnt lgkmcnt(5)\n\t"
      "v_smfmac_f32_16x16x64_bf16 v[104:107], v[80:83], a[80:87], %[idx]\n\t"
      "buffer_load_dwordx4 v[240:243], %[vU], %[rU], s74 offen offset:0 sc1\n\t"
      "v_smfmac_f32_16x16x64_bf16 v[108:111], v[80:83], a[208:215], %[idx]\n\t"
      "s_waitcnt lgkmcnt(4)\n\t"
      "v_smfmac_f32_16x16x64_bf16 v[104:107], v[84:87], a[88:95], v55\n\t"
      "v_smfmac_f32_16x16x64_bf16 v[108:111], v[84:87], a[216:223], v55\n\t"
      "buffer_load_dwordx4 v[244:247], %[vU], %[rU], s74 offen offset:1024 sc1\n\t"
      "s_waitcnt lgkmcnt(3)\n\t"
      "v_smfmac_f32_16x16x64_bf16 v[104:107], v[88:91], a[96:103], %[idx]\n\t"
      "v_smfmac_f32_16x16x64_bf16 v[108:111], v[88:91], a[224:231], %[idx]\n\t"
      "s_waitcnt lgkmcnt(2)\n\t"
      "v_smfmac_f32_16x16x64_bf16 v[104:107], v[92:95], a[104:111], v55\n\t"
      "v_smfmac_f32_16x16x64_bf16 v[108:111], v[92:95], a[232:239], v55\n\t"
      "s_waitcnt lgkmcnt(1)\n\t"
      "v_smfmac_f32_16x16x64_bf16 v[104:107], v[96:99], a[112:119], %[idx]\n\t"
      "s_waitcnt lgkmcnt(0)\n\t"
      "v_smfmac_f32_16x16x64_bf16 v[104:107], v[100:103], a[120:127], v55\n\t"
      "v_smfmac_f32_16x16x64_bf16 v[108:111], v[96:99], a[240:247], %[idx]\n\t"
      "v_smfmac_f32_16x16x64_bf16 v[108:111], v[100:103], a[248:255], v55\n\t"
      "s_nop 5\n\t"
      "v_cvt_pk_bf16_f32 v132, v104, v105\n\t"
      "v_cvt_pk_bf16_f32 v133, v106, v107\n\t"
      "v_cvt_pk_bf16_f32 v134, v108, v109\n\t"
      "v_cvt_pk_bf16_f32 v135, v110, v111\n\t"
      "ds_write2st64_b64 %[vWr], v[132:133], v[134:135] offset0:0 offset1:1\n\t"
      "s_memrealtime s[58:59]\n\t"
      "v_mov_b64 v[104:105], 0\n\t"
      "v_mov_b64 v[106:107], 0\n\t"
      "v_mov_b64 v[108:109], 0\n\t"
      "v_mov_b64 v[110:111], 0\n\t"
      "s_waitcnt lgkmcnt(0)\n\t"
      "s_barrier\n\t"
      "ds_read_b64_tr_b16 v[72:73], %[vRdF] offset:0\n\t"
      "ds_read_b64_tr_b16 v[74:75], %[vRdF] offset:512\n\t"
      "ds_read_b64_tr_b16 v[76:77], %[vRdF] offset:1024\n\t"
      "ds_read_b64_tr_b16 v[78:79], %[vRdF] offset:1536\n\t"
      "ds_read_b64_tr_b16 v[80:81], %[vRdF] offset:2048\n\t"
      "ds_read_b64_tr_b16 v[82:83], %[vRdF] offset:2560\n\t"
      "ds_read_b64_tr_b16 v[84:85], %[vRdF] offset:3072\n\t"
      "ds_read_b64_tr_b16 v[86:87], %[vRdF] offset:3584\n\t"
      "s_waitcnt lgkmcnt(6)\n\t"
      "v_mfma_f32_16x16x32_bf16 v[124:127], v[176:179], v[72:75], 0\n\t"
      "ds_read_b64_tr_b16 v[48:49], %[vRdK]\n\t"
      "v_mfma_f32_16x16x32_bf16 v[120:123], v[160:163], v[72:75], 0\n\t"
      "ds_read_b64_tr_b16 v[50:51], %[vRdK] offset:512\n\t"
      "v_mfma_f32_16x16x32_bf16 v[128:131], v[72:75], v[72:75], %[cinit]\n\t"
      "buffer_load_dwordx4 v[248:251], %[vU], %[rU], s74 offen offset:2048 sc1\n\t"
      "s_waitcnt lgkmcnt(6)\n\t"
      "v_mfma_f32_16x16x32_bf16 v[124:127], v[180:183], v[76:79], v[124:127]\n\t"
      "buffer_load_dwordx4 v[252:255], %[vU], %[rU], s74 offen offset:3072 sc1\n\t"
      "v_mfma_f32_16x16x32_bf16 v[120:123], v[164:167], v[76:79], v[120:123]\n\t"
      "v_mfma_f32_16x16x32_bf16 v[128:131], v[76:79], v[76:79], v[128:131]\n\t"
      "s_waitcnt lgkmcnt(4)\n\t"
      "v_mfma_f32_16x16x32_bf16 v[124:127], v[184:187], v[80:83], v[124:127]\n\t"
      "v_mfma_f32_16x16x32_bf16 v[120:123], v[168:171], v[80:83], v[120:123]\n\t"
      "s_waitcnt lgkmcnt(2)\n\t"
      "v_mfma_f32_16x16x32_bf16 v[124:127], v[188:191], v[84:87], v[124:127]\n\t"
      "v_mfma_f32_16x16x32_bf16 v[120:123], v[172:175], v[84:87], v[120:123]\n\t"
      "v_mfma_f32_16x16x32_bf16 v[128:131], v[80:83], v[80:83], v[128:131]\n\t"
      "s_nop 2\n\t"
      "v_mfma_f32_16x16x32_bf16 v[128:131], v[84:87], v[84:87], v[128:131]\n\t"
      "s_nop 2\n\t"
      "v_mov_b32_dpp v120, v124 row_ror:8 row_mask:0xf bank_mask:0xc\n\t"
      "v_mov_b32_dpp v121, v125 row_ror:8 row_mask:0xf bank_mask:0xc\n\t"
      "v_mov_b32_dpp v122, v126 row_ror:8 row_mask:0xf bank_mask:0xc\n\t"
      "v_mov_b32_dpp v123, v127 row_ror:8 row_mask:0xf bank_mask:0xc\n\t"
      "v_max3_f32 v145, v120, v121, v122\n\t"
      "v_mov_b32_dpp v128, v130 row_ror:8 row_mask:0xf bank_mask:0xc\n\t"
      "v_mov_b32_dpp v129, v131 row_ror:8 row_mask:0xf bank_mask:0xc\n\t"
      "v_max3_f32 v145, v145, v123, v128\n\t"
      "s_waitcnt lgkmcnt(0)\n\t"
      "v_max_f32 v145, v145, v129\n\t"
      "s_nop 1\n\t"
      "v_max_f32_dpp v145, v145, v145 row_ror:8 row_mask:0xf bank_mask:0xf\n\t"
      "v_mov_b32 v144, v145\n\t"
      "s_nop 1\n\t"
      "v_permlane16_swap_b32 v145, v144\n\t"
      "v_max_f32 v145, v145, v144\n\t"
      "v_mov_b32 v144, v145\n\t"
      "s_nop 1\n\t"
      "v_permlane32_swap_b32 v145, v144\n\t"
      "v_max_f32 v145, v145, v144\n\t"
      "v_mul_f32 v145, %[scl], v145\n\t"
      "v_fma_f32 v120, v120, %[scl], -v145\n\t"
      "v_fma_f32 v121, v121, %[scl], -v145\n\t"
      "v_exp_f32 v120, v120\n\t"
      "v_fma_f32 v122, v122, %[scl], -v145\n\t"
      "v_exp_f32 v121, v121\n\t"
      "v_fma_f32 v123, v123, %[scl], -v145\n\t"
      "v_exp_f32 v122, v122\n\t"
      "v_fma_f32 v128, v128, %[scl], -v145\n\t"
      "v_exp_f32 v123, v123\n\t"
      "v_fma_f32 v129, v129, %[scl], -v145\n\t"
      "v_exp_f32 v128, v128\n\t"
      "v_add_f32 v144, v120, v121\n\t"
      "v_exp_f32 v129, v129\n\t"
      "v_add_f32 v145, v122, v123\n\t"
      "v_add_f32 v144, v144, v128\n\t"
      "v_add_f32 v145, v145, v129\n\t"
      "v_add_f32 v144, v144, v145\n\t"
      "s_nop 1\n\t"
      "v_add_f32_dpp v144, v144, v144 row_ror:8 row_mask:0xf bank_mask:0xf\n\t"
      "v_mov_b32 v145, v144\n\t"
      "s_nop 1\n\t"
      "v_permlane16_swap_b32 v144, v145\n\t"
      "v_add_f32 v144, v144, v145\n\t"
      "v_mov_b32 v145, v144\n\t"
      "s_nop 1\n\t"
      "v_permlane32_swap_b32 v144, v145\n\t"
      "v_add_f32 v144, v144, v145\n\t"
      "v_rcp_f32 v144, v144\n\t"
      "s_nop 0\n\t"
      "v_mul_f32 v120, v120, v144\n\t"
      "v_mul_f32 v121, v121, v144\n\t"
      "v_mul_f32 v122, v122, v144\n\t"
      "v_mul_f32 v123, v123, v144\n\t"
      "v_cvt_pk_bf16_f32 v140, v120, v121\n\t"
      "v_cvt_pk_bf16_f32 v141, v122, v123\n\t"
      "v_cvt_pk_bf16_f32 v142, v120, v121\n\t"
      "v_cvt_pk_bf16_f32 v143, v122, v123\n\t"
      "v_mul_f32 v128, v128, v144\n\t"
      "v_mul_f32 v129, v129, v144\n\t"
      "v_mov_b32_dpp v142, v140 row_ror:8 row_mask:0xf bank_mask:0x3\n\t"
      "v_mov_b32_dpp v143, v141 row_ror:8 row_mask:0xf bank_mask:0x3\n\t"
      "v_mov_b32_dpp v140, v140 row_ror:8 row_mask:0xf bank_mask:0xc\n\t"
      "v_mov_b32_dpp v141, v141 row_ror:8 row_mask:0xf bank_mask:0xc\n\t"
      "v_cvt_pk_bf16_f32 v56, v128, v129\n\t"
      "v_cvt_pk_bf16_f32 v58, v128, v129\n\t"
      "v_smfmac_f32_16x16x64_bf16 v[104:107], v[140:143], v[192:199], %[idx]\n\t"
      "v_cmp_ge_i32 vcc, s52, v150\n\t"
      "v_permlane16_swap_b32 v56, v58\n\t"
      "v_smfmac_f32_16x16x64_bf16 v[108:111], v[140:143], v[200:207], %[idx]\n\t"
      "s_nop 0\n\t"
      "v_mov_b32_dpp v57, v56 row_ror:8 row_mask:0xf bank_mask:0xf\n\t"
      "v_smfmac_f32_16x16x64_bf16 v[112:115], v[140:143], v[208:215], %[idx]\n\t"
      "v_mov_b32_dpp v59, v58 row_ror:8 row_mask:0xf bank_mask:0xf\n\t"
      "buffer_store_dwordx4 v[56:59], %[po], %[rc], 0 offen sc1\n\t"
      "v_smfmac_f32_16x16x64_bf16 v[116:119], v[140:143], v[216:223], %[idx]\n\t"
      "s_nop 1\n\t"
      "v_cvt_pk_bf16_f32 v132, v104, v105\n\t"
      "v_cvt_pk_bf16_f32 v133, v106, v107\n\t"
      "v_cvt_pk_bf16_f32 v134, v108, v109\n\t"
      "v_cvt_pk_bf16_f32 v135, v110, v111\n\t"
      "v_cvt_pk_bf16_f32 v136, v112, v113\n\t"
      "v_cvt_pk_bf16_f32 v137, v114, v115\n\t"
      "v_cvt_pk_bf16_f32 v138, v116, v117\n\t"
      "v_cvt_pk_bf16_f32 v139, v118, v119\n\t"
      "v_permlane16_swap_b32 v132, v134\n\t"
      "v_permlane16_swap_b32 v133, v135\n\t"
      "buffer_store_dwordx4 v[132:135], %[xo0], %[rc], 0 offen sc1\n\t"
      "v_permlane16_swap_b32 v136, v138\n\t"
      "v_permlane16_swap_b32 v137, v139\n\t"
      "buffer_store_dwordx4 v[136:139], %[xo1], %[rc], 0 offen sc1\n\t"
      "v_mov_b64 v[104:105], 0\n\t"
      "v_mov_b64 v[106:107], 0\n\t"
      "v_mov_b64 v[108:109], 0\n\t"
      "v_mov_b64 v[110:111], 0\n\t"
      "v_mov_b64 v[112:113], 0\n\t"
      "v_mov_b64 v[114:115], 0\n\t"
      "v_mov_b64 v[116:117], 0\n\t"
      "v_mov_b64 v[118:119], 0\n\t"
      "s_bitcmp1_b32 s72, 30\n\t"
      "s_cbranch_scc1 LSK0_%=\n\t"
      "s_waitcnt vmcnt(12)\n\t"
      "LSR0_%=:\n\t"
      "ds_write_b128 %[vWm], v[152:155] offset:8192\n\t"
      "ds_write_b128 %[vWm], v[156:159] offset:9216\n\t"
      "s_waitcnt lgkmcnt(0)\n\t"
      "s_barrier\n\t"
      "ds_read_b128 v[160:163], %[vLd] offset:8192\n\t"
      "ds_read_b128 v[164:167], %[vLd] offset:9216\n\t"
      "ds_read_b128 v[168:171], %[vLd] offset:10240\n\t"
      "ds_read_b128 v[172:175], %[vLd] offset:11264\n\t"
      "ds_read_b128 v[176:179], %[vLd] offset:12288\n\t"
      "ds_read_b128 v[180:183], %[vLd] offset:13312\n\t"
      "ds_read_b128 v[184:187], %[vLd] offset:14336\n\t"
      "ds_read_b128 v[188:191], %[vLd] offset:15360\n\t"
      "s_cbranch_vccnz LFK0_%=\n\t"
      "LFR0_%=:\n\t"
      "buffer_load_dwordx2 v[62:63], off, %[rc], s63 sc1\n\t"
      "buffer_store_dword v151, %[vFc], %[rc], 0 offen sc1\n\t"
      "s_waitcnt vmcnt(1)\n\t"
      "LTA0_%=:\n\t"
      "v_readfirstlane_b32 s61, v62\n\t"
      "v_readfirstlane_b32 s56, v63\n\t"
      "s_sub_u32 s58, s58, 54\n\t"
      "s_sub_u32 s69, s58, s60\n\t"
      "s_cmp_gt_i32 s69, 12\n\t"
      "s_cselect_b32 s60, s58, s60\n\t"
      "s_add_u32 s69, s68, %[tag]\n\t"
      "s_cmp_eq_u32 s69, s56\n\t"
      "s_cselect_b32 s61, s61, s60\n\t"
      "s_sub_u32 s69, s61, s60\n\t"
      "s_add_u32 s69, s69, 400\n\t"
      "s_cmp_lt_u32 s69, 800\n\t"
      "s_cselect_b32 s60, s61, s60\n\t"
      "s_add_u32 s69, s55, 2\n\t"
      "s_cmp_lt_u32 s68, s69\n\t"
      "s_cselect_b32 s69, 30, 0\n\t"
      "s_add_u32 s60, s60, s69\n\t"
      "s_add_u32 s60, s60, 169\n\t"
      "s_add_u32 s68, s68, 1\n\t"
      "s_and_b32 s69, s68, 3\n\t"
      "s_lshl_b32 s69, s69, 17\n\t"
      "s_add_u32 s70, %[oM], s69\n\t"
      "LWA1_%=:\n\t"
      "s_memrealtime s[56:57]\n\t"
      "s_waitcnt lgkmcnt(0)\n\t"
      "s_sub_u32 s69, s60, s56\n\t"
      "s_cmp_gt_i32 s69, 0\n\t"
      "s_cbranch_scc1 LWA1_%=\n\t"
      "buffer_load_dwordx4 v[40:43], %[vMw], %[rc], s70 offen sc1\n\t"
      "buffer_load_dwordx4 v[44:47], %[vMw], %[rc], s70 offen offset:1024 sc1\n\t"
      "s_add_u32 s69, s68, 1\n\t"
      "s_lshl_b32 s72, s69, 13\n\t"
      "s_lshl_b32 s73, s69, 15\n\t"
      "v_cmp_ge_i32 vcc, s52, v150\n\t"
      "s_cmp_lg_u64 vcc, 0\n\t"
      "s_cselect_b32 s72, 0x40000000, s72\n\t"
      "s_cselect_b32 s73, 0x40000000, s73\n\t"
      "s_add_u32 s74, s73, 4096\n\t"
      "s_lshr_b32 s69, s68, 3\n\t"
      "s_lshl_b32 s69, s69, 14\n\t"
      "s_and_b32 s62, s68, 7\n\t"
      "s_lshl_b32 s62, s62, 10\n\t"
      "s_add_u32 s62, s62, s69\n\t"
      "s_add_u32 s63, s62, %[oAn]\n\t"
      "s_add_u32 s69, s68, 1\n\t"
      "s_lshl_b32 s78, s55, 2\n\t"
      "s_cmp_gt_u32 s68, 60\n\t"
      "s_cselect_b32 s78, s78, 0\n\t"
      "s_add_u32 s78, s78, s68\n\t"
      "s_lshl_b32 s78, s78, 16\n\t"
      "s_sub_u32 s80, s78, 65536\n\t"
      "s_min_u32 s52, s69, 62\n\t"
      "s_add_u32 s52, s52, s76\n\t"
      "s_add_u32 s52, s52, 1\n\t"
      "s_add_u32 s69, s68, s76\n\t"
      "v_mov_b32 v151, s69\n\t"
      "s_waitcnt vmcnt(1)\n\t"
      "v_max3_u32 v144, v40, v41, v42\n\t"
      "v_max_u32 v144, v144, v43\n\t"
      "v_cmp_eq_u32 s[96:97], -1, v144\n\t"
      "s_waitcnt vmcnt(0)\n\t"
      "v_max3_u32 v147, v44, v45, v46\n\t"
      "v_max_u32 v147, v147, v47\n\t"
      "v_cmp_eq_u32 s[98:99], -1, v147\n\t"
      "s_or_b64 s[100:101], s[96:97], s[98:99]\n\t"
      "s_cbranch_scc1 LFA1_%=\n\t"
      "LGA1_%=:\n\t"
      "ds_write_b128 %[vWm], v[40:43]\n\t"
      "ds_write_b128 %[vWm], v[44:47] offset:1024\n\t"
      "ds_read_b128 v[136:139], v60 offset:0\n\t"
      "ds_read_b128 v[140:143], v60 offset:1024\n\t"
      "v_smfmac_f32_16x16x64_bf16 v[104:107], v[40:43], a[0:7], %[idx]\n\t"
      "v_smfmac_f32_16x16x64_bf16 v[108:111], v[40:43], a[128:135], %[idx]\n\t"
      "v_smfmac_f32_16x16x64_bf16 v[104:107], v[44:47], a[16:23], %[idx]\n\t"
      "buffer_load_dwordx4 v[152:155], %[vMw], %[rK], s72 offen offset:0 sc1\n\t"
      "v_smfmac_f32_16x16x64_bf16 v[108:111], v[44:47], a[144:151], %[idx]\n\t"
      "buffer_load_dwordx4 v[156:159], %[vMw], %[rK], s72 offen offset:1024 sc1\n\t"
      "s_waitcnt lgkmcnt(2)\n\t"
      "s_barrier\n\t"
      "ds_read_b128 v[132:135], %[vRm1] offset:0\n\t"
      "ds_read_b128 v[128:131], v52 offset:0\n\t"
      "s_waitcnt lgkmcnt(2)\n\t"
      "v_smfmac_f32_16x16x64_bf16 v[104:107], v[136:139], a[8:15], v55\n\t"
      "ds_read_b128 v[124:127], %[vRm1] offset:1024\n\t"
      "v_smfmac_f32_16x16x64_bf16 v[108:111], v[136:139], a[136:143], v55\n\t"
      "ds_read_b128 v[120:123], v52 offset:1024\n\t"
      "buffer_load_dwordx4 v[192:195], %[vU], %[rU], s73 offen offset:0 sc1\n\t"
      "v_smfmac_f32_16x16x64_bf16 v[104:107], v[140:143], a[24:31], v55\n\t"
      "ds_read_b128 v[72:75], %[vRm2] offset:0\n\t"
      "v_smfmac_f32_16x16x64_bf16 v[108:111], v[140:143], a[152:159], v55\n\t"
      "ds_read_b128 v[76:79], v53 offset:0\n\t"
      "s_waitcnt lgkmcnt(5)\n\t"
      "v_smfmac_f32_16x16x64_bf16 v[104:107], v[132:135], a[32:39], %[idx]\n\t"
      "ds_read_b128 v[80:83], %[vRm2] offset:1024\n\t"
      "buffer_load_dwordx4 v[196:199], %[vU], %[rU], s73 offen offset:1024 sc1\n\t"
      "v_smfmac_f32_16x16x64_bf16 v[108:111], v[132:135], a[160:167], %[idx]\n\t"
      "ds_read_b128 v[84:87], v53 offset:1024\n\t"
      "s_waitcnt lgkmcnt(6)\n\t"
      "v_smfmac_f32_16x16x64_bf16 v[104:107], v[128:131], a[40:47], v55\n\t"
      "ds_read_b128 v[88:91], %[vRm3] offset:0\n\t"
      "v_smfmac_f32_16x16x64_bf16 v[108:111], v[128:131], a[168:175], v55\n\t"
      "ds_read_b128 v[92:95], v54 offset:0\n\t"
      "buffer_load_dwordx4 v[200:203], %[vU], %[rU], s73 offen offset:2048 sc1\n\t"
      "s_waitcnt lgkmcnt(7)\n\t"
      "v_smfmac_f32_16x16x64_bf16 v[104:107], v[124:127], a[48:55], %[idx]\n\t"
      "ds_read_b128 v[96:99], %[vRm3] offset:1024\n\t"
      "v_smfmac_f32_16x16x64_bf16 v[108:111], v[124:127], a[176:183], %[idx]\n\t"
      "ds_read_b128 v[100:103], v54 offset:1024\n\t"
      "s_waitcnt lgkmcnt(8)\n\t"
      "v_smfmac_f32_16x16x64_bf16 v[104:107], v[120:123], a[56:63], v55\n\t"
      "buffer_load_dwordx4 v[204:207], %[vU], %[rU], s73 offen offset:3072 sc1\n\t"
      "v_smfmac_f32_16x16x64_bf16 v[108:111], v[120:123], a[184:191], v55\n\t"
      "s_waitcnt lgkmcnt(7)\n\t"
      "v_smfmac_f32_16x16x64_bf16 v[104:107], v[72:75], a[64:71], %[idx]\n\t"
      "s_bitcmp1_b32 s72, 30\n\t"
      "s_cbranch_scc1 LFD1_%=\n\t"
      "LFE1_%=:\n\t"
      "v_smfmac_f32_16x16x64_bf16 v[108:111], v[72:75], a[192:199], %[idx]\n\t"
      "buffer_store_dwordx4 v[48:51], %[kmo], %[rKM], s80 offen sc1\n\t"
      "s_waitcnt lgkmcnt(6)\n\t"
      "v_smfmac_f32_16x16x64_bf16 v[104:107], v[76:79], a[72:79], v55\n\t"
      "v_smfmac_f32_16x16x64_bf16 v[108:111], v[76:79], a[200:207], v55\n\t"
      "s_waitcnt lgkmcnt(5)\n\t"
      "v_smfmac_f32_16x16x64_bf16 v[104:107], v[80:83], a[80:87], %[idx]\n\t"
      "buffer_load_dwordx4 v[208:211], %[vU], %[rU], s74 offen offset:0 sc1\n\t"
      "v_smfmac_f32_16x16x64_bf16 v[108:111], v[80:83], a[208:215], %[idx]\n\t"
      "s_waitcnt lgkmcnt(4)\n\t"
      "v_smfmac_f32_16x16x64_bf16 v[104:107], v[84:87], a[88:95], v55\n\t"
      "v_smfmac_f32_16x16x64_bf16 v[108:111], v[84:87], a[216:223], v55\n\t"
      "buffer_load_dwordx4 v[212:215], %[vU], %[rU], s74 offen offset:1024 sc1\n\t"
      "s_waitcnt lgkmcnt(3)\n\t"
      "v_smfmac_f32_16x16x64_bf16 v[104:107], v[88:91], a[96:103], %[idx]\n\t"
      "v_smfmac_f32_16x16x64_bf16 v[108:111], v[88:91], a[224:231], %[idx]\n\t"
      "s_waitcnt lgkmcnt(2)\n\t"
      "v_smfmac_f32_16x16x64_bf16 v[104:107], v[92:95], a[104:111], v55\n\t"
      "v_smfmac_f32_16x16x64_bf16 v[108:111], v[92:95], a[232:239], v55\n\t"
      "s_waitcnt lgkmcnt(1)\n\t"
      "v_smfmac_f32_16x16x64_bf16 v[104:107], v[96:99], a[112:119], %[idx]\n\t"
      "s_waitcnt lgkmcnt(0)\n\t"
      "v_smfmac_f32_16x16x64_bf16 v[104:107], v[100:103], a[120:127], v55\n\t"
      "v_smfmac_f32_16x16x64_bf16 v[108:111], v[96:99], a[240:247], %[idx]\n\t"
      "v_smfmac_f32_16x16x64_bf16 v[108:111], v[100:103], a[248:255], v55\n\t"
      "s_nop 5\n\t"
      "v_cvt_pk_bf16_f32 v132, v104, v105\n\t"
      "v_cvt_pk_bf16_f32 v133, v106, v107\n\t"
      "v_cvt_pk_bf16_f32 v134, v108, v109\n\t"
      "v_cvt_pk_bf16_f32 v135, v110, v111\n\t"
      "ds_write2st64_b64 %[vWr], v[132:133], v[134:135] offset0:0 offset1:1\n\t"
      "s_memrealtime s[58:59]\n\t"
      "v_mov_b64 v[104:105], 0\n\t"
      "v_mov_b64 v[106:107], 0\n\t"
      "v_mov_b64 v[108:109], 0\n\t"
      "v_mov_b64 v[110:111], 0\n\t"
      "s_waitcnt lgkmcnt(0)\n\t"
      "s_barrier\n\t"
      "s_cmp_eq_u32 s68, 63\n\t"
      "s_cbranch_scc1 LKM1_%=\n\t"
      "ds_read_b64_tr_b16 v[72:73], %[vRdF] offset:0\n\t"
      "ds_read_b64_tr_b16 v[74:75], %[vRdF] offset:512\n\t"
      "ds_read_b64_tr_b16 v[76:77], %[vRdF] offset:1024\n\t"
      "ds_read_b64_tr_b16 v[78:79], %[vRdF] offset:1536\n\t"
      "ds_read_b64_tr_b16 v[80:81], %[vRdF] offset:2048\n\t"
      "ds_read_b64_tr_b16 v[82:83], %[vRdF] offset:2560\n\t"
      "ds_read_b64_tr_b16 v[84:85], %[vRdF] offset:3072\n\t"
      "ds_read_b64_tr_b16 v[86:87], %[vRdF] offset:3584\n\t"
      "s_waitcnt lgkmcnt(6)\n\t"
      "v_mfma_f32_16x16x32_bf16 v[124:127], v[176:179], v[72:75], 0\n\t"
      "ds_read_b64_tr_b16 v[48:49], %[vRdK]\n\t"
      "v_mfma_f32_16x16x32_bf16 v[120:123], v[160:163], v[72:75], 0\n\t"
      "ds_read_b64_tr_b16 v[50:51], %[vRdK] offset:512\n\t"
      "v_mfma_f32_16x16x32_bf16 v[128:131], v[72:75], v[72:75], %[cinit]\n\t"
      "buffer_load_dwordx4 v[216:219], %[vU], %[rU], s74 offen offset:2048 sc1\n\t"
      "s_waitcnt lgkmcnt(6)\n\t"
      "v_mfma_f32_16x16x32_bf16 v[124:127], v[180:183], v[76:79], v[124:127]\n\t"
      "buffer_load_dwordx4 v[220:223], %[vU], %[rU], s74 offen offset:3072 sc1\n\t"
      "v_mfma_f32_16x16x32_bf16 v[120:123], v[164:167], v[76:79], v[120:123]\n\t"
      "v_mfma_f32_16x16x32_bf16 v[128:131], v[76:79], v[76:79], v[128:131]\n\t"
      "s_waitcnt lgkmcnt(4)\n\t"
      "v_mfma_f32_16x16x32_bf16 v[124:127], v[184:187], v[80:83], v[124:127]\n\t"
      "v_mfma_f32_16x16x32_bf16 v[120:123], v[168:171], v[80:83], v[120:123]\n\t"
      "s_waitcnt lgkmcnt(2)\n\t"
      "v_mfma_f32_16x16x32_bf16 v[124:127], v[188:191], v[84:87], v[124:127]\n\t"
      "v_mfma_f32_16x16x32_bf16 v[120:123], v[172:175], v[84:87], v[120:123]\n\t"
      "v_mfma_f32_16x16x32_bf16 v[128:131], v[80:83], v[80:83], v[128:131]\n\t"
      "s_nop 2\n\t"
      "v_mfma_f32_16x16x32_bf16 v[128:131], v[84:87], v[84:87], v[128:131]\n\t"
      "s_nop 2\n\t"
      "v_mov_b32_dpp v120, v124 row_ror:8 row_mask:0xf bank_mask:0xc\n\t"
      "v_mov_b32_dpp v121, v125 row_ror:8 row_mask:0xf bank_mask:0xc\n\t"
      "v_mov_b32_dpp v122, v126 row_ror:8 row_mask:0xf bank_mask:0xc\n\t"
      "v_mov_b32_dpp v123, v127 row_ror:8 row_mask:0xf bank_mask:0xc\n\t"
      "v_max3_f32 v145, v120, v121, v122\n\t"
      "v_mov_b32_dpp v128, v130 row_ror:8 row_mask:0xf bank_mask:0xc\n\t"
      "v_mov_b32_dpp v129, v131 row_ror:8 row_mask:0xf bank_mask:0xc\n\t"
      "v_max3_f32 v145, v145, v123, v128\n\t"
      "s_waitcnt lgkmcnt(0)\n\t"
      "v_max_f32 v145, v145, v129\n\t"
      "s_nop 1\n\t"
      "v_max_f32_dpp v145, v145, v145 row_ror:8 row_mask:0xf bank_mask:0xf\n\t"
      "v_mov_b32 v144, v145\n\t"
      "s_nop 1\n\t"
      "v_permlane16_swap_b32 v145, v144\n\t"
      "v_max_f32 v145, v145, v144\n\t"
      "v_mov_b32 v144, v145\n\t"
      "s_nop 1\n\t"
      "v_permlane32_swap_b32 v145, v144\n\t"
      "v_max_f32 v145, v145, v144\n\t"
      "v_mul_f32 v145, %[scl], v145\n\t"
      "v_fma_f32 v120, v120, %[scl], -v145\n\t"
      "v_fma_f32 v121, v121, %[scl], -v145\n\t"
      "v_exp_f32 v120, v120\n\t"
      "v_fma_f32 v122, v122, %[scl], -v145\n\t"
      "v_exp_f32 v121, v121\n\t"
      "v_fma_f32 v123, v123, %[scl], -v145\n\t"
      "v_exp_f32 v122, v122\n\t"
      "v_fma_f32 v128, v128, %[scl], -v145\n\t"
      "v_exp_f32 v123, v123\n\t"
      "v_fma_f32 v129, v129, %[scl], -v145\n\t"
      "v_exp_f32 v128, v128\n\t"
      "v_add_f32 v144, v120, v121\n\t"
      "v_exp_f32 v129, v129\n\t"
      "v_add_f32 v145, v122, v123\n\t"
      "v_add_f32 v144, v144, v128\n\t"
      "v_add_f32 v145, v145, v129\n\t"
      "v_add_f32 v144, v144, v145\n\t"
      "s_nop 1\n\t"
      "v_add_f32_dpp v144, v144, v144 row_ror:8 row_mask:0xf bank_mask:0xf\n\t"
      "v_mov_b32 v145, v144\n\t"
      "s_nop 1\n\t"
      "v_permlane16_swap_b32 v144, v145\n\t"
      "v_add_f32 v144, v144, v145\n\t"
      "v_mov_b32 v145, v144\n\t"
      "s_nop 1\n\t"
      "v_permlane32_swap_b32 v144, v145\n\t"
      "v_add_f32 v144, v144, v145\n\t"
      "v_rcp_f32 v144, v144\n\t"
      "s_nop 0\n\t"
      "v_mul_f32 v120, v120, v144\n\t"
      "v_mul_f32 v121, v121, v144\n\t"
      "v_mul_f32 v122, v122, v144\n\t"
      "v_mul_f32 v123, v123, v144\n\t"
      "v_cvt_pk_bf16_f32 v140, v120, v121\n\t"
      "v_cvt_pk_bf16_f32 v141, v122, v123\n\t"
      "v_cvt_pk_bf16_f32 v142, v120, v121\n\t"
      "v_cvt_pk_bf16_f32 v143, v122, v123\n\t"
      "v_mul_f32 v128, v128, v144\n\t"
      "v_mul_f32 v129, v129, v144\n\t"
      "v_mov_b32_dpp v142, v140 row_ror:8 row_mask:0xf bank_mask:0x3\n\t"
      "v_mov_b32_dpp v143, v141 row_ror:8 row_mask:0xf bank_mask:0x3\n\t"
      "v_mov_b32_dpp v140, v140 row_ror:8 row_mask:0xf bank_mask:0xc\n\t"
      "v_mov_b32_dpp v141, v141 row_ror:8 row_mask:0xf bank_mask:0xc\n\t"
      "v_cvt_pk_bf16_f32 v56, v128, v129\n\t"
      "v_cvt_pk_bf16_f32 v58, v128, v129\n\t"
      "v_smfmac_f32_16x16x64_bf16 v[104:107], v[140:143], v[224:231], %[idx]\n\t"
      "v_cmp_ge_i32 vcc, s52, v150\n\t"
      "v_permlane16_swap_b32 v56, v58\n\t"
      "v_smfmac_f32_16x16x64_bf16 v[108:111], v[140:143], v[232:239], %[idx]\n\t"
      "s_nop 0\n\t"
      "v_mov_b32_dpp v57, v56 row_ror:8 row_mask:0xf bank_mask:0xf\n\t"
      "v_smfmac_f32_16x16x64_bf16 v[112:115], v[140:143], v[240:247], %[idx]\n\t"
      "v_mov_b32_dpp v59, v58 row_ror:8 row_mask:0xf bank_mask:0xf\n\t"
      "buffer_store_dwordx4 v[56:59], %[po], %[rc], s79 offen sc1\n\t"
      "v_smfmac_f32_16x16x64_bf16 v[116:119], v[140:143], v[248:255], %[idx]\n\t"
      "s_nop 1\n\t"
      "v_cvt_pk_bf16_f32 v132, v104, v105\n\t"
      "v_cvt_pk_bf16_f32 v133, v106, v107\n\t"
      "v_cvt_pk_bf16_f32 v134, v108, v109\n\t"
      "v_cvt_pk_bf16_f32 v135, v110, v111\n\t"
      "v_cvt_pk_bf16_f32 v136, v112, v113\n\t"
      "v_cvt_pk_bf16_f32 v137, v114, v115\n\t"
      "v_cvt_pk_bf16_f32 v138, v116, v117\n\t"
      "v_cvt_pk_bf16_f32 v139, v118, v119\n\t"
      "v_permlane16_swap_b32 v132, v134\n\t"
      "v_permlane16_swap_b32 v133, v135\n\t"
      "buffer_store_dwordx4 v[132:135], %[xo0], %[rc], s77 offen sc1\n\t"
      "v_permlane16_swap_b32 v136, v138\n\t"
      "v_permlane16_swap_b32 v137, v139\n\t"
      "buffer_store_dwordx4 v[136:139], %[xo1], %[rc], s77 offen sc1\n\t"
      "v_mov_b64 v[104:105], 0\n\t"
      "v_mov_b64 v[106:107], 0\n\t"
      "v_mov_b64 v[108:109], 0\n\t"
      "v_mov_b64 v[110:111], 0\n\t"
      "v_mov_b64 v[112:113], 0\n\t"
      "v_mov_b64 v[114:115], 0\n\t"
      "v_mov_b64 v[116:117], 0\n\t"
      "v_mov_b64 v[118:119], 0\n\t"
      "s_bitcmp1_b32 s72, 30\n\t"
      "s_cbranch_scc1 LSK1_%=\n\t"
      "s_waitcnt vmcnt(12)\n\t"
      "LSR1_%=:\n\t"
      "ds_write_b128 %[vWm], v[152:155] offset:8192\n\t"
      "ds_write_b128 %[vWm], v[156:159] offset:9216\n\t"
      "s_waitcnt lgkmcnt(0)\n\t"
      "s_barrier\n\t"
      "ds_read_b128 v[160:163], %[vLd] offset:8192\n\t"
      "ds_read_b128 v[164:167], %[vLd] offset:9216\n\t"
      "ds_read_b128 v[168:171], %[vLd] offset:10240\n\t"
      "ds_read_b128 v[172:175], %[vLd] offset:11264\n\t"
      "ds_read_b128 v[176:179], %[vLd] offset:12288\n\t"
      "ds_read_b128 v[180:183], %[vLd] offset:13312\n\t"
      "ds_read_b128 v[184:187], %[vLd] offset:14336\n\t"
      "ds_read_b128 v[188:191], %[vLd] offset:15360\n\t"
      "s_cbranch_vccnz LFK1_%=\n\t"
      "LFR1_%=:\n\t"
      "buffer_load_dwordx2 v[62:63], off, %[rc], s63 sc1\n\t"
      "buffer_store_dword v151, %[vFc], %[rc], 0 offen sc1\n\t"
      "s_waitcnt vmcnt(1)\n\t"
      "LTA1_%=:\n\t"
      "v_readfirstlane_b32 s61, v62\n\t"
      "v_readfirstlane_b32 s56, v63\n\t"
      "s_sub_u32 s58, s58, 54\n\t"
      "s_sub_u32 s69, s58, s60\n\t"
      "s_cmp_gt_i32 s69, 12\n\t"
      "s_cselect_b32 s60, s58, s60\n\t"
      "s_add_u32 s69, s68, %[tag]\n\t"
      "s_cmp_eq_u32 s69, s56\n\t"
      "s_cselect_b32 s61, s61, s60\n\t"
      "s_sub_u32 s69, s61, s60\n\t"
      "s_add_u32 s69, s69, 400\n\t"
      "s_cmp_lt_u32 s69, 800\n\t"
      "s_cselect_b32 s60, s61, s60\n\t"
      "s_add_u32 s69, s55, 2\n\t"
      "s_cmp_lt_u32 s68, s69\n\t"
      "s_cselect_b32 s69, 30, 0\n\t"
      "s_add_u32 s60, s60, s69\n\t"
      "s_add_u32 s60, s60, 169\n\t"
      "s_add_u32 s68, s68, 1\n\t"
      "s_branch LLOOP_%=\n\t"
      "LKM1_%=:\n\t"
      "ds_read_b64_tr_b16 v[48:49], %[vRdK]\n\t"
      "ds_read_b64_tr_b16 v[50:51], %[vRdK] offset:512\n\t"
      "s_waitcnt lgkmcnt(0)\n\t"
      "buffer_store_dwordx4 v[48:51], %[kmo], %[rKM], s78 offen sc1\n\t"
      "s_add_u32 s69, s76, 64\n\t"
      "v_mov_b32 v151, s69\n\t"
      "s_waitcnt vmcnt(0)\n\t"
      "s_barrier\n\t"
      "buffer_store_dword v151, %[vFc], %[rc], 0 offen sc1\n\t"
      "s_cmp_eq_u32 s55, 1\n\t"
      "s_cbranch_scc1 LDONE_%=\n\t"
      "s_mov_b32 s55, 1\n\t"
      "s_xor_b32 %[oM], %[oM], %[dR]\n\t"
      "s_xor_b32 %[oAn], %[oAn], %[dR]\n\t"
      "s_add_u32 %[tag], %[tag], 256\n\t"
      "s_branch LLAYER_%=\n\t"
      "LDONE_%=:\n\t"
      "s_mov_b32 %[st], 0\n\t"
      "s_branch LEND_%=\n\t"
      "LFD0_%=:\n\t"
      "buffer_load_dword v150, %[vFk], %[rc], 0 offen sc1\n\t"
      "s_branch LFE0_%=\n\t"
      "LFA0_%=:\n\t"
      "s_mov_b64 exec, s[96:97]\n\t"
      "buffer_load_dwordx4 v[40:43], %[vMw], %[rc], s70 offen sc1\n\t"
      "s_mov_b64 exec, s[98:99]\n\t"
      "buffer_load_dwordx4 v[44:47], %[vMw], %[rc], s70 offen offset:1024 sc1\n\t"
      "s_mov_b64 exec, -1\n\t"
      "s_sleep 1\n\t"
      "s_mov_b64 exec, s[96:97]\n\t"
      "buffer_load_dwordx4 v[40:43], %[vMw], %[rc], s70 offen sc1\n\t"
      "s_mov_b64 exec, s[98:99]\n\t"
      "buffer_load_dwordx4 v[44:47], %[vMw], %[rc], s70 offen offset:1024 sc1\n\t"
      "s_mov_b64 exec, -1\n\t"
      "s_sleep 1\n\t"
      "LHA0_%=:\n\t"
      "s_mov_b64 exec, s[96:97]\n\t"
      "buffer_load_dwordx4 v[40:43], %[vMw], %[rc], s70 offen sc1\n\t"
      "s_mov_b64 exec, s[98:99]\n\t"
      "buffer_load_dwordx4 v[44:47], %[vMw], %[rc], s70 offen offset:1024 sc1\n\t"
      "s_mov_b64 exec, -1\n\t"
      "s_add_u32 s71, s71, 1\n\t"
      "s_cmp_gt_u32 s71, 131072\n\t"
      "s_cbranch_scc1 LFAIL_%=\n\t"
      "s_waitcnt vmcnt(5)\n\t"
      "v_max3_u32 v144, v40, v41, v42\n\t"
      "v_max_u32 v144, v144, v43\n\t"
      "v_cmp_eq_u32 s[96:97], -1, v144\n\t"
      "s_waitcnt vmcnt(4)\n\t"
      "v_max3_u32 v147, v44, v45, v46\n\t"
      "v_max_u32 v147, v147, v47\n\t"
      "v_cmp_eq_u32 s[98:99], -1, v147\n\t"
      "s_or_b64 s[100:101], s[96:97], s[98:99]\n\t"
      "s_cbranch_scc1 LHA0_%=\n\t"
      "s_branch LGA0_%=\n\t"
      "LFK0_%=:\n\t"
      "buffer_load_dword v150, %[vFk], %[rc], 0 offen sc1\n\t"
      "s_branch LFR0_%=\n\t"
      "LSK0_%=:\n\t"
      "s_waitcnt vmcnt(3)\n\t"
      "s_branch LSC0_%=\n\t"
      "LSL0_%=:\n\t"
      "buffer_load_dword v150, %[vFk], %[rc], 0 offen sc1\n\t"
      "s_add_u32 s71, s71, 1\n\t"
      "s_cmp_gt_u32 s71, 131072\n\t"
      "s_cbranch_scc1 LFAIL_%=\n\t"
      "s_waitcnt vmcnt(0)\n\t"
      "LSC0_%=:\n\t"
      "s_add_u32 s69, s68, 1\n\t"
      "s_lshl_b32 s72, s69, 13\n\t"
      "s_lshl_b32 s73, s69, 15\n\t"
      "s_add_u32 s74, s73, 4096\n\t"
      "s_add_u32 s69, s69, s76\n\t"
      "v_cmp_ge_i32 vcc, s69, v150\n\t"
      "s_cbranch_vccnz LSL0_%=\n\t"
      "buffer_load_dwordx4 v[152:155], %[vMw], %[rK], s72 offen offset:0 sc1\n\t"
      "buffer_load_dwordx4 v[156:159], %[vMw], %[rK], s72 offen offset:1024 sc1\n\t"
      "buffer_load_dwordx2 v[62:63], off, %[rc], s63 sc1\n\t"
      "buffer_load_dword v150, %[vFk], %[rc], 0 offen sc1\n\t"
      "buffer_load_dwordx4 v[224:227], %[vU], %[rU], s73 offen offset:0 sc1\n\t"
      "buffer_load_dwordx4 v[228:231], %[vU], %[rU], s73 offen offset:1024 sc1\n\t"
      "buffer_load_dwordx4 v[232:235], %[vU], %[rU], s73 offen offset:2048 sc1\n\t"
      "buffer_load_dwordx4 v[236:239], %[vU], %[rU], s73 offen offset:3072 sc1\n\t"
      "buffer_load_dwordx4 v[240:243], %[vU], %[rU], s74 offen offset:0 sc1\n\t"
      "buffer_load_dwordx4 v[244:247], %[vU], %[rU], s74 offen offset:1024 sc1\n\t"
      "buffer_load_dwordx4 v[248:251], %[vU], %[rU], s74 offen offset:2048 sc1\n\t"
      "buffer_load_dwordx4 v[252:255], %[vU], %[rU], s74 offen offset:3072 sc1\n\t"
      "s_waitcnt vmcnt(10)\n\t"
      "ds_write_b128 %[vWm], v[152:155] offset:8192\n\t"
      "ds_write_b128 %[vWm], v[156:159] offset:9216\n\t"
      "s_waitcnt lgkmcnt(0)\n\t"
      "s_barrier\n\t"
      "ds_read_b128 v[160:163], %[vLd] offset:8192\n\t"
      "ds_read_b128 v[164:167], %[vLd] offset:9216\n\t"
      "ds_read_b128 v[168:171], %[vLd] offset:10240\n\t"
      "ds_read_b128 v[172:175], %[vLd] offset:11264\n\t"
      "ds_read_b128 v[176:179], %[vLd] offset:12288\n\t"
      "ds_read_b128 v[180:183], %[vLd] offset:13312\n\t"
      "ds_read_b128 v[184:187], %[vLd] offset:14336\n\t"
      "ds_read_b128 v[188:191], %[vLd] offset:15360\n\t"
      "buffer_store_dword v151, %[vFc], %[rc], 0 offen sc1\n\t"
      "s_waitcnt vmcnt(9)\n\t"
      "s_branch LTA0_%=\n\t"
      "LFD1_%=:\n\t"
      "buffer_load_dword v150, %[vFk], %[rc], 0 offen sc1\n\t"
      "s_branch LFE1_%=\n\t"
      "LFA1_%=:\n\t"
      "s_mov_b64 exec, s[96:97]\n\t"
      "buffer_load_dwordx4 v[40:43], %[vMw], %[rc], s70 offen sc1\n\t"
      "s_mov_b64 exec, s[98:99]\n\t"
      "buffer_load_dwordx4 v[44:47], %[vMw], %[rc], s70 offen offset:1024 sc1\n\t"
      "s_mov_b64 exec, -1\n\t"
      "s_sleep 1\n\t"
      "s_mov_b64 exec, s[96:97]\n\t"
      "buffer_load_dwordx4 v[40:43], %[vMw], %[rc], s70 offen sc1\n\t"
      "s_mov_b64 exec, s[98:99]\n\t"
      "buffer_load_dwordx4 v[44:47], %[vMw], %[rc], s70 offen offset:1024 sc1\n\t"
      "s_mov_b64 exec, -1\n\t"
      "s_sleep 1\n\t"
      "LHA1_%=:\n\t"
      "s_mov_b64 exec, s[96:97]\n\t"
      "buffer_load_dwordx4 v[40:43], %[vMw], %[rc], s70 offen sc1\n\t"
      "s_mov_b64 exec, s[98:99]\n\t"
      "buffer_load_dwordx4 v[44:47], %[vMw], %[rc], s70 offen offset:1024 sc1\n\t"
      "s_mov_b64 exec, -1\n\t"
      "s_add_u32 s71, s71, 1\n\t"
      "s_cmp_gt_u32 s71, 131072\n\t"
      "s_cbranch_scc1 LFAIL_%=\n\t"
      "s_waitcnt vmcnt(5)\n\t"
      "v_max3_u32 v144, v40, v41, v42\n\t"
      "v_max_u32 v144, v144, v43\n\t"
      "v_cmp_eq_u32 s[96:97], -1, v144\n\t"
      "s_waitcnt vmcnt(4)\n\t"
      "v_max3_u32 v147, v44, v45, v46\n\t"
      "v_max_u32 v147, v147, v47\n\t"
      "v_cmp_eq_u32 s[98:99], -1, v147\n\t"
      "s_or_b64 s[100:101], s[96:97], s[98:99]\n\t"
      "s_cbranch_scc1 LHA1_%=\n\t"
      "s_branch LGA1_%=\n\t"
      "LFK1_%=:\n\t"
      "buffer_load_dword v150, %[vFk], %[rc], 0 offen sc1\n\t"
      "s_branch LFR1_%=\n\t"
      "LSK1_%=:\n\t"
      "s_waitcnt vmcnt(3)\n\t"
      "s_branch LSC1_%=\n\t"
      "LSL1_%=:\n\t"
      "buffer_load_dword v150, %[vFk], %[rc], 0 offen sc1\n\t"
      "s_add_u32 s71, s71, 1\n\t"
      "s_cmp_gt_u32 s71, 131072\n\t"
      "s_cbranch_scc1 LFAIL_%=\n\t"
      "s_waitcnt vmcnt(0)\n\t"
      "LSC1_%=:\n\t"
      "s_add_u32 s69, s68, 1\n\t"
      "s_lshl_b32 s72, s69, 13\n\t"
      "s_lshl_b32 s73, s69, 15\n\t"
      "s_add_u32 s74, s73, 4096\n\t"
      "s_add_u32 s69, s69, s76\n\t"
      "v_cmp_ge_i32 vcc, s69, v150\n\t"
      "s_cbranch_vccnz LSL1_%=\n\t"
      "buffer_load_dwordx4 v[152:155], %[vMw], %[rK], s72 offen offset:0 sc1\n\t"
      "buffer_load_dwordx4 v[156:159], %[vMw], %[rK], s72 offen offset:1024 sc1\n\t"
      "buffer_load_dwordx2 v[62:63], off, %[rc], s63 sc1\n\t"
      "buffer_load_dword v150, %[vFk], %[rc], 0 offen sc1\n\t"
      "buffer_load_dwordx4 v[192:195], %[vU], %[rU], s73 offen offset:0 sc1\n\t"
      "buffer_load_dwordx4 v[196:199], %[vU], %[rU], s73 offen offset:1024 sc1\n\t"
      "buffer_load_dwordx4 v[200:203], %[vU], %[rU], s73 offen offset:2048 sc1\n\t"
      "buffer_load_dwordx4 v[204:207], %[vU], %[rU], s73 offen offset:3072 sc1\n\t"
      "buffer_load_dwordx4 v[208:211], %[vU], %[rU], s74 offen offset:0 sc1\n\t"
      "buffer_load_dwordx4 v[212:215], %[vU], %[rU], s74 offen offset:1024 sc1\n\t"
      "buffer_load_dwordx4 v[216:219], %[vU], %[rU], s74 offen offset:2048 sc1\n\t"
      "buffer_load_dwordx4 v[220:223], %[vU], %[rU], s74 offen offset:3072 sc1\n\t"
      "s_waitcnt vmcnt(10)\n\t"
      "ds_write_b128 %[vWm], v[152:155] offset:8192\n\t"
      "ds_write_b128 %[vWm], v[156:159] offset:9216\n\t"
      "s_waitcnt lgkmcnt(0)\n\t"
      "s_barrier\n\t"
      "ds_read_b128 v[160:163], %[vLd] offset:8192\n\t"
      "ds_read_b128 v[164:167], %[vLd] offset:9216\n\t"
      "ds_read_b128 v[168:171], %[vLd] offset:10240\n\t"
      "ds_read_b128 v[172:175], %[vLd] offset:11264\n\t"
      "ds_read_b128 v[176:179], %[vLd] offset:12288\n\t"
      "ds_read_b128 v[180:183], %[vLd] offset:13312\n\t"
      "ds_read_b128 v[184:187], %[vLd] offset:14336\n\t"
      "ds_read_b128 v[188:191], %[vLd] offset:15360\n\t"
      "buffer_store_dword v151, %[vFc], %[rc], 0 offen sc1\n\t"
      "s_waitcnt vmcnt(9)\n\t"
      "s_branch LTA1_%=\n\t"
      "LFAIL_%=:\n\t"
      "s_mov_b32 %[st], 1\n\t"
      "LEND_%=:\n\t"
      "s_waitcnt vmcnt(0) lgkmcnt(0)\n\t"
      : [st] "=s"(st), [oM] "+s"(oM), [oAn] "+s"(oAn), [tag] "+s"(tag), [dR] "+s"(dR), [xo0] "+v"(xo[0]), [xo1] "+v"(xo[1]), [po] "+v"(po), [vFk] "+v"(vFk), [vFc] "+v"(vFc)
      : [rs] "s"(mkrs(ws, WS_BYTES)), [vL] "v"(lane * 16), [vMw] "v"(lane * 16 + w * 2048), [vLd] "v"(lb), [vWm] "v"(lb + w * 2048), [vRm1] "v"(lb + (1 ^ w) * 2048),
        [vRm2] "v"(lb + (2 ^ w) * 2048), [vRm3] "v"(lb + (3 ^ w) * 2048),
        [vU] "v"(w * 8192 + lane * 16),
        [kmo] "v"((n >= 8) ? (unsigned)(((w * 4 + g) * 8 + (n & 7)) * 16) : OOB), [idx] "v"((n < 8) ? 0x4444 : 0xEEEE),
        [vWr] "v"(img + (32 * w + n) * 32 + g * 8), [vRdF] "v"(rdF),
        [vRdK] "v"(rdF + w * 1024), [cinit] "v"(cinit),
        [sent] "v"(sent), [cns] "v"(0xfffeffffu),
        [rc] "s"(mkrs(comm, 4 << 20)), [rW] "s"(mkrs(Wq + h * 128, (4 << 20) - h * 256)), [rW2] "s"(mkrs(Wk + h * 128, (4 << 20) - h * 256)),
        [vG] "v"(WS_G), [vSw] "v"(WS_SW), [vTr] "v"(WS_TR),
        [rK] "s"(mkrs(ws + OFF_K + (size_t)h * HQ, 64 * 8192)),
        [rU] "s"(mkrs(ws + OFF_UT + (size_t)h * HU, 64 * 32768)),
        [rKM] "s"(mkrs(ws + OFF_KM + (size_t)h * 2048, (5 << 20) - h * 2048)), [tm0] "s"(now32()),
        [scl] "s"(SCL) TS_OPS(w == 0 ? h : w == 3 ? 64 + h : -1)
      :
        "v40", "v41", "v42", "v43", "v44", "v45", "v46", "v47", "v48", "v49", "v50", "v51", "v52", "v53", "v54", "v55", "v56", "v57", "v58", "v59", "v60", "v61", "v62", "v63",
        "v64", "v65", "v66", "v67", "v68", "v69", "v70", "v71", "v72", "v73", "v74", "v75", "v76", "v77", "v78", "v79", "v80", "v81", "v82", "v83", "v84", "v85", "v86", "v87",
        "v88", "v89", "v90", "v91", "v92", "v93", "v94", "v95", "v96", "v97", "v98", "v99", "v100", "v101", "v102", "v103", "v104", "v105", "v106", "v107", "v108", "v109", "v110", "v111",
        "v112", "v113", "v114", "v115", "v116", "v117", "v118", "v119", "v120", "v121", "v122", "v123", "v124", "v125", "v126", "v127", "v128", "v129", "v130", "v131", "v132", "v133", "v134", "v135",
        "v136", "v137", "v138", "v139", "v140", "v141", "v142", "v143", "v144", "v145", "v146", "v147", "v148", "v149", "v150", "v151", "v152", "v153", "v154", "v155", "v156", "v157", "v158", "v159",
        "v160", "v161", "v162", "v163", "v164", "v165", "v166", "v167", "v168", "v169", "v170", "v171", "v172", "v173", "v174", "v175", "v176", "v177", "v178", "v179", "v180", "v181", "v182", "v183",
        "v184", "v185", "v186", "v187", "v188", "v189", "v190", "v191", "v192", "v193", "v194", "v195", "v196", "v197", "v198", "v199", "v200", "v201", "v202", "v203", "v204", "v205", "v206", "v207",
        "v208", "v209", "v210", "v211", "v212", "v213", "v214", "v215", "v216", "v217", "v218", "v219", "v220", "v221", "v222", "v223", "v224", "v225", "v226", "v227", "v228", "v229", "v230", "v231",
        "v232", "v233", "v234", "v235", "v236", "v237", "v238", "v239", "v240", "v241", "v242", "v243", "v244", "v245", "v246", "v247", "v248", "v249", "v250", "v251", "v252", "v253", "v254", "v255",
        "a0", "a1", "a2", "a3", "a4", "a5", "a6", "a7", "a8", "a9", "a10", "a11", "a12", "a13", "a14", "a15", "a16", "a17", "a18", "a19", "a20", "a21", "a22", "a23",
        "a24", "a25", "a26", "a27", "a28", "a29", "a30", "a31", "a32", "a33", "a34", "a35", "a36", "a37", "a38", "a39", "a40", "a41", "a42", "a43", "a44", "a45", "a46", "a47",
        "a48", "a49", "a50", "a51", "a52", "a53", "a54", "a55", "a56", "a57", "a58", "a59", "a60", "a61", "a62", "a63", "a64", "a65", "a66", "a67", "a68", "a69", "a70", "a71",
        "a72", "a73", "a74", "a75", "a76", "a77", "a78", "a79", "a80", "a81", "a82", "a83", "a84", "a85", "a86", "a87", "a88", "a89", "a90", "a91", "a92", "a93", "a94", "a95",
        "a96", "a97", "a98", "a99", "a100", "a101", "a102", "a103", "a104", "a105", "a106", "a107", "a108", "a109", "a110", "a111", "a112", "a113", "a114", "a115", "a116", "a117", "a118", "a119",
        "a120", "a121", "a122", "a123", "a124", "a125", "a126", "a127", "a128", "a129", "a130", "a131", "a132", "a133", "a134", "a135", "a136", "a137", "a138", "a139", "a140", "a141", "a142", "a143",
        "a144", "a145", "a146", "a147", "a148", "a149", "a150", "a151", "a152", "a153", "a154", "a155", "a156", "a157", "a158", "a159", "a160", "a161", "a162", "a163", "a164", "a165", "a166", "a167",
        "a168", "a169", "a170", "a171", "a172", "a173", "a174", "a175", "a176", "a177", "a178", "a179", "a180", "a181", "a182", "a183", "a184", "a185", "a186", "a187", "a188", "a189", "a190", "a191",
        "a192", "a193", "a194", "a195", "a196", "a197", "a198", "a199", "a200", "a201", "a202", "a203", "a204", "a205", "a206", "a207", "a208", "a209", "a210", "a211", "a212", "a213", "a214", "a215",
        "a216", "a217", "a218", "a219", "a220", "a221", "a222", "a223", "a224", "a225", "a226", "a227", "a228", "a229", "a230", "a231", "a232", "a233", "a234", "a235", "a236", "a237", "a238", "a239",
        "a240", "a241", "a242", "a243", "a244", "a245", "a246", "a247", "a248", "a249", "a250", "a251", "a252", "a253", "a254", "a255", "s62", "s63", "s64", "s65", "s66", "s67", "s68", "s69",
        "s70", "s71", "s72", "s73", "s74", "s75", "s76", "s77", "s78", "s79", "s80", "s81", "s82", "s83", "s84", "s85", "s86", "s87", "s88", "s89", "s90", "s91", "s92", "s93",
        "s94", "s95", "s96", "s97", "s98", "s99", "s100", "s101", "s52", "s53", "s54", "s55", "s56", "s57", "s58", "s59", "s60", "s61", "vcc", "scc", "m0", "memory"
  );
  return st == 0;
}

DI bool role_b(u8* __restrict__ ws, const u16* Wv, const u16* Wo, u8* comm, int h, unsigned ldsb) {
  const int tid = threadIdx.x, w = __builtin_amdgcn_readfirstlane(tid >> 6), lane = tid & 63, n = lane & 15, g = lane >> 4;
  constexpr unsigned parA = 0, parB = 0;
  unsigned dR = 0x200000u;
  const unsigned img = ldsb + 8192, lb = ldsb + lane * 16;
  unsigned yo[2];
#pragma unroll
  for (int i = 0; i < 2; ++i) {
    const unsigned col = 128 * w + 64 * i + 32 * (g & 1) + 16 * (g >> 1) + n;
    yo[i] = C_PY + (parB ^ fl(col >> 4)) + cmap(((col >> 3) * 32 + h) * 128 + (col & 7) * 16);
  }
  const int bw = 2 * h + (w & 1);
  v4u sel;
#pragma unroll
  for (int j = 0; j < 4; ++j) sel[j] = ((n & 7) == 2 * j ? 0x3f80u : 0u) | ((n & 7) == 2 * j + 1 ? 0x3f800000u : 0u);
  const v4u sent = {0xffffffffu, 0xffffffffu, 0xffffffffu, 0xffffffffu};
  int st;
  const unsigned rp = W8 ? h & 7 : 4 + (h & 3), ring = C_RING + (parA ^ fl(rp));
  unsigned oM = ring + rp * 16384u, oAn = ring + 0x80000u + rp * 128u, tag = 0x5a5a0000u;
  unsigned mo = C_RING + (parA ^ fl(lane >> 3)) + bw * 128 + (n & 7) * 16 + (lane >> 3) * 16384u;
  unsigned mr = w >= 2 ? mo : lane < 32 ? (W8 ? (h >> 3) * 2 + w : h >> 2) * 512u + lane * 16u : OOB;
  unsigned vAn = (h == 0 && tid < 8) ? C_RING + (parA ^ fl(tid)) + 0x80000u + tid * 128u : OOB;
  unsigned vFc = tid == 192 ? L_A + 4096u + h * 128u : OOB, dM = 0;
  unsigned oX = C_PX + cmap(bw * 4096), oY = C_PY + cmap(bw * 4096), oP = C_PP + cmap((bw & 7) * 4096u);
  asm volatile(
      "s_nop 4\n\t"
      "s_mov_b32 s69, 188743808\n\t"
      "s_buffer_load_dword s60, %[rs], s69\n\t"
      "s_mov_b32 s69, 0\n\t"
      "buffer_load_dwordx4 a[0:3], %[vG], %[rW], s69 offen\n\t"
      "s_mov_b32 s69, 131072\n\t"
      "buffer_load_dwordx4 a[4:7], %[vG], %[rW], s69 offen\n\t"
      "s_mov_b32 s69, 262144\n\t"
      "buffer_load_dwordx4 a[8:11], %[vG], %[rW], s69 offen\n\t"
      "s_mov_b32 s69, 393216\n\t"
      "buffer_load_dwordx4 a[12:15], %[vG], %[rW], s69 offen\n\t"
      "s_mov_b32 s69, 524288\n\t"
      "buffer_load_dwordx4 a[16:19], %[vG], %[rW], s69 offen\n\t"
      "s_mov_b32 s69, 655360\n\t"
      "buffer_load_dwordx4 a[20:23], %[vG], %[rW], s69 offen\n\t"
      "s_mov_b32 s69, 786432\n\t"
      "buffer_load_dwordx4 a[24:27], %[vG], %[rW], s69 offen\n\t"
      "s_mov_b32 s69, 917504\n\t"
      "buffer_load_dwordx4 a[28:31], %[vG], %[rW], s69 offen\n\t"
      "s_mov_b32 s69, 1048576\n\t"
      "buffer_load_dwordx4 a[32:35], %[vG], %[rW], s69 offen\n\t"
      "s_mov_b32 s69, 1179648\n\t"
      "buffer_load_dwordx4 a[36:39], %[vG], %[rW], s69 offen\n\t"
      "s_mov_b32 s69, 1310720\n\t"
      "buffer_load_dwordx4 a[40:43], %[vG], %[rW], s69 offen\n\t"
      "s_mov_b32 s69, 1441792\n\t"
      "buffer_load_dwordx4 a[44:47], %[vG], %[rW], s69 offen\n\t"
      "s_mov_b32 s69, 1572864\n\t"
      "buffer_load_dwordx4 a[48:51], %[vG], %[rW], s69 offen\n\t"
      "s_mov_b32 s69, 1703936\n\t"
      "buffer_load_dwordx4 a[52:55], %[vG], %[rW], s69 offen\n\t"
      "s_mov_b32 s69, 1835008\n\t"
      "buffer_load_dwordx4 a[56:59], %[vG], %[rW], s69 offen\n\t"
      "s_mov_b32 s69, 1966080\n\t"
      "buffer_load_dwordx4 a[60:63], %[vG], %[rW], s69 offen\n\t"
      "s_mov_b32 s69, 2097152\n\t"
      "buffer_load_dwordx4 a[64:67], %[vG], %[rW], s69 offen\n\t"
      "s_mov_b32 s69, 2228224\n\t"
      "buffer_load_dwordx4 a[68:71], %[vG], %[rW], s69 offen\n\t"
      "s_mov_b32 s69, 2359296\n\t"
      "buffer_load_dwordx4 a[72:75], %[vG], %[rW], s69 offen\n\t"
      "s_mov_b32 s69, 2490368\n\t"
      "buffer_load_dwordx4 a[76:79], %[vG], %[rW], s69 offen\n\t"
      "s_mov_b32 s69, 2621440\n\t"
      "buffer_load_dwordx4 a[80:83], %[vG], %[rW], s69 offen\n\t"
      "s_mov_b32 s69, 2752512\n\t"
      "buffer_load_dwordx4 a[84:87], %[vG], %[rW], s69 offen\n\t"
      "s_mov_b32 s69, 2883584\n\t"
      "buffer_load_dwordx4 a[88:91], %[vG], %[rW], s69 offen\n\t"
      "s_mov_b32 s69, 3014656\n\t"
      "buffer_load_dwordx4 a[92:95], %[vG], %[rW], s69 offen\n\t"
      "s_mov_b32 s69, 3145728\n\t"
      "buffer_load_dwordx4 a[96:99], %[vG], %[rW], s69 offen\n\t"
      "s_mov_b32 s69, 3276800\n\t"
      "buffer_load_dwordx4 a[100:103], %[vG], %[rW], s69 offen\n\t"
      "s_mov_b32 s69, 3407872\n\t"
      "buffer_load_dwordx4 a[104:107], %[vG], %[rW], s69 offen\n\t"
      "s_mov_b32 s69, 3538944\n\t"
      "buffer_load_dwordx4 a[108:111], %[vG], %[rW], s69 offen\n\t"
      "s_mov_b32 s69, 3670016\n\t"
      "buffer_load_dwordx4 a[112:115], %[vG], %[rW], s69 offen\n\t"
      "s_mov_b32 s69, 3801088\n\t"
      "buffer_load_dwordx4 a[116:119], %[vG], %[rW], s69 offen\n\t"
      "s_mov_b32 s69, 3932160\n\t"
      "buffer_load_dwordx4 a[120:123], %[vG], %[rW], s69 offen\n\t"
      "s_mov_b32 s69, 4063232\n\t"
      "buffer_load_dwordx4 a[124:127], %[vG], %[rW], s69 offen\n\t"
      "s_mov_b32 s69, 0\n\t"
      "buffer_load_dwordx4 a[128:131], %[vG2], %[rW2], s69 offen\n\t"
      "s_mov_b32 s69, 4096\n\t"
      "buffer_load_dwordx4 a[132:135], %[vG2], %[rW2], s69 offen\n\t"
      "s_mov_b32 s69, 8192\n\t"
      "buffer_load_dwordx4 a[136:139], %[vG2], %[rW2], s69 offen\n\t"
      "s_mov_b32 s69, 12288\n\t"
      "buffer_load_dwordx4 a[140:143], %[vG2], %[rW2], s69 offen\n\t"
      "s_mov_b32 s69, 16384\n\t"
      "buffer_load_dwordx4 a[144:147], %[vG2], %[rW2], s69 offen\n\t"
      "s_mov_b32 s69, 20480\n\t"
      "buffer_load_dwordx4 a[148:151], %[vG2], %[rW2], s69 offen\n\t"
      "s_mov_b32 s69, 24576\n\t"
      "buffer_load_dwordx4 a[152:155], %[vG2], %[rW2], s69 offen\n\t"
      "s_mov_b32 s69, 28672\n\t"
      "buffer_load_dwordx4 a[156:159], %[vG2], %[rW2], s69 offen\n\t"
      "s_mov_b32 s69, 32768\n\t"
      "buffer_load_dwordx4 a[160:163], %[vG2], %[rW2], s69 offen\n\t"
      "s_mov_b32 s69, 36864\n\t"
      "buffer_load_dwordx4 a[164:167], %[vG2], %[rW2], s69 offen\n\t"
      "s_mov_b32 s69, 40960\n\t"
      "buffer_load_dwordx4 a[168:171], %[vG2], %[rW2], s69 offen\n\t"
      "s_mov_b32 s69, 45056\n\t"
      "buffer_load_dwordx4 a[172:175], %[vG2], %[rW2], s69 offen\n\t"
      "s_mov_b32 s69, 49152\n\t"
      "buffer_load_dwordx4 a[176:179], %[vG2], %[rW2], s69 offen\n\t"
      "s_mov_b32 s69, 53248\n\t"
      "buffer_load_dwordx4 a[180:183], %[vG2], %[rW2], s69 offen\n\t"
      "s_mov_b32 s69, 57344\n\t"
      "buffer_load_dwordx4 a[184:187], %[vG2], %[rW2], s69 offen\n\t"
      "s_mov_b32 s69, 61440\n\t"
      "buffer_load_dwordx4 a[188:191], %[vG2], %[rW2], s69 offen\n\t"
      "s_mov_b32 s69, 65536\n\t"
      "buffer_load_dwordx4 a[192:195], %[vG2], %[rW2], s69 offen\n\t"
      "s_mov_b32 s69, 69632\n\t"
      "buffer_load_dwordx4 a[196:199], %[vG2], %[rW2], s69 offen\n\t"
      "s_mov_b32 s69, 73728\n\t"
      "buffer_load_dwordx4 a[200:203], %[vG2], %[rW2], s69 offen\n\t"
      "s_mov_b32 s69, 77824\n\t"
      "buffer_load_dwordx4 a[204:207], %[vG2], %[rW2], s69 offen\n\t"
      "s_mov_b32 s69, 81920\n\t"
      "buffer_load_dwordx4 a[208:211], %[vG2], %[rW2], s69 offen\n\t"
      "s_mov_b32 s69, 86016\n\t"
      "buffer_load_dwordx4 a[212:215], %[vG2], %[rW2], s69 offen\n\t"
      "s_mov_b32 s69, 90112\n\t"
      "buffer_load_dwordx4 a[216:219], %[vG2], %[rW2], s69 offen\n\t"
      "s_mov_b32 s69, 94208\n\t"
      "buffer_load_dwordx4 a[220:223], %[vG2], %[rW2], s69 offen\n\t"
      "s_mov_b32 s69, 98304\n\t"
      "buffer_load_dwordx4 a[224:227], %[vG2], %[rW2], s69 offen\n\t"
      "s_mov_b32 s69, 102400\n\t"
      "buffer_load_dwordx4 a[228:231], %[vG2], %[rW2], s69 offen\n\t"
      "s_mov_b32 s69, 106496\n\t"
      "buffer_load_dwordx4 a[232:235], %[vG2], %[rW2], s69 offen\n\t"
      "s_mov_b32 s69, 110592\n\t"
      "buffer_load_dwordx4 a[236:239], %[vG2], %[rW2], s69 offen\n\t"
      "s_mov_b32 s69, 114688\n\t"
      "buffer_load_dwordx4 a[240:243], %[vG2], %[rW2], s69 offen\n\t"
      "s_mov_b32 s69, 118784\n\t"
      "buffer_load_dwordx4 a[244:247], %[vG2], %[rW2], s69 offen\n\t"
      "s_mov_b32 s69, 122880\n\t"
      "buffer_load_dwordx4 a[248:251], %[vG2], %[rW2], s69 offen\n\t"
      "s_mov_b32 s69, 126976\n\t"
      "buffer_load_dwordx4 a[252:255], %[vG2], %[rW2], s69 offen\n\t"
      "v_sub_u32 v40, %[vWm], %[vMw]\n\t"
      "v_add_u32 v41, %[vSw], v40\n\t"
      "v_add_u32 v42, 0x10000, v41\n\t"
      "v_xor_b32 v43, 0, %[vTr]\n\t"
      "v_add_u32 v43, v43, v40\n\t"
      "v_xor_b32 v44, 32768, %[vTr]\n\t"
      "v_add_u32 v44, v44, v40\n\t"
      "v_xor_b32 v45, 65536, %[vTr]\n\t"
      "v_add_u32 v45, v45, v40\n\t"
      "v_xor_b32 v46, 98304, %[vTr]\n\t"
      "v_add_u32 v46, v46, v40\n\t"
      "v_xor_b32 v47, 32, %[vTr]\n\t"
      "v_add_u32 v47, v47, v40\n\t"
      "v_xor_b32 v48, 32800, %[vTr]\n\t"
      "v_add_u32 v48, v48, v40\n\t"
      "v_xor_b32 v49, 65568, %[vTr]\n\t"
      "v_add_u32 v49, v49, v40\n\t"
      "v_xor_b32 v50, 98336, %[vTr]\n\t"
      "v_add_u32 v50, v50, v40\n\t"
      "s_waitcnt vmcnt(63)\n\t"
      "ds_write_b128 v41, a[0:3] offset:0\n\t"
      "s_waitcnt vmcnt(62)\n\t"
      "ds_write_b128 v41, a[4:7] offset:4096\n\t"
      "s_waitcnt vmcnt(61)\n\t"
      "ds_write_b128 v41, a[8:11] offset:8192\n\t"
      "s_waitcnt vmcnt(60)\n\t"
      "ds_write_b128 v41, a[12:15] offset:12288\n\t"
      "s_waitcnt vmcnt(59)\n\t"
      "ds_write_b128 v41, a[16:19] offset:16384\n\t"
      "s_waitcnt vmcnt(58)\n\t"
      "ds_write_b128 v41, a[20:23] offset:20480\n\t"
      "s_waitcnt vmcnt(57)\n\t"
      "ds_write_b128 v41, a[24:27] offset:24576\n\t"
      "s_waitcnt vmcnt(56)\n\t"
      "ds_write_b128 v41, a[28:31] offset:28672\n\t"
      "s_waitcnt vmcnt(55)\n\t"
      "ds_write_b128 v41, a[32:35] offset:32768\n\t"
      "s_waitcnt vmcnt(54)\n\t"
      "ds_write_b128 v41, a[36:39] offset:36864\n\t"
      "s_waitcnt vmcnt(53)\n\t"
      "ds_write_b128 v41, a[40:43] offset:40960\n\t"
      "s_waitcnt vmcnt(52)\n\t"
      "ds_write_b128 v41, a[44:47] offset:45056\n\t"
      "s_waitcnt vmcnt(51)\n\t"
      "ds_write_b128 v41, a[48:51] offset:49152\n\t"
      "s_waitcnt vmcnt(50)\n\t"
      "ds_write_b128 v41, a[52:55] offset:53248\n\t"
      "s_waitcnt vmcnt(49)\n\t"
      "ds_write_b128 v41, a[56:59] offset:57344\n\t"
      "s_waitcnt vmcnt(48)\n\t"
      "ds_write_b128 v41, a[60:63] offset:61440\n\t"
      "s_waitcnt vmcnt(47)\n\t"
      "ds_write_b128 v42, a[64:67] offset:0\n\t"
      "s_waitcnt vmcnt(46)\n\t"
      "ds_write_b128 v42, a[68:71] offset:4096\n\t"
      "s_waitcnt vmcnt(45)\n\t"
      "ds_write_b128 v42, a[72:75] offset:8192\n\t"
      "s_waitcnt vmcnt(44)\n\t"
      "ds_write_b128 v42, a[76:79] offset:12288\n\t"
      "s_waitcnt vmcnt(43)\n\t"
      "ds_write_b128 v42, a[80:83] offset:16384\n\t"
      "s_waitcnt vmcnt(42)\n\t"
      "ds_write_b128 v42, a[84:87] offset:20480\n\t"
      "s_waitcnt vmcnt(41)\n\t"
      "ds_write_b128 v42, a[88:91] offset:24576\n\t"
      "s_waitcnt vmcnt(40)\n\t"
      "ds_write_b128 v42, a[92:95] offset:28672\n\t"
      "s_waitcnt vmcnt(39)\n\t"
      "ds_write_b128 v42, a[96:99] offset:32768\n\t"
      "s_waitcnt vmcnt(38)\n\t"
      "ds_write_b128 v42, a[100:103] offset:36864\n\t"
      "s_waitcnt vmcnt(37)\n\t"
      "ds_write_b128 v42, a[104:107] offset:40960\n\t"
      "s_waitcnt vmcnt(36)\n\t"
      "ds_write_b128 v42, a[108:111] offset:45056\n\t"
      "s_waitcnt vmcnt(35)\n\t"
      "ds_write_b128 v42, a[112:115] offset:49152\n\t"
      "s_waitcnt vmcnt(34)\n\t"
      "ds_write_b128 v42, a[116:119] offset:53248\n\t"
      "s_waitcnt vmcnt(33)\n\t"
      "ds_write_b128 v42, a[120:123] offset:57344\n\t"
      "s_waitcnt vmcnt(32)\n\t"
      "ds_write_b128 v42, a[124:127] offset:61440\n\t"
      "s_waitcnt lgkmcnt(0)\n\t"
      "s_barrier\n\t"
      "ds_read_b64_tr_b16 a[0:1], v43 offset:0\n\t"
      "ds_read_b64_tr_b16 a[2:3], v43 offset:512\n\t"
      "ds_read_b64_tr_b16 a[4:5], v43 offset:8192\n\t"
      "ds_read_b64_tr_b16 a[6:7], v43 offset:8704\n\t"
      "ds_read_b64_tr_b16 a[8:9], v43 offset:16384\n\t"
      "ds_read_b64_tr_b16 a[10:11], v43 offset:16896\n\t"
      "ds_read_b64_tr_b16 a[12:13], v43 offset:24576\n\t"
      "ds_read_b64_tr_b16 a[14:15], v43 offset:25088\n\t"
      "ds_read_b64_tr_b16 a[16:17], v44 offset:0\n\t"
      "ds_read_b64_tr_b16 a[18:19], v44 offset:512\n\t"
      "ds_read_b64_tr_b16 a[20:21], v44 offset:8192\n\t"
      "ds_read_b64_tr_b16 a[22:23], v44 offset:8704\n\t"
      "ds_read_b64_tr_b16 a[24:25], v44 offset:16384\n\t"
      "ds_read_b64_tr_b16 a[26:27], v44 offset:16896\n\t"
      "ds_read_b64_tr_b16 a[28:29], v44 offset:24576\n\t"
      "ds_read_b64_tr_b16 a[30:31], v44 offset:25088\n\t"
      "ds_read_b64_tr_b16 a[32:33], v45 offset:0\n\t"
      "ds_read_b64_tr_b16 a[34:35], v45 offset:512\n\t"
      "ds_read_b64_tr_b16 a[36:37], v45 offset:8192\n\t"
      "ds_read_b64_tr_b16 a[38:39], v45 offset:8704\n\t"
      "ds_read_b64_tr_b16 a[40:41], v45 offset:16384\n\t"
      "ds_read_b64_tr_b16 a[42:43], v45 offset:16896\n\t"
      "ds_read_b64_tr_b16 a[44:45], v45 offset:24576\n\t"
      "ds_read_b64_tr_b16 a[46:47], v45 offset:25088\n\t"
      "ds_read_b64_tr_b16 a[48:49], v46 offset:0\n\t"
      "ds_read_b64_tr_b16 a[50:51], v46 offset:512\n\t"
      "ds_read_b64_tr_b16 a[52:53], v46 offset:8192\n\t"
      "ds_read_b64_tr_b16 a[54:55], v46 offset:8704\n\t"
      "ds_read_b64_tr_b16 a[56:57], v46 offset:16384\n\t"
      "ds_read_b64_tr_b16 a[58:59], v46 offset:16896\n\t"
      "ds_read_b64_tr_b16 a[60:61], v46 offset:24576\n\t"
      "ds_read_b64_tr_b16 a[62:63], v46 offset:25088\n\t"
      "ds_read_b64_tr_b16 a[64:65], v47 offset:0\n\t"
      "ds_read_b64_tr_b16 a[66:67], v47 offset:512\n\t"
      "ds_read_b64_tr_b16 a[68:69], v47 offset:8192\n\t"
      "ds_read_b64_tr_b16 a[70:71], v47 offset:8704\n\t"
      "ds_read_b64_tr_b16 a[72:73], v47 offset:16384\n\t"
      "ds_read_b64_tr_b16 a[74:75], v47 offset:16896\n\t"
      "ds_read_b64_tr_b16 a[76:77], v47 offset:24576\n\t"
      "ds_read_b64_tr_b16 a[78:79], v47 offset:25088\n\t"
      "ds_read_b64_tr_b16 a[80:81], v48 offset:0\n\t"
      "ds_read_b64_tr_b16 a[82:83], v48 offset:512\n\t"
      "ds_read_b64_tr_b16 a[84:85], v48 offset:8192\n\t"
      "ds_read_b64_tr_b16 a[86:87], v48 offset:8704\n\t"
      "ds_read_b64_tr_b16 a[88:89], v48 offset:16384\n\t"
      "ds_read_b64_tr_b16 a[90:91], v48 offset:16896\n\t"
      "ds_read_b64_tr_b16 a[92:93], v48 offset:24576\n\t"
      "ds_read_b64_tr_b16 a[94:95], v48 offset:25088\n\t"
      "ds_read_b64_tr_b16 a[96:97], v49 offset:0\n\t"
      "ds_read_b64_tr_b16 a[98:99], v49 offset:512\n\t"
      "ds_read_b64_tr_b16 a[100:101], v49 offset:8192\n\t"
      "ds_read_b64_tr_b16 a[102:103], v49 offset:8704\n\t"
      "ds_read_b64_tr_b16 a[104:105], v49 offset:16384\n\t"
      "ds_read_b64_tr_b16 a[106:107], v49 offset:16896\n\t"
      "ds_read_b64_tr_b16 a[108:109], v49 offset:24576\n\t"
      "ds_read_b64_tr_b16 a[110:111], v49 offset:25088\n\t"
      "ds_read_b64_tr_b16 a[112:113], v50 offset:0\n\t"
      "ds_read_b64_tr_b16 a[114:115], v50 offset:512\n\t"
      "ds_read_b64_tr_b16 a[116:117], v50 offset:8192\n\t"
      "ds_read_b64_tr_b16 a[118:119], v50 offset:8704\n\t"
      "ds_read_b64_tr_b16 a[120:121], v50 offset:16384\n\t"
      "ds_read_b64_tr_b16 a[122:123], v50 offset:16896\n\t"
      "ds_read_b64_tr_b16 a[124:125], v50 offset:24576\n\t"
      "ds_read_b64_tr_b16 a[126:127], v50 offset:25088\n\t"
      "s_waitcnt lgkmcnt(0)\n\t"
      "s_barrier\n\t"
      "v_add_u32 v41, %[vSw2], v40\n\t"
      "v_xor_b32 v42, 128, %[vSw2]\n\t"
      "v_add_u32 v42, v42, v40\n\t"
      "v_xor_b32 v43, 0, %[vTr2]\n\t"
      "v_add_u32 v43, v43, v40\n\t"
      "v_xor_b32 v44, 64, %[vTr2]\n\t"
      "v_add_u32 v44, v44, v40\n\t"
      "v_xor_b32 v45, 128, %[vTr2]\n\t"
      "v_add_u32 v45, v45, v40\n\t"
      "v_xor_b32 v46, 192, %[vTr2]\n\t"
      "v_add_u32 v46, v46, v40\n\t"
      "s_waitcnt vmcnt(31)\n\t"
      "ds_write_b128 v41, a[128:131] offset:0\n\t"
      "s_waitcnt vmcnt(30)\n\t"
      "ds_write_b128 v41, a[132:135] offset:1024\n\t"
      "s_waitcnt vmcnt(29)\n\t"
      "ds_write_b128 v41, a[136:139] offset:2048\n\t"
      "s_waitcnt vmcnt(28)\n\t"
      "ds_write_b128 v41, a[140:143] offset:3072\n\t"
      "s_waitcnt vmcnt(27)\n\t"
      "ds_write_b128 v42, a[144:147] offset:4096\n\t"
      "s_waitcnt vmcnt(26)\n\t"
      "ds_write_b128 v42, a[148:151] offset:5120\n\t"
      "s_waitcnt vmcnt(25)\n\t"
      "ds_write_b128 v42, a[152:155] offset:6144\n\t"
      "s_waitcnt vmcnt(24)\n\t"
      "ds_write_b128 v42, a[156:159] offset:7168\n\t"
      "s_waitcnt vmcnt(23)\n\t"
      "ds_write_b128 v41, a[160:163] offset:8192\n\t"
      "s_waitcnt vmcnt(22)\n\t"
      "ds_write_b128 v41, a[164:167] offset:9216\n\t"
      "s_waitcnt vmcnt(21)\n\t"
      "ds_write_b128 v41, a[168:171] offset:10240\n\t"
      "s_waitcnt vmcnt(20)\n\t"
      "ds_write_b128 v41, a[172:175] offset:11264\n\t"
      "s_waitcnt vmcnt(19)\n\t"
      "ds_write_b128 v42, a[176:179] offset:12288\n\t"
      "s_waitcnt vmcnt(18)\n\t"
      "ds_write_b128 v42, a[180:183] offset:13312\n\t"
      "s_waitcnt vmcnt(17)\n\t"
      "ds_write_b128 v42, a[184:187] offset:14336\n\t"
      "s_waitcnt vmcnt(16)\n\t"
      "ds_write_b128 v42, a[188:191] offset:15360\n\t"
      "s_waitcnt vmcnt(15)\n\t"
      "ds_write_b128 v41, a[192:195] offset:16384\n\t"
      "s_waitcnt vmcnt(14)\n\t"
      "ds_write_b128 v41, a[196:199] offset:17408\n\t"
      "s_waitcnt vmcnt(13)\n\t"
      "ds_write_b128 v41, a[200:203] offset:18432\n\t"
      "s_waitcnt vmcnt(12)\n\t"
      "ds_write_b128 v41, a[204:207] offset:19456\n\t"
      "s_waitcnt vmcnt(11)\n\t"
      "ds_write_b128 v42, a[208:211] offset:20480\n\t"
      "s_waitcnt vmcnt(10)\n\t"
      "ds_write_b128 v42, a[212:215] offset:21504\n\t"
      "s_waitcnt vmcnt(9)\n\t"
      "ds_write_b128 v42, a[216:219] offset:22528\n\t"
      "s_waitcnt vmcnt(8)\n\t"
      "ds_write_b128 v42, a[220:223] offset:23552\n\t"
      "s_waitcnt vmcnt(7)\n\t"
      "ds_write_b128 v41, a[224:227] offset:24576\n\t"
      "s_waitcnt vmcnt(6)\n\t"
      "ds_write_b128 v41, a[228:231] offset:25600\n\t"
      "s_waitcnt vmcnt(5)\n\t"
      "ds_write_b128 v41, a[232:235] offset:26624\n\t"
      "s_waitcnt vmcnt(4)\n\t"
      "ds_write_b128 v41, a[236:239] offset:27648\n\t"
      "s_waitcnt vmcnt(3)\n\t"
      "ds_write_b128 v42, a[240:243] offset:28672\n\t"
      "s_waitcnt vmcnt(2)\n\t"
      "ds_write_b128 v42, a[244:247] offset:29696\n\t"
      "s_waitcnt vmcnt(1)\n\t"
      "ds_write_b128 v42, a[248:251] offset:30720\n\t"
      "s_waitcnt vmcnt(0)\n\t"
      "ds_write_b128 v42, a[252:255] offset:31744\n\t"
      "s_mov_b32 s69, 188747776\n\t"
      "buffer_load_dwordx4 v[64:67], %[vL], %[rs], s69 offen sc1\n\t"
      "s_waitcnt lgkmcnt(0)\n\t"
      "ds_read_b64_tr_b16 a[128:129], v43 offset:0\n\t"
      "ds_read_b64_tr_b16 a[130:131], v43 offset:512\n\t"
      "ds_read_b64_tr_b16 a[132:133], v43 offset:2048\n\t"
      "ds_read_b64_tr_b16 a[134:135], v43 offset:2560\n\t"
      "ds_read_b64_tr_b16 a[136:137], v43 offset:8192\n\t"
      "ds_read_b64_tr_b16 a[138:139], v43 offset:8704\n\t"
      "ds_read_b64_tr_b16 a[140:141], v43 offset:10240\n\t"
      "ds_read_b64_tr_b16 a[142:143], v43 offset:10752\n\t"
      "ds_read_b64_tr_b16 a[144:145], v43 offset:16384\n\t"
      "ds_read_b64_tr_b16 a[146:147], v43 offset:16896\n\t"
      "ds_read_b64_tr_b16 a[148:149], v43 offset:18432\n\t"
      "ds_read_b64_tr_b16 a[150:151], v43 offset:18944\n\t"
      "ds_read_b64_tr_b16 a[152:153], v43 offset:24576\n\t"
      "ds_read_b64_tr_b16 a[154:155], v43 offset:25088\n\t"
      "ds_read_b64_tr_b16 a[156:157], v43 offset:26624\n\t"
      "ds_read_b64_tr_b16 a[158:159], v43 offset:27136\n\t"
      "ds_read_b64_tr_b16 a[160:161], v44 offset:0\n\t"
      "ds_read_b64_tr_b16 a[162:163], v44 offset:512\n\t"
      "ds_read_b64_tr_b16 a[164:165], v44 offset:2048\n\t"
      "ds_read_b64_tr_b16 a[166:167], v44 offset:2560\n\t"
      "ds_read_b64_tr_b16 a[168:169], v44 offset:8192\n\t"
      "ds_read_b64_tr_b16 a[170:171], v44 offset:8704\n\t"
      "ds_read_b64_tr_b16 a[172:173], v44 offset:10240\n\t"
      "ds_read_b64_tr_b16 a[174:175], v44 offset:10752\n\t"
      "ds_read_b64_tr_b16 a[176:177], v44 offset:16384\n\t"
      "ds_read_b64_tr_b16 a[178:179], v44 offset:16896\n\t"
      "ds_read_b64_tr_b16 a[180:181], v44 offset:18432\n\t"
      "ds_read_b64_tr_b16 a[182:183], v44 offset:18944\n\t"
      "ds_read_b64_tr_b16 a[184:185], v44 offset:24576\n\t"
      "ds_read_b64_tr_b16 a[186:187], v44 offset:25088\n\t"
      "ds_read_b64_tr_b16 a[188:189], v44 offset:26624\n\t"
      "ds_read_b64_tr_b16 a[190:191], v44 offset:27136\n\t"
      "ds_read_b64_tr_b16 a[192:193], v45 offset:0\n\t"
      "ds_read_b64_tr_b16 a[194:195], v45 offset:512\n\t"
      "ds_read_b64_tr_b16 a[196:197], v45 offset:2048\n\t"
      "ds_read_b64_tr_b16 a[198:199], v45 offset:2560\n\t"
      "ds_read_b64_tr_b16 a[200:201], v45 offset:8192\n\t"
      "ds_read_b64_tr_b16 a[202:203], v45 offset:8704\n\t"
      "ds_read_b64_tr_b16 a[204:205], v45 offset:10240\n\t"
      "ds_read_b64_tr_b16 a[206:207], v45 offset:10752\n\t"
      "ds_read_b64_tr_b16 a[208:209], v45 offset:16384\n\t"
      "ds_read_b64_tr_b16 a[210:211], v45 offset:16896\n\t"
      "ds_read_b64_tr_b16 a[212:213], v45 offset:18432\n\t"
      "ds_read_b64_tr_b16 a[214:215], v45 offset:18944\n\t"
      "ds_read_b64_tr_b16 a[216:217], v45 offset:24576\n\t"
      "ds_read_b64_tr_b16 a[218:219], v45 offset:25088\n\t"
      "ds_read_b64_tr_b16 a[220:221], v45 offset:26624\n\t"
      "ds_read_b64_tr_b16 a[222:223], v45 offset:27136\n\t"
      "ds_read_b64_tr_b16 a[224:225], v46 offset:0\n\t"
      "ds_read_b64_tr_b16 a[226:227], v46 offset:512\n\t"
      "ds_read_b64_tr_b16 a[228:229], v46 offset:2048\n\t"
      "ds_read_b64_tr_b16 a[230:231], v46 offset:2560\n\t"
      "ds_read_b64_tr_b16 a[232:233], v46 offset:8192\n\t"
      "ds_read_b64_tr_b16 a[234:235], v46 offset:8704\n\t"
      "ds_read_b64_tr_b16 a[236:237], v46 offset:10240\n\t"
      "ds_read_b64_tr_b16 a[238:239], v46 offset:10752\n\t"
      "ds_read_b64_tr_b16 a[240:241], v46 offset:16384\n\t"
      "ds_read_b64_tr_b16 a[242:243], v46 offset:16896\n\t"
      "ds_read_b64_tr_b16 a[244:245], v46 offset:18432\n\t"
      "ds_read_b64_tr_b16 a[246:247], v46 offset:18944\n\t"
      "ds_read_b64_tr_b16 a[248:249], v46 offset:24576\n\t"
      "ds_read_b64_tr_b16 a[250:251], v46 offset:25088\n\t"
      "ds_read_b64_tr_b16 a[252:253], v46 offset:26624\n\t"
      "ds_read_b64_tr_b16 a[254:255], v46 offset:27136\n\t"
      "s_waitcnt lgkmcnt(0)\n\t"
      "s_barrier\n\t"
      "s_mov_b32 s71, 0\n\t"
      "s_mov_b32 s55, 0\n\t"
      "v_accvgpr_read_b32 v112, a1\n\t"
      "v_accvgpr_mov_b32 a1, a65\n\t"
      "v_accvgpr_write_b32 a65, v112\n\t"
      "v_accvgpr_read_b32 v113, a3\n\t"
      "v_accvgpr_mov_b32 a3, a67\n\t"
      "v_accvgpr_write_b32 a67, v113\n\t"
      "v_accvgpr_read_b32 v114, a5\n\t"
      "v_accvgpr_mov_b32 a5, a69\n\t"
      "v_accvgpr_write_b32 a69, v114\n\t"
      "v_accvgpr_read_b32 v115, a7\n\t"
      "v_accvgpr_mov_b32 a7, a71\n\t"
      "v_accvgpr_write_b32 a71, v115\n\t"
      "v_accvgpr_read_b32 v116, a9\n\t"
      "v_accvgpr_mov_b32 a9, a73\n\t"
      "v_accvgpr_write_b32 a73, v116\n\t"
      "v_accvgpr_read_b32 v117, a11\n\t"
      "v_accvgpr_mov_b32 a11, a75\n\t"
      "v_accvgpr_write_b32 a75, v117\n\t"
      "v_accvgpr_read_b32 v118, a13\n\t"
      "v_accvgpr_mov_b32 a13, a77\n\t"
      "v_accvgpr_write_b32 a77, v118\n\t"
      "v_accvgpr_read_b32 v119, a15\n\t"
      "v_accvgpr_mov_b32 a15, a79\n\t"
      "v_accvgpr_write_b32 a79, v119\n\t"
      "v_accvgpr_read_b32 v112, a17\n\t"
      "v_accvgpr_mov_b32 a17, a81\n\t"
      "v_accvgpr_write_b32 a81, v112\n\t"
      "v_accvgpr_read_b32 v113, a19\n\t"
      "v_accvgpr_mov_b32 a19, a83\n\t"
      "v_accvgpr_write_b32 a83, v113\n\t"
      "v_accvgpr_read_b32 v114, a21\n\t"
      "v_accvgpr_mov_b32 a21, a85\n\t"
      "v_accvgpr_write_b32 a85, v114\n\t"
      "v_accvgpr_read_b32 v115, a23\n\t"
      "v_accvgpr_mov_b32 a23, a87\n\t"
      "v_accvgpr_write_b32 a87, v115\n\t"
      "v_accvgpr_read_b32 v116, a25\n\t"
      "v_accvgpr_mov_b32 a25, a89\n\t"
      "v_accvgpr_write_b32 a89, v116\n\t"
      "v_accvgpr_read_b32 v117, a27\n\t"
      "v_accvgpr_mov_b32 a27, a91\n\t"
      "v_accvgpr_write_b32 a91, v117\n\t"
      "v_accvgpr_read_b32 v118, a29\n\t"
      "v_accvgpr_mov_b32 a29, a93\n\t"
      "v_accvgpr_write_b32 a93, v118\n\t"
      "v_accvgpr_read_b32 v119, a31\n\t"
      "v_accvgpr_mov_b32 a31, a95\n\t"
      "v_accvgpr_write_b32 a95, v119\n\t"
      "v_accvgpr_read_b32 v112, a33\n\t"
      "v_accvgpr_mov_b32 a33, a97\n\t"
      "v_accvgpr_write_b32 a97, v112\n\t"
      "v_accvgpr_read_b32 v113, a35\n\t"
      "v_accvgpr_mov_b32 a35, a99\n\t"
      "v_accvgpr_write_b32 a99, v113\n\t"
      "v_accvgpr_read_b32 v114, a37\n\t"
      "v_accvgpr_mov_b32 a37, a101\n\t"
      "v_accvgpr_write_b32 a101, v114\n\t"
      "v_accvgpr_read_b32 v115, a39\n\t"
      "v_accvgpr_mov_b32 a39, a103\n\t"
      "v_accvgpr_write_b32 a103, v115\n\t"
      "v_accvgpr_read_b32 v116, a41\n\t"
      "v_accvgpr_mov_b32 a41, a105\n\t"
      "v_accvgpr_write_b32 a105, v116\n\t"
      "v_accvgpr_read_b32 v117, a43\n\t"
      "v_accvgpr_mov_b32 a43, a107\n\t"
      "v_accvgpr_write_b32 a107, v117\n\t"
      "v_accvgpr_read_b32 v118, a45\n\t"
      "v_accvgpr_mov_b32 a45, a109\n\t"
      "v_accvgpr_write_b32 a109, v118\n\t"
      "v_accvgpr_read_b32 v119, a47\n\t"
      "v_accvgpr_mov_b32 a47, a111\n\t"
      "v_accvgpr_write_b32 a111, v119\n\t"
      "v_accvgpr_read_b32 v112, a49\n\t"
      "v_accvgpr_mov_b32 a49, a113\n\t"
      "v_accvgpr_write_b32 a113, v112\n\t"
      "v_accvgpr_read_b32 v113, a51\n\t"
      "v_accvgpr_mov_b32 a51, a115\n\t"
      "v_accvgpr_write_b32 a115, v113\n\t"
      "v_accvgpr_read_b32 v114, a53\n\t"
      "v_accvgpr_mov_b32 a53, a117\n\t"
      "v_accvgpr_write_b32 a117, v114\n\t"
      "v_accvgpr_read_b32 v115, a55\n\t"
      "v_accvgpr_mov_b32 a55, a119\n\t"
      "v_accvgpr_write_b32 a119, v115\n\t"
      "v_accvgpr_read_b32 v116, a57\n\t"
      "v_accvgpr_mov_b32 a57, a121\n\t"
      "v_accvgpr_write_b32 a121, v116\n\t"
      "v_accvgpr_read_b32 v117, a59\n\t"
      "v_accvgpr_mov_b32 a59, a123\n\t"
      "v_accvgpr_write_b32 a123, v117\n\t"
      "v_accvgpr_read_b32 v118, a61\n\t"
      "v_accvgpr_mov_b32 a61, a125\n\t"
      "v_accvgpr_write_b32 a125, v118\n\t"
      "v_accvgpr_read_b32 v119, a63\n\t"
      "v_accvgpr_mov_b32 a63, a127\n\t"
      "v_accvgpr_write_b32 a127, v119\n\t"
      "s_mul_i32 s60, s60, 0x9e3779b1\n\t"
      "s_add_u32 s60, s60, 0x7f4a7c15\n\t"
      "s_branch LIN1_%=\n\t"
      "LIN0_%=:\n\t"
      "s_add_u32 s71, s71, 1\n\t"
      "s_cmp_gt_u32 s71, 131072\n\t"
      "s_cbranch_scc1 LFAIL_%=\n\t"
      "s_sleep 4\n\t"
      "s_mov_b32 s69, 188747776\n\t"
      "buffer_load_dwordx4 v[64:67], %[vL], %[rs], s69 offen sc1\n\t"
      "LIN1_%=:\n\t"
      "s_waitcnt vmcnt(0)\n\t"
      "v_cmp_ne_u32 vcc, s60, v64\n\t"
      "v_cmp_ne_u32 s[64:65], s60, v66\n\t"
      "s_or_b64 vcc, vcc, s[64:65]\n\t"
      "s_cbranch_scc1 LIN0_%=\n\t"
      "v_add_u32 v64, v65, v67\n\t"
      "s_nop 1\n\t"
      "v_readlane_b32 s69, v64, 0\n\t"
      "v_readlane_b32 s61, v64, 1\n\t"
      "s_cmp_lt_u32 s61, s69\n\t"
      "s_cselect_b32 s61, 8192, 0\n\t"
      "v_readlane_b32 s69, v64, 2\n\t"
      "v_readlane_b32 s62, v64, 3\n\t"
      "s_cmp_lt_u32 s62, s69\n\t"
      "s_cselect_b32 s62, 8192, 0\n\t"
      "v_add_u32 %[mo], s61, %[mo]\n\t"
      "v_add_u32 %[vAn], s61, %[vAn]\n\t"
      "v_add_u32 %[vFc], s61, %[vFc]\n\t"
      "v_add_u32 %[yo0], s62, %[yo0]\n\t"
      "v_add_u32 %[yo1], s62, %[yo1]\n\t"
      "s_add_u32 %[oM], %[oM], s61\n\t"
      "s_add_u32 %[oAn], %[oAn], s61\n\t"
      "s_add_u32 %[oX], %[oX], s61\n\t"
      "s_add_u32 %[oP], %[oP], s61\n\t"
      "s_add_u32 %[oY], %[oY], s62\n\t"
      "s_xor_b32 s69, s61, s62\n\t"
      "s_or_b32 %[dR], %[dR], s69\n\t"
      "s_cmp_lt_u32 %[wv], 2\n\t"
      "s_cselect_b32 %[dM], 0, %[dR]\n\t"
      "s_cselect_b32 s69, 0, s61\n\t"
      "v_add_u32 %[mr], s69, %[mr]\n\t"
      "s_mov_b32 s60, %[tm0]\n\t"
      "v_mov_b64 v[72:73], 0\n\t"
      "v_mov_b64 v[74:75], 0\n\t"
      "v_mov_b64 v[76:77], 0\n\t"
      "v_mov_b64 v[78:79], 0\n\t"
      "v_mov_b64 v[80:81], 0\n\t"
      "v_mov_b64 v[82:83], 0\n\t"
      "v_mov_b64 v[84:85], 0\n\t"
      "v_mov_b64 v[86:87], 0\n\t"
      "v_mov_b64 v[88:89], 0\n\t"
      "v_mov_b64 v[90:91], 0\n\t"
      "v_mov_b64 v[92:93], 0\n\t"
      "v_mov_b64 v[94:95], 0\n\t"
      "v_xor_b32 v76, 128, %[vRm1]\n\t"
      "v_xor_b32 v77, 128, %[vRm2]\n\t"
      "v_xor_b32 v78, 128, %[vRm3]\n\t"
      "v_xor_b32 v79, 0xaaaa, %[idx]\n\t"
      "v_xor_b32 v228, 128, %[vWm]\n\t"
      "LLAYER_%=:\n\t"
      "s_mov_b32 s68, 0\n\t"
      "s_lshl_b32 s76, s55, 6\n\t"
      "s_add_u32 s67, %[oX], 524288\n\t"
      "s_lshl_b32 s53, s55, 19\n\t"
      "s_add_u32 s72, %[oY], s53\n\t"
      "s_xor_b32 s66, s72, 524288\n\t"
      "s_add_u32 s54, %[oP], 65536\n\t"
      "LLOOP_%=:\n\t"
      "s_and_b32 s69, s68, 3\n\t"
      "s_lshl_b32 s69, s69, 17\n\t"
      "s_add_u32 s70, %[oM], s69\n\t"
      "LWB0_%=:\n\t"
      "s_memrealtime s[56:57]\n\t"
      "s_waitcnt lgkmcnt(0)\n\t"
      "s_sub_u32 s69, s60, s56\n\t"
      "s_cmp_gt_i32 s69, 0\n\t"
      "s_cbranch_scc1 LWB0_%=\n\t"
      "buffer_load_dwordx4 v[40:43], %[vMw], %[rc], s70 offen sc1\n\t"
      "buffer_load_dwordx4 v[44:47], %[vMw], %[rc], s70 offen offset:1024 sc1\n\t"
      "s_max_u32 s69, s68, 1\n\t"
      "s_add_u32 s69, s69, s76\n\t"
      "s_sub_u32 s69, s69, 1\n\t"
      "v_mov_b32 v111, s69\n\t"
      "s_lshl_b32 s78, s55, 2\n\t"
      "s_cmp_gt_u32 s68, 60\n\t"
      "s_cselect_b32 s78, s78, 0\n\t"
      "s_add_u32 s78, s78, s68\n\t"
      "s_lshl_b32 s78, s78, 18\n\t"
      "s_sub_u32 s78, s78, 262144\n\t"
      "s_xor_b32 s75, s53, 0\n\t"
      "s_cmp_eq_u32 s68, 63\n\t"
      "s_cselect_b32 s75, 0x40000000, s75\n\t"
      "s_add_u32 s69, s68, 1\n\t"
      "s_and_b32 s69, s69, 3\n\t"
      "s_lshl_b32 s79, s69, 17\n\t"
      "s_xor_b32 s80, s79, 262144\n\t"
      "s_lshr_b32 s69, s68, 3\n\t"
      "s_lshl_b32 s69, s69, 14\n\t"
      "s_and_b32 s62, s68, 7\n\t"
      "s_lshl_b32 s62, s62, 10\n\t"
      "s_add_u32 s62, s62, s69\n\t"
      "s_add_u32 s63, s62, %[oAn]\n\t"
      "s_add_u32 s69, s68, %[tag]\n\t"
      "v_mov_b32 v169, s69\n\t"
      "s_cmp_lt_u32 %[wv], 2\n\t"
      "s_cselect_b32 s80, s54, s80\n\t"
      "s_cbranch_scc0 LRS0_%=\n\t"
      "s_sleep 1\n\t"
      "buffer_store_dwordx4 %[sent], %[vL], %[rc], s67 offen offset:0 sc1\n\t"
      "buffer_store_dwordx4 %[sent], %[vL], %[rc], s67 offen offset:1024 sc1\n\t"
      "buffer_store_dwordx4 %[sent], %[vL], %[rc], s67 offen offset:2048 sc1\n\t"
      "buffer_store_dwordx4 %[sent], %[vL], %[rc], s67 offen offset:3072 sc1\n\t"
      "LRT0_%=:\n\t"
      "s_waitcnt vmcnt(5)\n\t"
      "v_max3_u32 v104, v40, v41, v42\n\t"
      "v_max_u32 v104, v104, v43\n\t"
      "v_cmp_eq_u32 s[96:97], -1, v104\n\t"
      "s_waitcnt vmcnt(4)\n\t"
      "v_max3_u32 v105, v44, v45, v46\n\t"
      "v_max_u32 v105, v105, v47\n\t"
      "v_cmp_eq_u32 s[98:99], -1, v105\n\t"
      "s_or_b64 s[100:101], s[96:97], s[98:99]\n\t"
      "s_cbranch_scc1 LFB0_%=\n\t"
      "LGB0_%=:\n\t"
      "ds_write_b128 %[vWm], v[40:43]\n\t"
      "ds_write_b128 %[vWm], v[44:47] offset:1024\n\t"
      "ds_read_b128 v[64:67], v228 offset:0\n\t"
      "ds_read_b128 v[68:71], v228 offset:1024\n\t"
      "v_smfmac_f32_16x16x64_bf16 v[72:75], v[40:43], a[0:7], %[idx]\n\t"
      "s_nop 0\n\t"
      "v_smfmac_f32_16x16x64_bf16 v[72:75], v[44:47], a[8:15], %[idx]\n\t"
      "s_waitcnt lgkmcnt(2)\n\t"
      "s_barrier\n\t"
      "s_memrealtime s[56:57]\n\t"
      "ds_read_b128 v[176:179], %[vRm1] offset:0\n\t"
      "ds_read_b128 v[180:183], v76 offset:0\n\t"
      "ds_read_b128 v[184:187], %[vRm1] offset:1024\n\t"
      "ds_read_b128 v[188:191], v76 offset:1024\n\t"
      "s_waitcnt lgkmcnt(4)\n\t"
      "v_smfmac_f32_16x16x64_bf16 v[72:75], v[64:67], a[64:71], v79\n\t"
      "ds_read_b128 v[192:195], %[vRm2] offset:0\n\t"
      "v_smfmac_f32_16x16x64_bf16 v[72:75], v[68:71], a[72:79], v79\n\t"
      "s_cmp_eq_u32 %[ms], 0\n\t"
      "s_cbranch_scc1 LAN0_%=\n\t"
      "LAR0_%=:\n\t"
      "ds_read_b128 v[196:199], v77 offset:0\n\t"
      "s_waitcnt lgkmcnt(5)\n\t"
      "v_smfmac_f32_16x16x64_bf16 v[72:75], v[176:179], a[16:23], %[idx]\n\t"
      "ds_read_b128 v[200:203], %[vRm2] offset:1024\n\t"
      "s_waitcnt lgkmcnt(5)\n\t"
      "v_smfmac_f32_16x16x64_bf16 v[72:75], v[180:183], a[80:87], v79\n\t"
      "ds_read_b128 v[204:207], v77 offset:1024\n\t"
      "s_waitcnt lgkmcnt(5)\n\t"
      "v_smfmac_f32_16x16x64_bf16 v[72:75], v[184:187], a[24:31], %[idx]\n\t"
      "ds_read_b128 v[208:211], %[vRm3] offset:0\n\t"
      "s_waitcnt lgkmcnt(5)\n\t"
      "v_smfmac_f32_16x16x64_bf16 v[72:75], v[188:191], a[88:95], v79\n\t"
      "ds_read_b128 v[212:215], v78 offset:0\n\t"
      "s_waitcnt lgkmcnt(5)\n\t"
      "v_smfmac_f32_16x16x64_bf16 v[72:75], v[192:195], a[32:39], %[idx]\n\t"
      "ds_read_b128 v[216:219], %[vRm3] offset:1024\n\t"
      "s_waitcnt lgkmcnt(5)\n\t"
      "v_smfmac_f32_16x16x64_bf16 v[72:75], v[196:199], a[96:103], v79\n\t"
      "ds_read_b128 v[220:223], v78 offset:1024\n\t"
      "s_waitcnt lgkmcnt(5)\n\t"
      "v_smfmac_f32_16x16x64_bf16 v[72:75], v[200:203], a[40:47], %[idx]\n\t"
      "buffer_store_dwordx4 %[sent], %[mr], %[rc], s80 offen sc1\n\t"
      "s_waitcnt lgkmcnt(4)\n\t"
      "v_smfmac_f32_16x16x64_bf16 v[72:75], v[204:207], a[104:111], v79\n\t"
      "buffer_store_dwordx4 v[96:99], %[yt0], %[rY], s78 offen offset:0 sc1\n\t"
      "s_waitcnt lgkmcnt(3)\n\t"
      "v_smfmac_f32_16x16x64_bf16 v[72:75], v[208:211], a[48:55], %[idx]\n\t"
      "buffer_store_dwordx4 v[100:103], %[yt0], %[rY], s78 offen offset:1024 sc1\n\t"
      "s_waitcnt lgkmcnt(2)\n\t"
      "v_smfmac_f32_16x16x64_bf16 v[72:75], v[212:215], a[112:119], v79\n\t"
      "buffer_store_dword v111, %[vFc], %[rc], 0 offen sc1\n\t"
      "s_waitcnt lgkmcnt(1)\n\t"
      "v_smfmac_f32_16x16x64_bf16 v[72:75], v[216:219], a[56:63], %[idx]\n\t"
      "s_waitcnt lgkmcnt(0)\n\t"
      "v_smfmac_f32_16x16x64_bf16 v[72:75], v[220:223], a[120:127], v79\n\t"
      "s_nop 7\n\t"
      "v_cvt_pk_bf16_f32 v96, v72, v73\n\t"
      "v_cvt_pk_bf16_f32 v97, v74, v75\n\t"
      "ds_write_b64 %[vWr], v[96:97]\n\t"
      "s_memrealtime s[58:59]\n\t"
      "v_mov_b64 v[72:73], 0\n\t"
      "v_mov_b64 v[74:75], 0\n\t"
      "s_waitcnt lgkmcnt(0)\n\t"
      "s_barrier\n\t"
      "ds_read_b64_tr_b16 v[48:49], %[vRd] offset:0\n\t"
      "ds_read_b64_tr_b16 v[50:51], %[vRd] offset:256\n\t"
      "ds_read_b64_tr_b16 v[52:53], %[vRd] offset:512\n\t"
      "ds_read_b64_tr_b16 v[54:55], %[vRd] offset:768\n\t"
      "ds_read_b64_tr_b16 v[56:57], %[vRd] offset:1024\n\t"
      "ds_read_b64_tr_b16 v[58:59], %[vRd] offset:1280\n\t"
      "ds_read_b64_tr_b16 v[60:61], %[vRd] offset:1536\n\t"
      "ds_read_b64_tr_b16 v[62:63], %[vRd] offset:1792\n\t"
      "s_waitcnt lgkmcnt(6)\n\t"
      "v_smfmac_f32_16x16x64_bf16 v[80:83], v[48:51], a[128:135], %[idx]\n\t"
      "s_waitcnt lgkmcnt(4)\n\t"
      "v_smfmac_f32_16x16x64_bf16 v[80:83], v[52:55], a[136:143], %[idx]\n\t"
      "s_waitcnt lgkmcnt(2)\n\t"
      "v_smfmac_f32_16x16x64_bf16 v[80:83], v[56:59], a[144:151], %[idx]\n\t"
      "s_waitcnt lgkmcnt(0)\n\t"
      "v_smfmac_f32_16x16x64_bf16 v[80:83], v[60:63], a[152:159], %[idx]\n\t"
      "buffer_load_dwordx2 v[106:107], off, %[rc], s63 sc1\n\t"
      "v_smfmac_f32_16x16x64_bf16 v[84:87], v[48:51], a[160:167], %[idx]\n\t"
      "v_smfmac_f32_16x16x64_bf16 v[84:87], v[52:55], a[168:175], %[idx]\n\t"
      "v_smfmac_f32_16x16x64_bf16 v[84:87], v[56:59], a[176:183], %[idx]\n\t"
      "v_smfmac_f32_16x16x64_bf16 v[84:87], v[60:63], a[184:191], %[idx]\n\t"
      "v_smfmac_f32_16x16x64_bf16 v[88:91], v[48:51], a[192:199], %[idx]\n\t"
      "s_nop 0\n\t"
      "s_nop 1\n\t"
      "v_smfmac_f32_16x16x64_bf16 v[88:91], v[52:55], a[200:207], %[idx]\n\t"
      "v_cvt_pk_bf16_f32 v96, v80, v81\n\t"
      "v_smfmac_f32_16x16x64_bf16 v[88:91], v[56:59], a[208:215], %[idx]\n\t"
      "v_cvt_pk_bf16_f32 v97, v82, v83\n\t"
      "v_smfmac_f32_16x16x64_bf16 v[88:91], v[60:63], a[216:223], %[idx]\n\t"
      "v_cvt_pk_bf16_f32 v98, v84, v85\n\t"
      "v_smfmac_f32_16x16x64_bf16 v[92:95], v[48:51], a[224:231], %[idx]\n\t"
      "v_cvt_pk_bf16_f32 v99, v86, v87\n\t"
      "v_smfmac_f32_16x16x64_bf16 v[92:95], v[52:55], a[232:239], %[idx]\n\t"
      "v_permlane16_swap_b32 v96, v98\n\t"
      "v_smfmac_f32_16x16x64_bf16 v[92:95], v[56:59], a[240:247], %[idx]\n\t"
      "v_permlane16_swap_b32 v97, v99\n\t"
      "v_smfmac_f32_16x16x64_bf16 v[92:95], v[60:63], a[248:255], %[idx]\n\t"
      "buffer_store_dwordx4 v[96:99], %[yo0], %[rc], s75 offen sc1\n\t"
      "v_cvt_pk_bf16_f32 v100, v88, v89\n\t"
      "v_cvt_pk_bf16_f32 v101, v90, v91\n\t"
      "s_nop 4\n\t"
      "v_cvt_pk_bf16_f32 v102, v92, v93\n\t"
      "v_cvt_pk_bf16_f32 v103, v94, v95\n\t"
      "s_nop 0\n\t"
      "v_permlane16_swap_b32 v100, v102\n\t"
      "v_permlane16_swap_b32 v101, v103\n\t"
      "buffer_store_dwordx4 v[100:103], %[yo1], %[rc], s75 offen sc1\n\t"
      "v_mov_b64 v[80:81], 0\n\t"
      "v_mov_b64 v[82:83], 0\n\t"
      "v_mov_b64 v[84:85], 0\n\t"
      "v_mov_b64 v[86:87], 0\n\t"
      "v_mov_b64 v[88:89], 0\n\t"
      "v_mov_b64 v[90:91], 0\n\t"
      "v_mov_b64 v[92:93], 0\n\t"
      "v_mov_b64 v[94:95], 0\n\t"
      "s_cmp_lt_u32 %[wv], 2\n\t"
      "s_cbranch_scc0 LYS0_%=\n\t"
      "buffer_store_dwordx4 %[sent], %[vL], %[rc], s66 offen offset:0 sc1\n\t"
      "buffer_store_dwordx4 %[sent], %[vL], %[rc], s66 offen offset:1024 sc1\n\t"
      "buffer_store_dwordx4 %[sent], %[vL], %[rc], s66 offen offset:2048 sc1\n\t"
      "buffer_store_dwordx4 %[sent], %[vL], %[rc], s66 offen offset:3072 sc1\n\t"
      "LYT0_%=:\n\t"
      "s_waitcnt vmcnt(6)\n\t"
      "v_readfirstlane_b32 s61, v106\n\t"
      "v_readfirstlane_b32 s56, v107\n\t"
      "s_sub_u32 s58, s58, 38\n\t"
      "s_sub_u32 s69, s58, s60\n\t"
      "s_cmp_gt_i32 s69, 12\n\t"
      "s_cselect_b32 s60, s58, s60\n\t"
      "s_add_u32 s69, s68, %[tag]\n\t"
      "s_cmp_eq_u32 s69, s56\n\t"
      "s_cselect_b32 s61, s61, s60\n\t"
      "s_sub_u32 s69, s61, s60\n\t"
      "s_add_u32 s69, s69, 400\n\t"
      "s_cmp_lt_u32 s69, 800\n\t"
      "s_cselect_b32 s60, s61, s60\n\t"
      "s_cmp_lt_u32 %[wv], 2\n\t"
      "s_cbranch_scc0 LNR0_%=\n\t"
      "s_add_u32 s61, s60, 109\n\t"
      "LWX00_%=:\n\t"
      "s_memrealtime s[56:57]\n\t"
      "s_waitcnt lgkmcnt(0)\n\t"
      "s_sub_u32 s69, s61, s56\n\t"
      "s_cmp_gt_i32 s69, 0\n\t"
      "s_cbranch_scc1 LWX00_%=\n\t"
      "buffer_load_dwordx4 v[144:147], %[vL], %[rc], s72 offen offset:0 sc1\n\t"
      "buffer_load_dwordx4 v[148:151], %[vL], %[rc], s72 offen offset:1024 sc1\n\t"
      "buffer_load_dwordx4 v[152:155], %[vL], %[rc], s72 offen offset:2048 sc1\n\t"
      "buffer_load_dwordx4 v[156:159], %[vL], %[rc], s72 offen offset:3072 sc1\n\t"
      "s_add_u32 s61, s60, 112\n\t"
      "LWX10_%=:\n\t"
      "s_memrealtime s[56:57]\n\t"
      "s_waitcnt lgkmcnt(0)\n\t"
      "s_sub_u32 s69, s61, s56\n\t"
      "s_cmp_gt_i32 s69, 0\n\t"
      "s_cbranch_scc1 LWX10_%=\n\t"
      "buffer_load_dwordx4 v[128:131], %[vL], %[rc], %[oP] offen offset:0 sc1\n\t"
      "buffer_load_dwordx4 v[132:135], %[vL], %[rc], %[oP] offen offset:1024 sc1\n\t"
      "buffer_load_dwordx4 v[136:139], %[vL], %[rc], %[oP] offen offset:2048 sc1\n\t"
      "buffer_load_dwordx4 v[140:143], %[vL], %[rc], %[oP] offen offset:3072 sc1\n\t"
      "s_add_u32 s61, s60, 120\n\t"
      "LWX20_%=:\n\t"
      "s_memrealtime s[56:57]\n\t"
      "s_waitcnt lgkmcnt(0)\n\t"
      "s_sub_u32 s69, s61, s56\n\t"
      "s_cmp_gt_i32 s69, 0\n\t"
      "s_cbranch_scc1 LWX20_%=\n\t"
      "buffer_load_dwordx4 v[112:115], %[vL], %[rc], %[oX] offen offset:0 sc1\n\t"
      "buffer_load_dwordx4 v[116:119], %[vL], %[rc], %[oX] offen offset:1024 sc1\n\t"
      "buffer_load_dwordx4 v[120:123], %[vL], %[rc], %[oX] offen offset:2048 sc1\n\t"
      "buffer_load_dwordx4 v[124:127], %[vL], %[rc], %[oX] offen offset:3072 sc1\n\t"
      "s_waitcnt vmcnt(7)\n\t"
      "v_mfma_f32_16x16x32_bf16 v[160:163], v[144:147], v[128:131], 0\n\t"
      "v_max3_u32 v104, v128, v129, v130\n\t"
      "s_waitcnt vmcnt(6)\n\t"
      "v_mfma_f32_16x16x32_bf16 v[160:163], v[148:151], v[132:135], v[160:163]\n\t"
      "v_max3_u32 v105, v131, v132, v133\n\t"
      "v_max3_u32 v104, v104, v134, v135\n\t"
      "s_waitcnt vmcnt(5)\n\t"
      "v_mfma_f32_16x16x32_bf16 v[160:163], v[152:155], v[136:139], v[160:163]\n\t"
      "v_max3_u32 v105, v105, v136, v137\n\t"
      "v_max3_u32 v104, v104, v138, v139\n\t"
      "s_waitcnt vmcnt(4)\n\t"
      "v_mfma_f32_16x16x32_bf16 v[160:163], v[156:159], v[140:143], v[160:163]\n\t"
      "v_max3_u32 v105, v105, v140, v141\n\t"
      "v_max3_u32 v104, v104, v142, v143\n\t"
      "v_max_u32 v104, v104, v105\n\t"
      "v_cmp_eq_u32 vcc, -1, v104\n\t"
      "s_cbranch_vccnz LXF0_%=\n\t"
      "s_waitcnt vmcnt(3)\n\t"
      "v_mfma_f32_16x16x32_bf16 v[160:163], v[112:115], %[sel], v[160:163]\n\t"
      "s_waitcnt vmcnt(2)\n\t"
      "v_mfma_f32_16x16x32_bf16 v[160:163], v[116:119], %[sel], v[160:163]\n\t"
      "s_waitcnt vmcnt(1)\n\t"
      "v_mfma_f32_16x16x32_bf16 v[160:163], v[120:123], %[sel], v[160:163]\n\t"
      "s_waitcnt vmcnt(0)\n\t"
      "v_mfma_f32_16x16x32_bf16 v[160:163], v[124:127], %[sel], v[160:163]\n\t"
      "s_nop 7\n\t"
      "v_permlane32_swap_b32 v160, v162\n\t"
      "v_permlane32_swap_b32 v161, v163\n\t"
      "s_nop 0\n\t"
      "v_add_f32_dpp v104, v162, v160 row_ror:8 row_mask:0xf bank_mask:0x3\n\t"
      "v_add_f32_dpp v104, v160, v162 row_ror:8 row_mask:0xf bank_mask:0xc\n\t"
      "v_add_f32_dpp v105, v163, v161 row_ror:8 row_mask:0xf bank_mask:0x3\n\t"
      "v_add_f32_dpp v105, v161, v163 row_ror:8 row_mask:0xf bank_mask:0xc\n\t"
      "v_cvt_pk_bf16_f32 v164, v104, v105\n\t"
      "v_cvt_pk_bf16_f32 v166, v104, v105\n\t"
      "v_cmp_u_f32 vcc, v104, v105\n\t"
      "s_cbranch_vccnz LXF0_%=\n\t"
      "LXG0_%=:\n\t"
      "v_permlane16_swap_b32 v164, v166\n\t"
      "v_mov_b32 v165, v164\n\t"
      "v_mov_b32 v167, v166\n\t"
      "s_nop 0\n\t"
      "v_permlane32_swap_b32 v164, v165\n\t"
      "v_permlane32_swap_b32 v166, v167\n\t"
      "buffer_store_dwordx4 v[164:167], %[mo], %[rc], s79 offen sc1\n\t"
      "LNR0_%=:\n\t"
      "s_add_u32 s69, s55, 2\n\t"
      "s_cmp_lt_u32 s68, s69\n\t"
      "s_cselect_b32 s69, 30, 0\n\t"
      "s_add_u32 s60, s60, s69\n\t"
      "s_add_u32 s60, s60, 169\n\t"
      "s_add_u32 s68, s68, 1\n\t"
      "s_and_b32 s69, s68, 3\n\t"
      "s_lshl_b32 s69, s69, 17\n\t"
      "s_add_u32 s70, %[oM], s69\n\t"
      "LWB1_%=:\n\t"
      "s_memrealtime s[56:57]\n\t"
      "s_waitcnt lgkmcnt(0)\n\t"
      "s_sub_u32 s69, s60, s56\n\t"
      "s_cmp_gt_i32 s69, 0\n\t"
      "s_cbranch_scc1 LWB1_%=\n\t"
      "buffer_load_dwordx4 v[40:43], %[vMw], %[rc], s70 offen sc1\n\t"
      "buffer_load_dwordx4 v[44:47], %[vMw], %[rc], s70 offen offset:1024 sc1\n\t"
      "s_max_u32 s69, s68, 1\n\t"
      "s_add_u32 s69, s69, s76\n\t"
      "s_sub_u32 s69, s69, 1\n\t"
      "v_mov_b32 v111, s69\n\t"
      "s_lshl_b32 s78, s55, 2\n\t"
      "s_cmp_gt_u32 s68, 60\n\t"
      "s_cselect_b32 s78, s78, 0\n\t"
      "s_add_u32 s78, s78, s68\n\t"
      "s_lshl_b32 s78, s78, 18\n\t"
      "s_sub_u32 s78, s78, 262144\n\t"
      "s_xor_b32 s75, s53, 524288\n\t"
      "s_cmp_eq_u32 s68, 63\n\t"
      "s_cselect_b32 s75, 0x40000000, s75\n\t"
      "s_add_u32 s69, s68, 1\n\t"
      "s_and_b32 s69, s69, 3\n\t"
      "s_lshl_b32 s79, s69, 17\n\t"
      "s_xor_b32 s80, s79, 262144\n\t"
      "s_lshr_b32 s69, s68, 3\n\t"
      "s_lshl_b32 s69, s69, 14\n\t"
      "s_and_b32 s62, s68, 7\n\t"
      "s_lshl_b32 s62, s62, 10\n\t"
      "s_add_u32 s62, s62, s69\n\t"
      "s_add_u32 s63, s62, %[oAn]\n\t"
      "s_add_u32 s69, s68, %[tag]\n\t"
      "v_mov_b32 v169, s69\n\t"
      "s_cmp_lt_u32 %[wv], 2\n\t"
      "s_cselect_b32 s80, %[oP], s80\n\t"
      "s_cbranch_scc0 LRS1_%=\n\t"
      "s_sleep 1\n\t"
      "buffer_store_dwordx4 %[sent], %[vL], %[rc], %[oX] offen offset:0 sc1\n\t"
      "buffer_store_dwordx4 %[sent], %[vL], %[rc], %[oX] offen offset:1024 sc1\n\t"
      "buffer_store_dwordx4 %[sent], %[vL], %[rc], %[oX] offen offset:2048 sc1\n\t"
      "buffer_store_dwordx4 %[sent], %[vL], %[rc], %[oX] offen offset:3072 sc1\n\t"
      "LRT1_%=:\n\t"
      "s_waitcnt vmcnt(5)\n\t"
      "v_max3_u32 v104, v40, v41, v42\n\t"
      "v_max_u32 v104, v104, v43\n\t"
      "v_cmp_eq_u32 s[96:97], -1, v104\n\t"
      "s_waitcnt vmcnt(4)\n\t"
      "v_max3_u32 v105, v44, v45, v46\n\t"
      "v_max_u32 v105, v105, v47\n\t"
      "v_cmp_eq_u32 s[98:99], -1, v105\n\t"
      "s_or_b64 s[100:101], s[96:97], s[98:99]\n\t"
      "s_cbranch_scc1 LFB1_%=\n\t"
      "LGB1_%=:\n\t"
      "ds_write_b128 %[vWm], v[40:43]\n\t"
      "ds_write_b128 %[vWm], v[44:47] offset:1024\n\t"
      "ds_read_b128 v[64:67], v228 offset:0\n\t"
      "ds_read_b128 v[68:71], v228 offset:1024\n\t"
      "v_smfmac_f32_16x16x64_bf16 v[72:75], v[40:43], a[0:7], %[idx]\n\t"
      "s_nop 0\n\t"
      "v_smfmac_f32_16x16x64_bf16 v[72:75], v[44:47], a[8:15], %[idx]\n\t"
      "s_waitcnt lgkmcnt(2)\n\t"
      "s_barrier\n\t"
      "s_memrealtime s[56:57]\n\t"
      "ds_read_b128 v[176:179], %[vRm1] offset:0\n\t"
      "ds_read_b128 v[180:183], v76 offset:0\n\t"
      "ds_read_b128 v[184:187], %[vRm1] offset:1024\n\t"
      "ds_read_b128 v[188:191], v76 offset:1024\n\t"
      "s_waitcnt lgkmcnt(4)\n\t"
      "v_smfmac_f32_16x16x64_bf16 v[72:75], v[64:67], a[64:71], v79\n\t"
      "ds_read_b128 v[192:195], %[vRm2] offset:0\n\t"
      "v_smfmac_f32_16x16x64_bf16 v[72:75], v[68:71], a[72:79], v79\n\t"
      "s_cmp_eq_u32 %[ms], 0\n\t"
      "s_cbranch_scc1 LAN1_%=\n\t"
      "LAR1_%=:\n\t"
      "ds_read_b128 v[196:199], v77 offset:0\n\t"
      "s_waitcnt lgkmcnt(5)\n\t"
      "v_smfmac_f32_16x16x64_bf16 v[72:75], v[176:179], a[16:23], %[idx]\n\t"
      "ds_read_b128 v[200:203], %[vRm2] offset:1024\n\t"
      "s_waitcnt lgkmcnt(5)\n\t"
      "v_smfmac_f32_16x16x64_bf16 v[72:75], v[180:183], a[80:87], v79\n\t"
      "ds_read_b128 v[204:207], v77 offset:1024\n\t"
      "s_waitcnt lgkmcnt(5)\n\t"
      "v_smfmac_f32_16x16x64_bf16 v[72:75], v[184:187], a[24:31], %[idx]\n\t"
      "ds_read_b128 v[208:211], %[vRm3] offset:0\n\t"
      "s_waitcnt lgkmcnt(5)\n\t"
      "v_smfmac_f32_16x16x64_bf16 v[72:75], v[188:191], a[88:95], v79\n\t"
      "ds_read_b128 v[212:215], v78 offset:0\n\t"
      "s_waitcnt lgkmcnt(5)\n\t"
      "v_smfmac_f32_16x16x64_bf16 v[72:75], v[192:195], a[32:39], %[idx]\n\t"
      "ds_read_b128 v[216:219], %[vRm3] offset:1024\n\t"
      "s_waitcnt lgkmcnt(5)\n\t"
      "v_smfmac_f32_16x16x64_bf16 v[72:75], v[196:199], a[96:103], v79\n\t"
      "ds_read_b128 v[220:223], v78 offset:1024\n\t"
      "s_waitcnt lgkmcnt(5)\n\t"
      "v_smfmac_f32_16x16x64_bf16 v[72:75], v[200:203], a[40:47], %[idx]\n\t"
      "buffer_store_dwordx4 %[sent], %[mr], %[rc], s80 offen sc1\n\t"
      "s_waitcnt lgkmcnt(4)\n\t"
      "v_smfmac_f32_16x16x64_bf16 v[72:75], v[204:207], a[104:111], v79\n\t"
      "buffer_store_dwordx4 v[96:99], %[yt0], %[rY], s78 offen offset:0 sc1\n\t"
      "s_waitcnt lgkmcnt(3)\n\t"
      "v_smfmac_f32_16x16x64_bf16 v[72:75], v[208:211], a[48:55], %[idx]\n\t"
      "buffer_store_dwordx4 v[100:103], %[yt0], %[rY], s78 offen offset:1024 sc1\n\t"
      "s_waitcnt lgkmcnt(2)\n\t"
      "v_smfmac_f32_16x16x64_bf16 v[72:75], v[212:215], a[112:119], v79\n\t"
      "buffer_store_dword v111, %[vFc], %[rc], 0 offen sc1\n\t"
      "s_waitcnt lgkmcnt(1)\n\t"
      "v_smfmac_f32_16x16x64_bf16 v[72:75], v[216:219], a[56:63], %[idx]\n\t"
      "s_waitcnt lgkmcnt(0)\n\t"
      "v_smfmac_f32_16x16x64_bf16 v[72:75], v[220:223], a[120:127], v79\n\t"
      "s_nop 7\n\t"
      "v_cvt_pk_bf16_f32 v96, v72, v73\n\t"
      "v_cvt_pk_bf16_f32 v97, v74, v75\n\t"
      "ds_write_b64 %[vWr], v[96:97]\n\t"
      "s_memrealtime s[58:59]\n\t"
      "v_mov_b64 v[72:73], 0\n\t"
      "v_mov_b64 v[74:75], 0\n\t"
      "s_waitcnt lgkmcnt(0)\n\t"
      "s_barrier\n\t"
      "ds_read_b64_tr_b16 v[48:49], %[vRd] offset:0\n\t"
      "ds_read_b64_tr_b16 v[50:51], %[vRd] offset:256\n\t"
      "ds_read_b64_tr_b16 v[52:53], %[vRd] offset:512\n\t"
      "ds_read_b64_tr_b16 v[54:55], %[vRd] offset:768\n\t"
      "ds_read_b64_tr_b16 v[56:57], %[vRd] offset:1024\n\t"
      "ds_read_b64_tr_b16 v[58:59], %[vRd] offset:1280\n\t"
      "ds_read_b64_tr_b16 v[60:61], %[vRd] offset:1536\n\t"
      "ds_read_b64_tr_b16 v[62:63], %[vRd] offset:1792\n\t"
      "s_waitcnt lgkmcnt(6)\n\t"
      "v_smfmac_f32_16x16x64_bf16 v[80:83], v[48:51], a[128:135], %[idx]\n\t"
      "s_waitcnt lgkmcnt(4)\n\t"
      "v_smfmac_f32_16x16x64_bf16 v[80:83], v[52:55], a[136:143], %[idx]\n\t"
      "s_waitcnt lgkmcnt(2)\n\t"
      "v_smfmac_f32_16x16x64_bf16 v[80:83], v[56:59], a[144:151], %[idx]\n\t"
      "s_waitcnt lgkmcnt(0)\n\t"
      "v_smfmac_f32_16x16x64_bf16 v[80:83], v[60:63], a[152:159], %[idx]\n\t"
      "buffer_load_dwordx2 v[106:107], off, %[rc], s63 sc1\n\t"
      "v_smfmac_f32_16x16x64_bf16 v[84:87], v[48:51], a[160:167], %[idx]\n\t"
      "v_smfmac_f32_16x16x64_bf16 v[84:87], v[52:55], a[168:175], %[idx]\n\t"
      "v_smfmac_f32_16x16x64_bf16 v[84:87], v[56:59], a[176:183], %[idx]\n\t"
      "v_smfmac_f32_16x16x64_bf16 v[84:87], v[60:63], a[184:191], %[idx]\n\t"
      "v_smfmac_f32_16x16x64_bf16 v[88:91], v[48:51], a[192:199], %[idx]\n\t"
      "s_nop 0\n\t"
      "s_nop 1\n\t"
      "v_smfmac_f32_16x16x64_bf16 v[88:91], v[52:55], a[200:207], %[idx]\n\t"
      "v_cvt_pk_bf16_f32 v96, v80, v81\n\t"
      "v_smfmac_f32_16x16x64_bf16 v[88:91], v[56:59], a[208:215], %[idx]\n\t"
      "v_cvt_pk_bf16_f32 v97, v82, v83\n\t"
      "v_smfmac_f32_16x16x64_bf16 v[88:91], v[60:63], a[216:223], %[idx]\n\t"
      "v_cvt_pk_bf16_f32 v98, v84, v85\n\t"
      "v_smfmac_f32_16x16x64_bf16 v[92:95], v[48:51], a[224:231], %[idx]\n\t"
      "v_cvt_pk_bf16_f32 v99, v86, v87\n\t"
      "v_smfmac_f32_16x16x64_bf16 v[92:95], v[52:55], a[232:239], %[idx]\n\t"
      "v_permlane16_swap_b32 v96, v98\n\t"
      "v_smfmac_f32_16x16x64_bf16 v[92:95], v[56:59], a[240:247], %[idx]\n\t"
      "v_permlane16_swap_b32 v97, v99\n\t"
      "v_smfmac_f32_16x16x64_bf16 v[92:95], v[60:63], a[248:255], %[idx]\n\t"
      "buffer_store_dwordx4 v[96:99], %[yo0], %[rc], s75 offen sc1\n\t"
      "v_cvt_pk_bf16_f32 v100, v88, v89\n\t"
      "v_cvt_pk_bf16_f32 v101, v90, v91\n\t"
      "s_nop 4\n\t"
      "v_cvt_pk_bf16_f32 v102, v92, v93\n\t"
      "v_cvt_pk_bf16_f32 v103, v94, v95\n\t"
      "s_nop 0\n\t"
      "v_permlane16_swap_b32 v100, v102\n\t"
      "v_permlane16_swap_b32 v101, v103\n\t"
      "buffer_store_dwordx4 v[100:103], %[yo1], %[rc], s75 offen sc1\n\t"
      "v_mov_b64 v[80:81], 0\n\t"
      "v_mov_b64 v[82:83], 0\n\t"
      "v_mov_b64 v[84:85], 0\n\t"
      "v_mov_b64 v[86:87], 0\n\t"
      "v_mov_b64 v[88:89], 0\n\t"
      "v_mov_b64 v[90:91], 0\n\t"
      "v_mov_b64 v[92:93], 0\n\t"
      "v_mov_b64 v[94:95], 0\n\t"
      "s_cmp_lt_u32 %[wv], 2\n\t"
      "s_cbranch_scc0 LYS1_%=\n\t"
      "buffer_store_dwordx4 %[sent], %[vL], %[rc], s72 offen offset:0 sc1\n\t"
      "buffer_store_dwordx4 %[sent], %[vL], %[rc], s72 offen offset:1024 sc1\n\t"
      "buffer_store_dwordx4 %[sent], %[vL], %[rc], s72 offen offset:2048 sc1\n\t"
      "buffer_store_dwordx4 %[sent], %[vL], %[rc], s72 offen offset:3072 sc1\n\t"
      "LYT1_%=:\n\t"
      "s_cmp_eq_u32 s68, 63\n\t"
      "s_cbranch_scc1 LLAST_%=\n\t"
      "s_waitcnt vmcnt(6)\n\t"
      "v_readfirstlane_b32 s61, v106\n\t"
      "v_readfirstlane_b32 s56, v107\n\t"
      "s_sub_u32 s58, s58, 38\n\t"
      "s_sub_u32 s69, s58, s60\n\t"
      "s_cmp_gt_i32 s69, 12\n\t"
      "s_cselect_b32 s60, s58, s60\n\t"
      "s_add_u32 s69, s68, %[tag]\n\t"
      "s_cmp_eq_u32 s69, s56\n\t"
      "s_cselect_b32 s61, s61, s60\n\t"
      "s_sub_u32 s69, s61, s60\n\t"
      "s_add_u32 s69, s69, 400\n\t"
      "s_cmp_lt_u32 s69, 800\n\t"
      "s_cselect_b32 s60, s61, s60\n\t"
      "s_cmp_lt_u32 %[wv], 2\n\t"
      "s_cbranch_scc0 LNR1_%=\n\t"
      "s_add_u32 s61, s60, 109\n\t"
      "LWX01_%=:\n\t"
      "s_memrealtime s[56:57]\n\t"
      "s_waitcnt lgkmcnt(0)\n\t"
      "s_sub_u32 s69, s61, s56\n\t"
      "s_cmp_gt_i32 s69, 0\n\t"
      "s_cbranch_scc1 LWX01_%=\n\t"
      "buffer_load_dwordx4 v[144:147], %[vL], %[rc], s66 offen offset:0 sc1\n\t"
      "buffer_load_dwordx4 v[148:151], %[vL], %[rc], s66 offen offset:1024 sc1\n\t"
      "buffer_load_dwordx4 v[152:155], %[vL], %[rc], s66 offen offset:2048 sc1\n\t"
      "buffer_load_dwordx4 v[156:159], %[vL], %[rc], s66 offen offset:3072 sc1\n\t"
      "s_add_u32 s61, s60, 112\n\t"
      "LWX11_%=:\n\t"
      "s_memrealtime s[56:57]\n\t"
      "s_waitcnt lgkmcnt(0)\n\t"
      "s_sub_u32 s69, s61, s56\n\t"
      "s_cmp_gt_i32 s69, 0\n\t"
      "s_cbranch_scc1 LWX11_%=\n\t"
      "buffer_load_dwordx4 v[128:131], %[vL], %[rc], s54 offen offset:0 sc1\n\t"
      "buffer_load_dwordx4 v[132:135], %[vL], %[rc], s54 offen offset:1024 sc1\n\t"
      "buffer_load_dwordx4 v[136:139], %[vL], %[rc], s54 offen offset:2048 sc1\n\t"
      "buffer_load_dwordx4 v[140:143], %[vL], %[rc], s54 offen offset:3072 sc1\n\t"
      "s_add_u32 s61, s60, 120\n\t"
      "LWX21_%=:\n\t"
      "s_memrealtime s[56:57]\n\t"
      "s_waitcnt lgkmcnt(0)\n\t"
      "s_sub_u32 s69, s61, s56\n\t"
      "s_cmp_gt_i32 s69, 0\n\t"
      "s_cbranch_scc1 LWX21_%=\n\t"
      "buffer_load_dwordx4 v[112:115], %[vL], %[rc], s67 offen offset:0 sc1\n\t"
      "buffer_load_dwordx4 v[116:119], %[vL], %[rc], s67 offen offset:1024 sc1\n\t"
      "buffer_load_dwordx4 v[120:123], %[vL], %[rc], s67 offen offset:2048 sc1\n\t"
      "buffer_load_dwordx4 v[124:127], %[vL], %[rc], s67 offen offset:3072 sc1\n\t"
      "s_waitcnt vmcnt(7)\n\t"
      "v_mfma_f32_16x16x32_bf16 v[160:163], v[144:147], v[128:131], 0\n\t"
      "v_max3_u32 v104, v128, v129, v130\n\t"
      "s_waitcnt vmcnt(6)\n\t"
      "v_mfma_f32_16x16x32_bf16 v[160:163], v[148:151], v[132:135], v[160:163]\n\t"
      "v_max3_u32 v105, v131, v132, v133\n\t"
      "v_max3_u32 v104, v104, v134, v135\n\t"
      "s_waitcnt vmcnt(5)\n\t"
      "v_mfma_f32_16x16x32_bf16 v[160:163], v[152:155], v[136:139], v[160:163]\n\t"
      "v_max3_u32 v105, v105, v136, v137\n\t"
      "v_max3_u32 v104, v104, v138, v139\n\t"
      "s_waitcnt vmcnt(4)\n\t"
      "v_mfma_f32_16x16x32_bf16 v[160:163], v[156:159], v[140:143], v[160:163]\n\t"
      "v_max3_u32 v105, v105, v140, v141\n\t"
      "v_max3_u32 v104, v104, v142, v143\n\t"
      "v_max_u32 v104, v104, v105\n\t"
      "v_cmp_eq_u32 vcc, -1, v104\n\t"
      "s_cbranch_vccnz LXF1_%=\n\t"
      "s_waitcnt vmcnt(3)\n\t"
      "v_mfma_f32_16x16x32_bf16 v[160:163], v[112:115], %[sel], v[160:163]\n\t"
      "s_waitcnt vmcnt(2)\n\t"
      "v_mfma_f32_16x16x32_bf16 v[160:163], v[116:119], %[sel], v[160:163]\n\t"
      "s_waitcnt vmcnt(1)\n\t"
      "v_mfma_f32_16x16x32_bf16 v[160:163], v[120:123], %[sel], v[160:163]\n\t"
      "s_waitcnt vmcnt(0)\n\t"
      "v_mfma_f32_16x16x32_bf16 v[160:163], v[124:127], %[sel], v[160:163]\n\t"
      "s_nop 7\n\t"
      "v_permlane32_swap_b32 v160, v162\n\t"
      "v_permlane32_swap_b32 v161, v163\n\t"
      "s_nop 0\n\t"
      "v_add_f32_dpp v104, v162, v160 row_ror:8 row_mask:0xf bank_mask:0x3\n\t"
      "v_add_f32_dpp v104, v160, v162 row_ror:8 row_mask:0xf bank_mask:0xc\n\t"
      "v_add_f32_dpp v105, v163, v161 row_ror:8 row_mask:0xf bank_mask:0x3\n\t"
      "v_add_f32_dpp v105, v161, v163 row_ror:8 row_mask:0xf bank_mask:0xc\n\t"
      "v_cvt_pk_bf16_f32 v164, v104, v105\n\t"
      "v_cvt_pk_bf16_f32 v166, v104, v105\n\t"
      "v_cmp_u_f32 vcc, v104, v105\n\t"
      "s_cbranch_vccnz LXF1_%=\n\t"
      "LXG1_%=:\n\t"
      "v_permlane16_swap_b32 v164, v166\n\t"
      "v_mov_b32 v165, v164\n\t"
      "v_mov_b32 v167, v166\n\t"
      "s_nop 0\n\t"
      "v_permlane32_swap_b32 v164, v165\n\t"
      "v_permlane32_swap_b32 v166, v167\n\t"
      "buffer_store_dwordx4 v[164:167], %[mo], %[rc], s79 offen sc1\n\t"
      "LNR1_%=:\n\t"
      "s_add_u32 s69, s55, 2\n\t"
      "s_cmp_lt_u32 s68, s69\n\t"
      "s_cselect_b32 s69, 30, 0\n\t"
      "s_add_u32 s60, s60, s69\n\t"
      "s_add_u32 s60, s60, 169\n\t"
      "s_add_u32 s68, s68, 1\n\t"
      "s_branch LLOOP_%=\n\t"
      "LLAST_%=:\n\t"
      "s_add_u32 s78, s78, 262144\n\t"
      "buffer_store_dwordx4 v[96:99], %[yt0], %[rY], s78 offen offset:0 sc1\n\t"
      "buffer_store_dwordx4 v[100:103], %[yt0], %[rY], s78 offen offset:1024 sc1\n\t"
      "s_add_u32 s69, s76, 64\n\t"
      "v_mov_b32 v111, s69\n\t"
      "s_waitcnt vmcnt(0)\n\t"
      "s_barrier\n\t"
      "buffer_store_dword v111, %[vFc], %[rc], 0 offen sc1\n\t"
      "s_cmp_eq_u32 s55, 1\n\t"
      "s_cbranch_scc1 LDONE_%=\n\t"
      "s_mov_b32 s55, 1\n\t"
      "s_xor_b32 %[oM], %[oM], %[dR]\n\t"
      "s_xor_b32 %[oAn], %[oAn], %[dR]\n\t"
      "s_add_u32 %[tag], %[tag], 256\n\t"
      "v_xor_b32 %[mo], %[dR], %[mo]\n\t"
      "v_xor_b32 %[vAn], %[dR], %[vAn]\n\t"
      "v_xor_b32 %[mr], %[dM], %[mr]\n\t"
      "s_branch LLAYER_%=\n\t"
      "LDONE_%=:\n\t"
      "s_mov_b32 %[st], 0\n\t"
      "s_branch LEND_%=\n\t"
      "LRS0_%=:\n\t"
      "s_waitcnt vmcnt(0)\n\t"
      "s_branch LRT0_%=\n\t"
      "LAN0_%=:\n\t"
      "s_waitcnt lgkmcnt(0)\n\t"
      "s_sub_u32 s69, s56, s60\n\t"
      "s_cmp_gt_i32 s69, 54\n\t"
      "s_cselect_b32 s61, 18, 32\n\t"
      "s_sub_u32 s56, s56, s61\n\t"
      "s_cmp_gt_i32 s69, 44\n\t"
      "s_cselect_b32 s61, s56, s60\n\t"
      "v_mov_b32 v168, s61\n\t"
      "buffer_store_dwordx2 v[168:169], %[vAn], %[rc], s62 offen sc1\n\t"
      "s_branch LAR0_%=\n\t"
      "LFB0_%=:\n\t"
      "s_mov_b64 exec, s[96:97]\n\t"
      "buffer_load_dwordx4 v[40:43], %[vMw], %[rc], s70 offen sc1\n\t"
      "s_mov_b64 exec, s[98:99]\n\t"
      "buffer_load_dwordx4 v[44:47], %[vMw], %[rc], s70 offen offset:1024 sc1\n\t"
      "s_mov_b64 exec, -1\n\t"
      "s_sleep 1\n\t"
      "s_mov_b64 exec, s[96:97]\n\t"
      "buffer_load_dwordx4 v[40:43], %[vMw], %[rc], s70 offen sc1\n\t"
      "s_mov_b64 exec, s[98:99]\n\t"
      "buffer_load_dwordx4 v[44:47], %[vMw], %[rc], s70 offen offset:1024 sc1\n\t"
      "s_mov_b64 exec, -1\n\t"
      "s_sleep 1\n\t"
      "LHB0_%=:\n\t"
      "s_mov_b64 exec, s[96:97]\n\t"
      "buffer_load_dwordx4 v[40:43], %[vMw], %[rc], s70 offen sc1\n\t"
      "s_mov_b64 exec, s[98:99]\n\t"
      "buffer_load_dwordx4 v[44:47], %[vMw], %[rc], s70 offen offset:1024 sc1\n\t"
      "s_mov_b64 exec, -1\n\t"
      "s_add_u32 s71, s71, 1\n\t"
      "s_cmp_gt_u32 s71, 131072\n\t"
      "s_cbranch_scc1 LFAIL_%=\n\t"
      "s_waitcnt vmcnt(5)\n\t"
      "v_max3_u32 v104, v40, v41, v42\n\t"
      "v_max_u32 v104, v104, v43\n\t"
      "v_cmp_eq_u32 s[96:97], -1, v104\n\t"
      "s_waitcnt vmcnt(4)\n\t"
      "v_max3_u32 v105, v44, v45, v46\n\t"
      "v_max_u32 v105, v105, v47\n\t"
      "v_cmp_eq_u32 s[98:99], -1, v105\n\t"
      "s_or_b64 s[100:101], s[96:97], s[98:99]\n\t"
      "s_cbranch_scc1 LHB0_%=\n\t"
      "s_branch LGB0_%=\n\t"
      "LYS0_%=:\n\t"
      "s_waitcnt vmcnt(2)\n\t"
      "s_branch LYT0_%=\n\t"
      "LXF0_%=:\n\t"
      "buffer_load_dwordx4 v[128:131], %[vL], %[rc], %[oP] offen offset:0 sc1\n\t"
      "buffer_load_dwordx4 v[132:135], %[vL], %[rc], %[oP] offen offset:1024 sc1\n\t"
      "buffer_load_dwordx4 v[136:139], %[vL], %[rc], %[oP] offen offset:2048 sc1\n\t"
      "buffer_load_dwordx4 v[140:143], %[vL], %[rc], %[oP] offen offset:3072 sc1\n\t"
      "buffer_load_dwordx4 v[112:115], %[vL], %[rc], %[oX] offen offset:0 sc1\n\t"
      "buffer_load_dwordx4 v[116:119], %[vL], %[rc], %[oX] offen offset:1024 sc1\n\t"
      "buffer_load_dwordx4 v[120:123], %[vL], %[rc], %[oX] offen offset:2048 sc1\n\t"
      "buffer_load_dwordx4 v[124:127], %[vL], %[rc], %[oX] offen offset:3072 sc1\n\t"
      "s_waitcnt vmcnt(7)\n\t"
      "v_mfma_f32_16x16x32_bf16 v[160:163], v[144:147], v[128:131], 0\n\t"
      "v_max3_u32 v104, v128, v129, v130\n\t"
      "s_waitcnt vmcnt(6)\n\t"
      "v_mfma_f32_16x16x32_bf16 v[160:163], v[148:151], v[132:135], v[160:163]\n\t"
      "v_max3_u32 v105, v131, v132, v133\n\t"
      "v_max3_u32 v104, v104, v134, v135\n\t"
      "s_waitcnt vmcnt(5)\n\t"
      "v_mfma_f32_16x16x32_bf16 v[160:163], v[152:155], v[136:139], v[160:163]\n\t"
      "v_max3_u32 v105, v105, v136, v137\n\t"
      "v_max3_u32 v104, v104, v138, v139\n\t"
      "s_waitcnt vmcnt(4)\n\t"
      "v_mfma_f32_16x16x32_bf16 v[160:163], v[156:159], v[140:143], v[160:163]\n\t"
      "v_max3_u32 v105, v105, v140, v141\n\t"
      "v_max3_u32 v104, v104, v142, v143\n\t"
      "v_max_u32 v104, v104, v105\n\t"
      "v_cmp_eq_u32 vcc, -1, v104\n\t"
      "s_cbranch_vccnz LXE10_%=\n\t"
      "s_waitcnt vmcnt(3)\n\t"
      "v_mfma_f32_16x16x32_bf16 v[160:163], v[112:115], %[sel], v[160:163]\n\t"
      "s_waitcnt vmcnt(2)\n\t"
      "v_mfma_f32_16x16x32_bf16 v[160:163], v[116:119], %[sel], v[160:163]\n\t"
      "s_waitcnt vmcnt(1)\n\t"
      "v_mfma_f32_16x16x32_bf16 v[160:163], v[120:123], %[sel], v[160:163]\n\t"
      "s_waitcnt vmcnt(0)\n\t"
      "v_mfma_f32_16x16x32_bf16 v[160:163], v[124:127], %[sel], v[160:163]\n\t"
      "s_nop 7\n\t"
      "v_permlane32_swap_b32 v160, v162\n\t"
      "v_permlane32_swap_b32 v161, v163\n\t"
      "s_nop 0\n\t"
      "v_add_f32_dpp v104, v162, v160 row_ror:8 row_mask:0xf bank_mask:0x3\n\t"
      "v_add_f32_dpp v104, v160, v162 row_ror:8 row_mask:0xf bank_mask:0xc\n\t"
      "v_add_f32_dpp v105, v163, v161 row_ror:8 row_mask:0xf bank_mask:0x3\n\t"
      "v_add_f32_dpp v105, v161, v163 row_ror:8 row_mask:0xf bank_mask:0xc\n\t"
      "v_cvt_pk_bf16_f32 v164, v104, v105\n\t"
      "v_cvt_pk_bf16_f32 v166, v104, v105\n\t"
      "v_cmp_u_f32 vcc, v104, v105\n\t"
      "s_cbranch_vccz LXG0_%=\n\t"
      "LXE10_%=:\n\t"
      "buffer_load_dwordx4 v[128:131], %[vL], %[rc], %[oP] offen offset:0 sc1\n\t"
      "buffer_load_dwordx4 v[132:135], %[vL], %[rc], %[oP] offen offset:1024 sc1\n\t"
      "buffer_load_dwordx4 v[136:139], %[vL], %[rc], %[oP] offen offset:2048 sc1\n\t"
      "buffer_load_dwordx4 v[140:143], %[vL], %[rc], %[oP] offen offset:3072 sc1\n\t"
      "buffer_load_dwordx4 v[112:115], %[vL], %[rc], %[oX] offen offset:0 sc1\n\t"
      "buffer_load_dwordx4 v[116:119], %[vL], %[rc], %[oX] offen offset:1024 sc1\n\t"
      "buffer_load_dwordx4 v[120:123], %[vL], %[rc], %[oX] offen offset:2048 sc1\n\t"
      "buffer_load_dwordx4 v[124:127], %[vL], %[rc], %[oX] offen offset:3072 sc1\n\t"
      "s_waitcnt vmcnt(7)\n\t"
      "v_mfma_f32_16x16x32_bf16 v[160:163], v[144:147], v[128:131], 0\n\t"
      "v_max3_u32 v104, v128, v129, v130\n\t"
      "s_waitcnt vmcnt(6)\n\t"
      "v_mfma_f32_16x16x32_bf16 v[160:163], v[148:151], v[132:135], v[160:163]\n\t"
      "v_max3_u32 v105, v131, v132, v133\n\t"
      "v_max3_u32 v104, v104, v134, v135\n\t"
      "s_waitcnt vmcnt(5)\n\t"
      "v_mfma_f32_16x16x32_bf16 v[160:163], v[152:155], v[136:139], v[160:163]\n\t"
      "v_max3_u32 v105, v105, v136, v137\n\t"
      "v_max3_u32 v104, v104, v138, v139\n\t"
      "s_waitcnt vmcnt(4)\n\t"
      "v_mfma_f32_16x16x32_bf16 v[160:163], v[156:159], v[140:143], v[160:163]\n\t"
      "v_max3_u32 v105, v105, v140, v141\n\t"
      "v_max3_u32 v104, v104, v142, v143\n\t"
      "v_max_u32 v104, v104, v105\n\t"
      "v_cmp_eq_u32 vcc, -1, v104\n\t"
      "s_cbranch_vccnz LXE20_%=\n\t"
      "s_waitcnt vmcnt(3)\n\t"
      "v_mfma_f32_16x16x32_bf16 v[160:163], v[112:115], %[sel], v[160:163]\n\t"
      "s_waitcnt vmcnt(2)\n\t"
      "v_mfma_f32_16x16x32_bf16 v[160:163], v[116:119], %[sel], v[160:163]\n\t"
      "s_waitcnt vmcnt(1)\n\t"
      "v_mfma_f32_16x16x32_bf16 v[160:163], v[120:123], %[sel], v[160:163]\n\t"
      "s_waitcnt vmcnt(0)\n\t"
      "v_mfma_f32_16x16x32_bf16 v[160:163], v[124:127], %[sel], v[160:163]\n\t"
      "s_nop 7\n\t"
      "v_permlane32_swap_b32 v160, v162\n\t"
      "v_permlane32_swap_b32 v161, v163\n\t"
      "s_nop 0\n\t"
      "v_add_f32_dpp v104, v162, v160 row_ror:8 row_mask:0xf bank_mask:0x3\n\t"
      "v_add_f32_dpp v104, v160, v162 row_ror:8 row_mask:0xf bank_mask:0xc\n\t"
      "v_add_f32_dpp v105, v163, v161 row_ror:8 row_mask:0xf bank_mask:0x3\n\t"
      "v_add_f32_dpp v105, v161, v163 row_ror:8 row_mask:0xf bank_mask:0xc\n\t"
      "v_cvt_pk_bf16_f32 v164, v104, v105\n\t"
      "v_cvt_pk_bf16_f32 v166, v104, v105\n\t"
      "v_cmp_u_f32 vcc, v104, v105\n\t"
      "s_cbranch_vccz LXG0_%=\n\t"
      "LXE20_%=:\n\t"
      "buffer_load_dwordx4 v[144:147], %[vL], %[rc], s72 offen offset:0 sc1\n\t"
      "buffer_load_dwordx4 v[148:151], %[vL], %[rc], s72 offen offset:1024 sc1\n\t"
      "buffer_load_dwordx4 v[152:155], %[vL], %[rc], s72 offen offset:2048 sc1\n\t"
      "buffer_load_dwordx4 v[156:159], %[vL], %[rc], s72 offen offset:3072 sc1\n\t"
      "buffer_load_dwordx4 v[128:131], %[vL], %[rc], %[oP] offen offset:0 sc1\n\t"
      "buffer_load_dwordx4 v[132:135], %[vL], %[rc], %[oP] offen offset:1024 sc1\n\t"
      "buffer_load_dwordx4 v[136:139], %[vL], %[rc], %[oP] offen offset:2048 sc1\n\t"
      "buffer_load_dwordx4 v[140:143], %[vL], %[rc], %[oP] offen offset:3072 sc1\n\t"
      "buffer_load_dwordx4 v[112:115], %[vL], %[rc], %[oX] offen offset:0 sc1\n\t"
      "buffer_load_dwordx4 v[116:119], %[vL], %[rc], %[oX] offen offset:1024 sc1\n\t"
      "buffer_load_dwordx4 v[120:123], %[vL], %[rc], %[oX] offen offset:2048 sc1\n\t"
      "buffer_load_dwordx4 v[124:127], %[vL], %[rc], %[oX] offen offset:3072 sc1\n\t"
      "s_waitcnt vmcnt(7)\n\t"
      "v_mfma_f32_16x16x32_bf16 v[160:163], v[144:147], v[128:131], 0\n\t"
      "v_max3_u32 v104, v128, v129, v130\n\t"
      "s_waitcnt vmcnt(6)\n\t"
      "v_mfma_f32_16x16x32_bf16 v[160:163], v[148:151], v[132:135], v[160:163]\n\t"
      "v_max3_u32 v105, v131, v132, v133\n\t"
      "v_max3_u32 v104, v104, v134, v135\n\t"
      "s_waitcnt vmcnt(5)\n\t"
      "v_mfma_f32_16x16x32_bf16 v[160:163], v[152:155], v[136:139], v[160:163]\n\t"
      "v_max3_u32 v105, v105, v136, v137\n\t"
      "v_max3_u32 v104, v104, v138, v139\n\t"
      "s_waitcnt vmcnt(4)\n\t"
      "v_mfma_f32_16x16x32_bf16 v[160:163], v[156:159], v[140:143], v[160:163]\n\t"
      "v_max3_u32 v105, v105, v140, v141\n\t"
      "v_max3_u32 v104, v104, v142, v143\n\t"
      "v_max_u32 v104, v104, v105\n\t"
      "v_cmp_eq_u32 vcc, -1, v104\n\t"
      "s_cbranch_vccnz LXE30_%=\n\t"
      "s_waitcnt vmcnt(3)\n\t"
      "v_mfma_f32_16x16x32_bf16 v[160:163], v[112:115], %[sel], v[160:163]\n\t"
      "s_waitcnt vmcnt(2)\n\t"
      "v_mfma_f32_16x16x32_bf16 v[160:163], v[116:119], %[sel], v[160:163]\n\t"
      "s_waitcnt vmcnt(1)\n\t"
      "v_mfma_f32_16x16x32_bf16 v[160:163], v[120:123], %[sel], v[160:163]\n\t"
      "s_waitcnt vmcnt(0)\n\t"
      "v_mfma_f32_16x16x32_bf16 v[160:163], v[124:127], %[sel], v[160:163]\n\t"
      "s_nop 7\n\t"
      "v_permlane32_swap_b32 v160, v162\n\t"
      "v_permlane32_swap_b32 v161, v163\n\t"
      "s_nop 0\n\t"
      "v_add_f32_dpp v104, v162, v160 row_ror:8 row_mask:0xf bank_mask:0x3\n\t"
      "v_add_f32_dpp v104, v160, v162 row_ror:8 row_mask:0xf bank_mask:0xc\n\t"
      "v_add_f32_dpp v105, v163, v161 row_ror:8 row_mask:0xf bank_mask:0x3\n\t"
      "v_add_f32_dpp v105, v161, v163 row_ror:8 row_mask:0xf bank_mask:0xc\n\t"
      "v_cvt_pk_bf16_f32 v164, v104, v105\n\t"
      "v_cvt_pk_bf16_f32 v166, v104, v105\n\t"
      "v_cmp_u_f32 vcc, v104, v105\n\t"
      "s_cbranch_vccz LXG0_%=\n\t"
      "s_branch LXH0_%=\n\t"
      "LXS0_%=:\n\t"
      "s_sleep 2\n\t"
      "LXE30_%=:\n\t"
      "s_add_u32 s71, s71, 1\n\t"
      "s_cmp_gt_u32 s71, 131072\n\t"
      "s_cbranch_scc1 LFAIL_%=\n\t"
      "v_lshlrev_b32 v104, 2, %[vL]\n\t"
      "buffer_load_dword v105, v104, %[rc], %[oP] offen sc1\n\t"
      "s_waitcnt vmcnt(0)\n\t"
      "v_cmp_ne_u32 vcc, -1, v105\n\t"
      "s_cbranch_vccz LXS0_%=\n\t"
      "LXL0_%=:\n\t"
      "s_add_u32 s71, s71, 1\n\t"
      "s_cmp_gt_u32 s71, 131072\n\t"
      "s_cbranch_scc1 LFAIL_%=\n\t"
      "buffer_load_dwordx4 v[128:131], %[vL], %[rc], %[oP] offen offset:0 sc1\n\t"
      "buffer_load_dwordx4 v[132:135], %[vL], %[rc], %[oP] offen offset:1024 sc1\n\t"
      "buffer_load_dwordx4 v[136:139], %[vL], %[rc], %[oP] offen offset:2048 sc1\n\t"
      "buffer_load_dwordx4 v[140:143], %[vL], %[rc], %[oP] offen offset:3072 sc1\n\t"
      "buffer_load_dwordx4 v[112:115], %[vL], %[rc], %[oX] offen offset:0 sc1\n\t"
      "buffer_load_dwordx4 v[116:119], %[vL], %[rc], %[oX] offen offset:1024 sc1\n\t"
      "buffer_load_dwordx4 v[120:123], %[vL], %[rc], %[oX] offen offset:2048 sc1\n\t"
      "buffer_load_dwordx4 v[124:127], %[vL], %[rc], %[oX] offen offset:3072 sc1\n\t"
      "s_waitcnt vmcnt(7)\n\t"
      "v_mfma_f32_16x16x32_bf16 v[160:163], v[144:147], v[128:131], 0\n\t"
      "v_max3_u32 v104, v128, v129, v130\n\t"
      "s_waitcnt vmcnt(6)\n\t"
      "v_mfma_f32_16x16x32_bf16 v[160:163], v[148:151], v[132:135], v[160:163]\n\t"
      "v_max3_u32 v105, v131, v132, v133\n\t"
      "v_max3_u32 v104, v104, v134, v135\n\t"
      "s_waitcnt vmcnt(5)\n\t"
      "v_mfma_f32_16x16x32_bf16 v[160:163], v[152:155], v[136:139], v[160:163]\n\t"
      "v_max3_u32 v105, v105, v136, v137\n\t"
      "v_max3_u32 v104, v104, v138, v139\n\t"
      "s_waitcnt vmcnt(4)\n\t"
      "v_mfma_f32_16x16x32_bf16 v[160:163], v[156:159], v[140:143], v[160:163]\n\t"
      "v_max3_u32 v105, v105, v140, v141\n\t"
      "v_max3_u32 v104, v104, v142, v143\n\t"
      "v_max_u32 v104, v104, v105\n\t"
      "v_cmp_eq_u32 vcc, -1, v104\n\t"
      "s_cbranch_vccnz LXL0_%=\n\t"
      "s_waitcnt vmcnt(3)\n\t"
      "v_mfma_f32_16x16x32_bf16 v[160:163], v[112:115], %[sel], v[160:163]\n\t"
      "s_waitcnt vmcnt(2)\n\t"
      "v_mfma_f32_16x16x32_bf16 v[160:163], v[116:119], %[sel], v[160:163]\n\t"
      "s_waitcnt vmcnt(1)\n\t"
      "v_mfma_f32_16x16x32_bf16 v[160:163], v[120:123], %[sel], v[160:163]\n\t"
      "s_waitcnt vmcnt(0)\n\t"
      "v_mfma_f32_16x16x32_bf16 v[160:163], v[124:127], %[sel], v[160:163]\n\t"
      "s_nop 7\n\t"
      "v_permlane32_swap_b32 v160, v162\n\t"
      "v_permlane32_swap_b32 v161, v163\n\t"
      "s_nop 0\n\t"
      "v_add_f32_dpp v104, v162, v160 row_ror:8 row_mask:0xf bank_mask:0x3\n\t"
      "v_add_f32_dpp v104, v160, v162 row_ror:8 row_mask:0xf bank_mask:0xc\n\t"
      "v_add_f32_dpp v105, v163, v161 row_ror:8 row_mask:0xf bank_mask:0x3\n\t"
      "v_add_f32_dpp v105, v161, v163 row_ror:8 row_mask:0xf bank_mask:0xc\n\t"
      "v_cvt_pk_bf16_f32 v164, v104, v105\n\t"
      "v_cvt_pk_bf16_f32 v166, v104, v105\n\t"
      "v_cmp_u_f32 vcc, v104, v105\n\t"
      "s_cbranch_vccz LXG0_%=\n\t"
      "s_branch LXE20_%=\n\t"
      "LXH0_%=:\n\t"
      "s_mov_b64 s[100:101], 0\n\t"
      "v_max3_u32 v104, v144, v145, v146\n\t"
      "v_max_u32 v104, v104, v147\n\t"
      "v_cmp_eq_u32 vcc, -1, v104\n\t"
      "s_or_b64 s[100:101], s[100:101], vcc\n\t"
      "s_mov_b64 exec, vcc\n\t"
      "buffer_load_dwordx4 v[144:147], %[vL], %[rc], s72 offen offset:0 sc1\n\t"
      "s_mov_b64 exec, -1\n\t"
      "v_max3_u32 v104, v148, v149, v150\n\t"
      "v_max_u32 v104, v104, v151\n\t"
      "v_cmp_eq_u32 vcc, -1, v104\n\t"
      "s_or_b64 s[100:101], s[100:101], vcc\n\t"
      "s_mov_b64 exec, vcc\n\t"
      "buffer_load_dwordx4 v[148:151], %[vL], %[rc], s72 offen offset:1024 sc1\n\t"
      "s_mov_b64 exec, -1\n\t"
      "v_max3_u32 v104, v152, v153, v154\n\t"
      "v_max_u32 v104, v104, v155\n\t"
      "v_cmp_eq_u32 vcc, -1, v104\n\t"
      "s_or_b64 s[100:101], s[100:101], vcc\n\t"
      "s_mov_b64 exec, vcc\n\t"
      "buffer_load_dwordx4 v[152:155], %[vL], %[rc], s72 offen offset:2048 sc1\n\t"
      "s_mov_b64 exec, -1\n\t"
      "v_max3_u32 v104, v156, v157, v158\n\t"
      "v_max_u32 v104, v104, v159\n\t"
      "v_cmp_eq_u32 vcc, -1, v104\n\t"
      "s_or_b64 s[100:101], s[100:101], vcc\n\t"
      "s_mov_b64 exec, vcc\n\t"
      "buffer_load_dwordx4 v[156:159], %[vL], %[rc], s72 offen offset:3072 sc1\n\t"
      "s_mov_b64 exec, -1\n\t"
      "v_max3_u32 v104, v128, v129, v130\n\t"
      "v_max_u32 v104, v104, v131\n\t"
      "v_cmp_eq_u32 vcc, -1, v104\n\t"
      "s_or_b64 s[100:101], s[100:101], vcc\n\t"
      "s_mov_b64 exec, vcc\n\t"
      "buffer_load_dwordx4 v[128:131], %[vL], %[rc], %[oP] offen offset:0 sc1\n\t"
      "s_mov_b64 exec, -1\n\t"
      "v_max3_u32 v104, v132, v133, v134\n\t"
      "v_max_u32 v104, v104, v135\n\t"
      "v_cmp_eq_u32 vcc, -1, v104\n\t"
      "s_or_b64 s[100:101], s[100:101], vcc\n\t"
      "s_mov_b64 exec, vcc\n\t"
      "buffer_load_dwordx4 v[132:135], %[vL], %[rc], %[oP] offen offset:1024 sc1\n\t"
      "s_mov_b64 exec, -1\n\t"
      "v_max3_u32 v104, v136, v137, v138\n\t"
      "v_max_u32 v104, v104, v139\n\t"
      "v_cmp_eq_u32 vcc, -1, v104\n\t"
      "s_or_b64 s[100:101], s[100:101], vcc\n\t"
      "s_mov_b64 exec, vcc\n\t"
      "buffer_load_dwordx4 v[136:139], %[vL], %[rc], %[oP] offen offset:2048 sc1\n\t"
      "s_mov_b64 exec, -1\n\t"
      "v_max3_u32 v104, v140, v141, v142\n\t"
      "v_max_u32 v104, v104, v143\n\t"
      "v_cmp_eq_u32 vcc, -1, v104\n\t"
      "s_or_b64 s[100:101], s[100:101], vcc\n\t"
      "s_mov_b64 exec, vcc\n\t"
      "buffer_load_dwordx4 v[140:143], %[vL], %[rc], %[oP] offen offset:3072 sc1\n\t"
      "s_mov_b64 exec, -1\n\t"
      "v_max3_u32 v104, v112, v113, v114\n\t"
      "v_max_u32 v104, v104, v115\n\t"
      "v_cmp_eq_u32 vcc, -1, v104\n\t"
      "s_or_b64 s[100:101], s[100:101], vcc\n\t"
      "s_mov_b64 exec, vcc\n\t"
      "buffer_load_dwordx4 v[112:115], %[vL], %[rc], %[oX] offen offset:0 sc1\n\t"
      "s_mov_b64 exec, -1\n\t"
      "v_max3_u32 v104, v116, v117, v118\n\t"
      "v_max_u32 v104, v104, v119\n\t"
      "v_cmp_eq_u32 vcc, -1, v104\n\t"
      "s_or_b64 s[100:101], s[100:101], vcc\n\t"
      "s_mov_b64 exec, vcc\n\t"
      "buffer_load_dwordx4 v[116:119], %[vL], %[rc], %[oX] offen offset:1024 sc1\n\t"
      "s_mov_b64 exec, -1\n\t"
      "v_max3_u32 v104, v120, v121, v122\n\t"
      "v_max_u32 v104, v104, v123\n\t"
      "v_cmp_eq_u32 vcc, -1, v104\n\t"
      "s_or_b64 s[100:101], s[100:101], vcc\n\t"
      "s_mov_b64 exec, vcc\n\t"
      "buffer_load_dwordx4 v[120:123], %[vL], %[rc], %[oX] offen offset:2048 sc1\n\t"
      "s_mov_b64 exec, -1\n\t"
      "v_max3_u32 v104, v124, v125, v126\n\t"
      "v_max_u32 v104, v104, v127\n\t"
      "v_cmp_eq_u32 vcc, -1, v104\n\t"
      "s_or_b64 s[100:101], s[100:101], vcc\n\t"
      "s_mov_b64 exec, vcc\n\t"
      "buffer_load_dwordx4 v[124:127], %[vL], %[rc], %[oX] offen offset:3072 sc1\n\t"
      "s_mov_b64 exec, -1\n\t"
      "s_cmp_eq_u64 s[100:101], 0\n\t"
      "s_cbranch_scc1 LXG0_%=\n\t"
      "s_add_u32 s71, s71, 1\n\t"
      "s_cmp_gt_u32 s71, 131072\n\t"
      "s_cbranch_scc1 LFAIL_%=\n\t"
      "s_waitcnt vmcnt(0)\n\t"
      "v_mfma_f32_16x16x32_bf16 v[160:163], v[144:147], v[128:131], 0\n\t"
      "v_mfma_f32_16x16x32_bf16 v[160:163], v[148:151], v[132:135], v[160:163]\n\t"
      "v_mfma_f32_16x16x32_bf16 v[160:163], v[152:155], v[136:139], v[160:163]\n\t"
      "v_mfma_f32_16x16x32_bf16 v[160:163], v[156:159], v[140:143], v[160:163]\n\t"
      "v_mfma_f32_16x16x32_bf16 v[160:163], v[112:115], %[sel], v[160:163]\n\t"
      "v_mfma_f32_16x16x32_bf16 v[160:163], v[116:119], %[sel], v[160:163]\n\t"
      "v_mfma_f32_16x16x32_bf16 v[160:163], v[120:123], %[sel], v[160:163]\n\t"
      "v_mfma_f32_16x16x32_bf16 v[160:163], v[124:127], %[sel], v[160:163]\n\t"
      "s_nop 7\n\t"
      "v_permlane32_swap_b32 v160, v162\n\t"
      "v_permlane32_swap_b32 v161, v163\n\t"
      "s_nop 0\n\t"
      "v_add_f32_dpp v104, v162, v160 row_ror:8 row_mask:0xf bank_mask:0x3\n\t"
      "v_add_f32_dpp v104, v160, v162 row_ror:8 row_mask:0xf bank_mask:0xc\n\t"
      "v_add_f32_dpp v105, v163, v161 row_ror:8 row_mask:0xf bank_mask:0x3\n\t"
      "v_add_f32_dpp v105, v161, v163 row_ror:8 row_mask:0xf bank_mask:0xc\n\t"
      "v_cvt_pk_bf16_f32 v164, v104, v105\n\t"
      "v_cvt_pk_bf16_f32 v166, v104, v105\n\t"
      "v_cmp_u_f32 vcc, v104, v105\n\t"
      "s_cbranch_vccnz LXH0_%=\n\t"
      "s_branch LXG0_%=\n\t"
      "LRS1_%=:\n\t"
      "s_waitcnt vmcnt(0)\n\t"
      "s_branch LRT1_%=\n\t"
      "LAN1_%=:\n\t"
      "s_waitcnt lgkmcnt(0)\n\t"
      "s_sub_u32 s69, s56, s60\n\t"
      "s_cmp_gt_i32 s69, 54\n\t"
      "s_cselect_b32 s61, 18, 32\n\t"
      "s_sub_u32 s56, s56, s61\n\t"
      "s_cmp_gt_i32 s69, 44\n\t"
      "s_cselect_b32 s61, s56, s60\n\t"
      "v_mov_b32 v168, s61\n\t"
      "buffer_store_dwordx2 v[168:169], %[vAn], %[rc], s62 offen sc1\n\t"
      "s_branch LAR1_%=\n\t"
      "LFB1_%=:\n\t"
      "s_mov_b64 exec, s[96:97]\n\t"
      "buffer_load_dwordx4 v[40:43], %[vMw], %[rc], s70 offen sc1\n\t"
      "s_mov_b64 exec, s[98:99]\n\t"
      "buffer_load_dwordx4 v[44:47], %[vMw], %[rc], s70 offen offset:1024 sc1\n\t"
      "s_mov_b64 exec, -1\n\t"
      "s_sleep 1\n\t"
      "s_mov_b64 exec, s[96:97]\n\t"
      "buffer_load_dwordx4 v[40:43], %[vMw], %[rc], s70 offen sc1\n\t"
      "s_mov_b64 exec, s[98:99]\n\t"
      "buffer_load_dwordx4 v[44:47], %[vMw], %[rc], s70 offen offset:1024 sc1\n\t"
      "s_mov_b64 exec, -1\n\t"
      "s_sleep 1\n\t"
      "LHB1_%=:\n\t"
      "s_mov_b64 exec, s[96:97]\n\t"
      "buffer_load_dwordx4 v[40:43], %[vMw], %[rc], s70 offen sc1\n\t"
      "s_mov_b64 exec, s[98:99]\n\t"
      "buffer_load_dwordx4 v[44:47], %[vMw], %[rc], s70 offen offset:1024 sc1\n\t"
      "s_mov_b64 exec, -1\n\t"
      "s_add_u32 s71, s71, 1\n\t"
      "s_cmp_gt_u32 s71, 131072\n\t"
      "s_cbranch_scc1 LFAIL_%=\n\t"
      "s_waitcnt vmcnt(5)\n\t"
      "v_max3_u32 v104, v40, v41, v42\n\t"
      "v_max_u32 v104, v104, v43\n\t"
      "v_cmp_eq_u32 s[96:97], -1, v104\n\t"
      "s_waitcnt vmcnt(4)\n\t"
      "v_max3_u32 v105, v44, v45, v46\n\t"
      "v_max_u32 v105, v105, v47\n\t"
      "v_cmp_eq_u32 s[98:99], -1, v105\n\t"
      "s_or_b64 s[100:101], s[96:97], s[98:99]\n\t"
      "s_cbranch_scc1 LHB1_%=\n\t"
      "s_branch LGB1_%=\n\t"
      "LYS1_%=:\n\t"
      "s_waitcnt vmcnt(2)\n\t"
      "s_branch LYT1_%=\n\t"
      "LXF1_%=:\n\t"
      "buffer_load_dwordx4 v[128:131], %[vL], %[rc], s54 offen offset:0 sc1\n\t"
      "buffer_load_dwordx4 v[132:135], %[vL], %[rc], s54 offen offset:1024 sc1\n\t"
      "buffer_load_dwordx4 v[136:139], %[vL], %[rc], s54 offen offset:2048 sc1\n\t"
      "buffer_load_dwordx4 v[140:143], %[vL], %[rc], s54 offen offset:3072 sc1\n\t"
      "buffer_load_dwordx4 v[112:115], %[vL], %[rc], s67 offen offset:0 sc1\n\t"
      "buffer_load_dwordx4 v[116:119], %[vL], %[rc], s67 offen offset:1024 sc1\n\t"
      "buffer_load_dwordx4 v[120:123], %[vL], %[rc], s67 offen offset:2048 sc1\n\t"
      "buffer_load_dwordx4 v[124:127], %[vL], %[rc], s67 offen offset:3072 sc1\n\t"
      "s_waitcnt vmcnt(7)\n\t"
      "v_mfma_f32_16x16x32_bf16 v[160:163], v[144:147], v[128:131], 0\n\t"
      "v_max3_u32 v104, v128, v129, v130\n\t"
      "s_waitcnt vmcnt(6)\n\t"
      "v_mfma_f32_16x16x32_bf16 v[160:163], v[148:151], v[132:135], v[160:163]\n\t"
      "v_max3_u32 v105, v131, v132, v133\n\t"
      "v_max3_u32 v104, v104, v134, v135\n\t"
      "s_waitcnt vmcnt(5)\n\t"
      "v_mfma_f32_16x16x32_bf16 v[160:163], v[152:155], v[136:139], v[160:163]\n\t"
      "v_max3_u32 v105, v105, v136, v137\n\t"
      "v_max3_u32 v104, v104, v138, v139\n\t"
      "s_waitcnt vmcnt(4)\n\t"
      "v_mfma_f32_16x16x32_bf16 v[160:163], v[156:159], v[140:143], v[160:163]\n\t"
      "v_max3_u32 v105, v105, v140, v141\n\t"
      "v_max3_u32 v104, v104, v142, v143\n\t"
      "v_max_u32 v104, v104, v105\n\t"
      "v_cmp_eq_u32 vcc, -1, v104\n\t"
      "s_cbranch_vccnz LXE11_%=\n\t"
      "s_waitcnt vmcnt(3)\n\t"
      "v_mfma_f32_16x16x32_bf16 v[160:163], v[112:115], %[sel], v[160:163]\n\t"
      "s_waitcnt vmcnt(2)\n\t"
      "v_mfma_f32_16x16x32_bf16 v[160:163], v[116:119], %[sel], v[160:163]\n\t"
      "s_waitcnt vmcnt(1)\n\t"
      "v_mfma_f32_16x16x32_bf16 v[160:163], v[120:123], %[sel], v[160:163]\n\t"
      "s_waitcnt vmcnt(0)\n\t"
      "v_mfma_f32_16x16x32_bf16 v[160:163], v[124:127], %[sel], v[160:163]\n\t"
      "s_nop 7\n\t"
      "v_permlane32_swap_b32 v160, v162\n\t"
      "v_permlane32_swap_b32 v161, v163\n\t"
      "s_nop 0\n\t"
      "v_add_f32_dpp v104, v162, v160 row_ror:8 row_mask:0xf bank_mask:0x3\n\t"
      "v_add_f32_dpp v104, v160, v162 row_ror:8 row_mask:0xf bank_mask:0xc\n\t"
      "v_add_f32_dpp v105, v163, v161 row_ror:8 row_mask:0xf bank_mask:0x3\n\t"
      "v_add_f32_dpp v105, v161, v163 row_ror:8 row_mask:0xf bank_mask:0xc\n\t"
      "v_cvt_pk_bf16_f32 v164, v104, v105\n\t"
      "v_cvt_pk_bf16_f32 v166, v104, v105\n\t"
      "v_cmp_u_f32 vcc, v104, v105\n\t"
      "s_cbranch_vccz LXG1_%=\n\t"
      "LXE11_%=:\n\t"
      "buffer_load_dwordx4 v[128:131], %[vL], %[rc], s54 offen offset:0 sc1\n\t"
      "buffer_load_dwordx4 v[132:135], %[vL], %[rc], s54 offen offset:1024 sc1\n\t"
      "buffer_load_dwordx4 v[136:139], %[vL], %[rc], s54 offen offset:2048 sc1\n\t"
      "buffer_load_dwordx4 v[140:143], %[vL], %[rc], s54 offen offset:3072 sc1\n\t"
      "buffer_load_dwordx4 v[112:115], %[vL], %[rc], s67 offen offset:0 sc1\n\t"
      "buffer_load_dwordx4 v[116:119], %[vL], %[rc], s67 offen offset:1024 sc1\n\t"
      "buffer_load_dwordx4 v[120:123], %[vL], %[rc], s67 offen offset:2048 sc1\n\t"
      "buffer_load_dwordx4 v[124:127], %[vL], %[rc], s67 offen offset:3072 sc1\n\t"
      "s_waitcnt vmcnt(7)\n\t"
      "v_mfma_f32_16x16x32_bf16 v[160:163], v[144:147], v[128:131], 0\n\t"
      "v_max3_u32 v104, v128, v129, v130\n\t"
      "s_waitcnt vmcnt(6)\n\t"
      "v_mfma_f32_16x16x32_bf16 v[160:163], v[148:151], v[132:135], v[160:163]\n\t"
      "v_max3_u32 v105, v131, v132, v133\n\t"
      "v_max3_u32 v104, v104, v134, v135\n\t"
      "s_waitcnt vmcnt(5)\n\t"
      "v_mfma_f32_16x16x32_bf16 v[160:163], v[152:155], v[136:139], v[160:163]\n\t"
      "v_max3_u32 v105, v105, v136, v137\n\t"
      "v_max3_u32 v104, v104, v138, v139\n\t"
      "s_waitcnt vmcnt(4)\n\t"
      "v_mfma_f32_16x16x32_bf16 v[160:163], v[156:159], v[140:143], v[160:163]\n\t"
      "v_max3_u32 v105, v105, v140, v141\n\t"
      "v_max3_u32 v104, v104, v142, v143\n\t"
      "v_max_u32 v104, v104, v105\n\t"
      "v_cmp_eq_u32 vcc, -1, v104\n\t"
      "s_cbranch_vccnz LXE21_%=\n\t"
      "s_waitcnt vmcnt(3)\n\t"
      "v_mfma_f32_16x16x32_bf16 v[160:163], v[112:115], %[sel], v[160:163]\n\t"
      "s_waitcnt vmcnt(2)\n\t"
      "v_mfma_f32_16x16x32_bf16 v[160:163], v[116:119], %[sel], v[160:163]\n\t"
      "s_waitcnt vmcnt(1)\n\t"
      "v_mfma_f32_16x16x32_bf16 v[160:163], v[120:123], %[sel], v[160:163]\n\t"
      "s_waitcnt vmcnt(0)\n\t"
      "v_mfma_f32_16x16x32_bf16 v[160:163], v[124:127], %[sel], v[160:163]\n\t"
      "s_nop 7\n\t"
      "v_permlane32_swap_b32 v160, v162\n\t"
      "v_permlane32_swap_b32 v161, v163\n\t"
      "s_nop 0\n\t"
      "v_add_f32_dpp v104, v162, v160 row_ror:8 row_mask:0xf bank_mask:0x3\n\t"
      "v_add_f32_dpp v104, v160, v162 row_ror:8 row_mask:0xf bank_mask:0xc\n\t"
      "v_add_f32_dpp v105, v163, v161 row_ror:8 row_mask:0xf bank_mask:0x3\n\t"
      "v_add_f32_dpp v105, v161, v163 row_ror:8 row_mask:0xf bank_mask:0xc\n\t"
      "v_cvt_pk_bf16_f32 v164, v104, v105\n\t"
      "v_cvt_pk_bf16_f32 v166, v104, v105\n\t"
      "v_cmp_u_f32 vcc, v104, v105\n\t"
      "s_cbranch_vccz LXG1_%=\n\t"
      "LXE21_%=:\n\t"
      "buffer_load_dwordx4 v[144:147], %[vL], %[rc], s66 offen offset:0 sc1\n\t"
      "buffer_load_dwordx4 v[148:151], %[vL], %[rc], s66 offen offset:1024 sc1\n\t"
      "buffer_load_dwordx4 v[152:155], %[vL], %[rc], s66 offen offset:2048 sc1\n\t"
      "buffer_load_dwordx4 v[156:159], %[vL], %[rc], s66 offen offset:3072 sc1\n\t"
      "buffer_load_dwordx4 v[128:131], %[vL], %[rc], s54 offen offset:0 sc1\n\t"
      "buffer_load_dwordx4 v[132:135], %[vL], %[rc], s54 offen offset:1024 sc1\n\t"
      "buffer_load_dwordx4 v[136:139], %[vL], %[rc], s54 offen offset:2048 sc1\n\t"
      "buffer_load_dwordx4 v[140:143], %[vL], %[rc], s54 offen offset:3072 sc1\n\t"
      "buffer_load_dwordx4 v[112:115], %[vL], %[rc], s67 offen offset:0 sc1\n\t"
      "buffer_load_dwordx4 v[116:119], %[vL], %[rc], s67 offen offset:1024 sc1\n\t"
      "buffer_load_dwordx4 v[120:123], %[vL], %[rc], s67 offen offset:2048 sc1\n\t"
      "buffer_load_dwordx4 v[124:127], %[vL], %[rc], s67 offen offset:3072 sc1\n\t"
      "s_waitcnt vmcnt(7)\n\t"
      "v_mfma_f32_16x16x32_bf16 v[160:163], v[144:147], v[128:131], 0\n\t"
      "v_max3_u32 v104, v128, v129, v130\n\t"
      "s_waitcnt vmcnt(6)\n\t"
      "v_mfma_f32_16x16x32_bf16 v[160:163], v[148:151], v[132:135], v[160:163]\n\t"
      "v_max3_u32 v105, v131, v132, v133\n\t"
      "v_max3_u32 v104, v104, v134, v135\n\t"
      "s_waitcnt vmcnt(5)\n\t"
      "v_mfma_f32_16x16x32_bf16 v[160:163], v[152:155], v[136:139], v[160:163]\n\t"
      "v_max3_u32 v105, v105, v136, v137\n\t"
      "v_max3_u32 v104, v104, v138, v139\n\t"
      "s_waitcnt vmcnt(4)\n\t"
      "v_mfma_f32_16x16x32_bf16 v[160:163], v[156:159], v[140:143], v[160:163]\n\t"
      "v_max3_u32 v105, v105, v140, v141\n\t"
      "v_max3_u32 v104, v104, v142, v143\n\t"
      "v_max_u32 v104, v104, v105\n\t"
      "v_cmp_eq_u32 vcc, -1, v104\n\t"
      "s_cbranch_vccnz LXE31_%=\n\t"
      "s_waitcnt vmcnt(3)\n\t"
      "v_mfma_f32_16x16x32_bf16 v[160:163], v[112:115], %[sel], v[160:163]\n\t"
      "s_waitcnt vmcnt(2)\n\t"
      "v_mfma_f32_16x16x32_bf16 v[160:163], v[116:119], %[sel], v[160:163]\n\t"
      "s_waitcnt vmcnt(1)\n\t"
      "v_mfma_f32_16x16x32_bf16 v[160:163], v[120:123], %[sel], v[160:163]\n\t"
      "s_waitcnt vmcnt(0)\n\t"
      "v_mfma_f32_16x16x32_bf16 v[160:163], v[124:127], %[sel], v[160:163]\n\t"
      "s_nop 7\n\t"
      "v_permlane32_swap_b32 v160, v162\n\t"
      "v_permlane32_swap_b32 v161, v163\n\t"
      "s_nop 0\n\t"
      "v_add_f32_dpp v104, v162, v160 row_ror:8 row_mask:0xf bank_mask:0x3\n\t"
      "v_add_f32_dpp v104, v160, v162 row_ror:8 row_mask:0xf bank_mask:0xc\n\t"
      "v_add_f32_dpp v105, v163, v161 row_ror:8 row_mask:0xf bank_mask:0x3\n\t"
      "v_add_f32_dpp v105, v161, v163 row_ror:8 row_mask:0xf bank_mask:0xc\n\t"
      "v_cvt_pk_bf16_f32 v164, v104, v105\n\t"
      "v_cvt_pk_bf16_f32 v166, v104, v105\n\t"
      "v_cmp_u_f32 vcc, v104, v105\n\t"
      "s_cbranch_vccz LXG1_%=\n\t"
      "s_branch LXH1_%=\n\t"
      "LXS1_%=:\n\t"
      "s_sleep 2\n\t"
      "LXE31_%=:\n\t"
      "s_add_u32 s71, s71, 1\n\t"
      "s_cmp_gt_u32 s71, 131072\n\t"
      "s_cbranch_scc1 LFAIL_%=\n\t"
      "v_lshlrev_b32 v104, 2, %[vL]\n\t"
      "buffer_load_dword v105, v104, %[rc], s54 offen sc1\n\t"
      "s_waitcnt vmcnt(0)\n\t"
      "v_cmp_ne_u32 vcc, -1, v105\n\t"
      "s_cbranch_vccz LXS1_%=\n\t"
      "LXL1_%=:\n\t"
      "s_add_u32 s71, s71, 1\n\t"
      "s_cmp_gt_u32 s71, 131072\n\t"
      "s_cbranch_scc1 LFAIL_%=\n\t"
      "buffer_load_dwordx4 v[128:131], %[vL], %[rc], s54 offen offset:0 sc1\n\t"
      "buffer_load_dwordx4 v[132:135], %[vL], %[rc], s54 offen offset:1024 sc1\n\t"
      "buffer_load_dwordx4 v[136:139], %[vL], %[rc], s54 offen offset:2048 sc1\n\t"
      "buffer_load_dwordx4 v[140:143], %[vL], %[rc], s54 offen offset:3072 sc1\n\t"
      "buffer_load_dwordx4 v[112:115], %[vL], %[rc], s67 offen offset:0 sc1\n\t"
      "buffer_load_dwordx4 v[116:119], %[vL], %[rc], s67 offen offset:1024 sc1\n\t"
      "buffer_load_dwordx4 v[120:123], %[vL], %[rc], s67 offen offset:2048 sc1\n\t"
      "buffer_load_dwordx4 v[124:127], %[vL], %[rc], s67 offen offset:3072 sc1\n\t"
      "s_waitcnt vmcnt(7)\n\t"
      "v_mfma_f32_16x16x32_bf16 v[160:163], v[144:147], v[128:131], 0\n\t"
      "v_max3_u32 v104, v128, v129, v130\n\t"
      "s_waitcnt vmcnt(6)\n\t"
      "v_mfma_f32_16x16x32_bf16 v[160:163], v[148:151], v[132:135], v[160:163]\n\t"
      "v_max3_u32 v105, v131, v132, v133\n\t"
      "v_max3_u32 v104, v104, v134, v135\n\t"
      "s_waitcnt vmcnt(5)\n\t"
      "v_mfma_f32_16x16x32_bf16 v[160:163], v[152:155], v[136:139], v[160:163]\n\t"
      "v_max3_u32 v105, v105, v136, v137\n\t"
      "v_max3_u32 v104, v104, v138, v139\n\t"
      "s_waitcnt vmcnt(4)\n\t"
      "v_mfma_f32_16x16x32_bf16 v[160:163], v[156:159], v[140:143], v[160:163]\n\t"
      "v_max3_u32 v105, v105, v140, v141\n\t"
      "v_max3_u32 v104, v104, v142, v143\n\t"
      "v_max_u32 v104, v104, v105\n\t"
      "v_cmp_eq_u32 vcc, -1, v104\n\t"
      "s_cbranch_vccnz LXL1_%=\n\t"
      "s_waitcnt vmcnt(3)\n\t"
      "v_mfma_f32_16x16x32_bf16 v[160:163], v[112:115], %[sel], v[160:163]\n\t"
      "s_waitcnt vmcnt(2)\n\t"
      "v_mfma_f32_16x16x32_bf16 v[160:163], v[116:119], %[sel], v[160:163]\n\t"
      "s_waitcnt vmcnt(1)\n\t"
      "v_mfma_f32_16x16x32_bf16 v[160:163], v[120:123], %[sel], v[160:163]\n\t"
      "s_waitcnt vmcnt(0)\n\t"
      "v_mfma_f32_16x16x32_bf16 v[160:163], v[124:127], %[sel], v[160:163]\n\t"
      "s_nop 7\n\t"
      "v_permlane32_swap_b32 v160, v162\n\t"
      "v_permlane32_swap_b32 v161, v163\n\t"
      "s_nop 0\n\t"
      "v_add_f32_dpp v104, v162, v160 row_ror:8 row_mask:0xf bank_mask:0x3\n\t"
      "v_add_f32_dpp v104, v160, v162 row_ror:8 row_mask:0xf bank_mask:0xc\n\t"
      "v_add_f32_dpp v105, v163, v161 row_ror:8 row_mask:0xf bank_mask:0x3\n\t"
      "v_add_f32_dpp v105, v161, v163 row_ror:8 row_mask:0xf bank_mask:0xc\n\t"
      "v_cvt_pk_bf16_f32 v164, v104, v105\n\t"
      "v_cvt_pk_bf16_f32 v166, v104, v105\n\t"
      "v_cmp_u_f32 vcc, v104, v105\n\t"
      "s_cbranch_vccz LXG1_%=\n\t"
      "s_branch LXE21_%=\n\t"
      "LXH1_%=:\n\t"
      "s_mov_b64 s[100:101], 0\n\t"
      "v_max3_u32 v104, v144, v145, v146\n\t"
      "v_max_u32 v104, v104, v147\n\t"
      "v_cmp_eq_u32 vcc, -1, v104\n\t"
      "s_or_b64 s[100:101], s[100:101], vcc\n\t"
      "s_mov_b64 exec, vcc\n\t"
      "buffer_load_dwordx4 v[144:147], %[vL], %[rc], s66 offen offset:0 sc1\n\t"
      "s_mov_b64 exec, -1\n\t"
      "v_max3_u32 v104, v148, v149, v150\n\t"
      "v_max_u32 v104, v104, v151\n\t"
      "v_cmp_eq_u32 vcc, -1, v104\n\t"
      "s_or_b64 s[100:101], s[100:101], vcc\n\t"
      "s_mov_b64 exec, vcc\n\t"
      "buffer_load_dwordx4 v[148:151], %[vL], %[rc], s66 offen offset:1024 sc1\n\t"
      "s_mov_b64 exec, -1\n\t"
      "v_max3_u32 v104, v152, v153, v154\n\t"
      "v_max_u32 v104, v104, v155\n\t"
      "v_cmp_eq_u32 vcc, -1, v104\n\t"
      "s_or_b64 s[100:101], s[100:101], vcc\n\t"
      "s_mov_b64 exec, vcc\n\t"
      "buffer_load_dwordx4 v[152:155], %[vL], %[rc], s66 offen offset:2048 sc1\n\t"
      "s_mov_b64 exec, -1\n\t"
      "v_max3_u32 v104, v156, v157, v158\n\t"
      "v_max_u32 v104, v104, v159\n\t"
      "v_cmp_eq_u32 vcc, -1, v104\n\t"
      "s_or_b64 s[100:101], s[100:101], vcc\n\t"
      "s_mov_b64 exec, vcc\n\t"
      "buffer_load_dwordx4 v[156:159], %[vL], %[rc], s66 offen offset:3072 sc1\n\t"
      "s_mov_b64 exec, -1\n\t"
      "v_max3_u32 v104, v128, v129, v130\n\t"
      "v_max_u32 v104, v104, v131\n\t"
      "v_cmp_eq_u32 vcc, -1, v104\n\t"
      "s_or_b64 s[100:101], s[100:101], vcc\n\t"
      "s_mov_b64 exec, vcc\n\t"
      "buffer_load_dwordx4 v[128:131], %[vL], %[rc], s54 offen offset:0 sc1\n\t"
      "s_mov_b64 exec, -1\n\t"
      "v_max3_u32 v104, v132, v133, v134\n\t"
      "v_max_u32 v104, v104, v135\n\t"
      "v_cmp_eq_u32 vcc, -1, v104\n\t"
      "s_or_b64 s[100:101], s[100:101], vcc\n\t"
      "s_mov_b64 exec, vcc\n\t"
      "buffer_load_dwordx4 v[132:135], %[vL], %[rc], s54 offen offset:1024 sc1\n\t"
      "s_mov_b64 exec, -1\n\t"
      "v_max3_u32 v104, v136, v137, v138\n\t"
      "v_max_u32 v104, v104, v139\n\t"
      "v_cmp_eq_u32 vcc, -1, v104\n\t"
      "s_or_b64 s[100:101], s[100:101], vcc\n\t"
      "s_mov_b64 exec, vcc\n\t"
      "buffer_load_dwordx4 v[136:139], %[vL], %[rc], s54 offen offset:2048 sc1\n\t"
      "s_mov_b64 exec, -1\n\t"
      "v_max3_u32 v104, v140, v141, v142\n\t"
      "v_max_u32 v104, v104, v143\n\t"
      "v_cmp_eq_u32 vcc, -1, v104\n\t"
      "s_or_b64 s[100:101], s[100:101], vcc\n\t"
      "s_mov_b64 exec, vcc\n\t"
      "buffer_load_dwordx4 v[140:143], %[vL], %[rc], s54 offen offset:3072 sc1\n\t"
      "s_mov_b64 exec, -1\n\t"
      "v_max3_u32 v104, v112, v113, v114\n\t"
      "v_max_u32 v104, v104, v115\n\t"
      "v_cmp_eq_u32 vcc, -1, v104\n\t"
      "s_or_b64 s[100:101], s[100:101], vcc\n\t"
      "s_mov_b64 exec, vcc\n\t"
      "buffer_load_dwordx4 v[112:115], %[vL], %[rc], s67 offen offset:0 sc1\n\t"
      "s_mov_b64 exec, -1\n\t"
      "v_max3_u32 v104, v116, v117, v118\n\t"
      "v_max_u32 v104, v104, v119\n\t"
      "v_cmp_eq_u32 vcc, -1, v104\n\t"
      "s_or_b64 s[100:101], s[100:101], vcc\n\t"
      "s_mov_b64 exec, vcc\n\t"
      "buffer_load_dwordx4 v[116:119], %[vL], %[rc], s67 offen offset:1024 sc1\n\t"
      "s_mov_b64 exec, -1\n\t"
      "v_max3_u32 v104, v120, v121, v122\n\t"
      "v_max_u32 v104, v104, v123\n\t"
      "v_cmp_eq_u32 vcc, -1, v104\n\t"
      "s_or_b64 s[100:101], s[100:101], vcc\n\t"
      "s_mov_b64 exec, vcc\n\t"
      "buffer_load_dwordx4 v[120:123], %[vL], %[rc], s67 offen offset:2048 sc1\n\t"
      "s_mov_b64 exec, -1\n\t"
      "v_max3_u32 v104, v124, v125, v126\n\t"
      "v_max_u32 v104, v104, v127\n\t"
      "v_cmp_eq_u32 vcc, -1, v104\n\t"
      "s_or_b64 s[100:101], s[100:101], vcc\n\t"
      "s_mov_b64 exec, vcc\n\t"
      "buffer_load_dwordx4 v[124:127], %[vL], %[rc], s67 offen offset:3072 sc1\n\t"
      "s_mov_b64 exec, -1\n\t"
      "s_cmp_eq_u64 s[100:101], 0\n\t"
      "s_cbranch_scc1 LXG1_%=\n\t"
      "s_add_u32 s71, s71, 1\n\t"
      "s_cmp_gt_u32 s71, 131072\n\t"
      "s_cbranch_scc1 LFAIL_%=\n\t"
      "s_waitcnt vmcnt(0)\n\t"
      "v_mfma_f32_16x16x32_bf16 v[160:163], v[144:147], v[128:131], 0\n\t"
      "v_mfma_f32_16x16x32_bf16 v[160:163], v[148:151], v[132:135], v[160:163]\n\t"
      "v_mfma_f32_16x16x32_bf16 v[160:163], v[152:155], v[136:139], v[160:163]\n\t"
      "v_mfma_f32_16x16x32_bf16 v[160:163], v[156:159], v[140:143], v[160:163]\n\t"
      "v_mfma_f32_16x16x32_bf16 v[160:163], v[112:115], %[sel], v[160:163]\n\t"
      "v_mfma_f32_16x16x32_bf16 v[160:163], v[116:119], %[sel], v[160:163]\n\t"
      "v_mfma_f32_16x16x32_bf16 v[160:163], v[120:123], %[sel], v[160:163]\n\t"
      "v_mfma_f32_16x16x32_bf16 v[160:163], v[124:127], %[sel], v[160:163]\n\t"
      "s_nop 7\n\t"
      "v_permlane32_swap_b32 v160, v162\n\t"
      "v_permlane32_swap_b32 v161, v163\n\t"
      "s_nop 0\n\t"
      "v_add_f32_dpp v104, v162, v160 row_ror:8 row_mask:0xf bank_mask:0x3\n\t"
      "v_add_f32_dpp v104, v160, v162 row_ror:8 row_mask:0xf bank_mask:0xc\n\t"
      "v_add_f32_dpp v105, v163, v161 row_ror:8 row_mask:0xf bank_mask:0x3\n\t"
      "v_add_f32_dpp v105, v161, v163 row_ror:8 row_mask:0xf bank_mask:0xc\n\t"
      "v_cvt_pk_bf16_f32 v164, v104, v105\n\t"
      "v_cvt_pk_bf16_f32 v166, v104, v105\n\t"
      "v_cmp_u_f32 vcc, v104, v105\n\t"
      "s_cbranch_vccnz LXH1_%=\n\t"
      "s_branch LXG1_%=\n\t"
      "LFAIL_%=:\n\t"
      "s_mov_b32 %[st], 1\n\t"
      "LEND_%=:\n\t"
      "s_waitcnt vmcnt(0) lgkmcnt(0)\n\t"
      : [st] "=s"(st), [oM] "+s"(oM), [oAn] "+s"(oAn), [tag] "+s"(tag), [mo] "+v"(mo), [vAn] "+v"(vAn), [mr] "+v"(mr), [dR] "+s"(dR), [dM] "+s"(dM),
        [oX] "+s"(oX), [oY] "+s"(oY), [oP] "+s"(oP), [vFc] "+v"(vFc), [yo0] "+v"(yo[0]), [yo1] "+v"(yo[1])
      : [rs] "s"(mkrs(ws, WS_BYTES)), [vL] "v"(lane * 16), [vMw] "v"(lane * 16 + w * 2048), [vWm] "v"(lb + w * 2048), [vRm1] "v"(lb + (1 ^ w) * 2048),
        [vRm2] "v"(lb + (2 ^ w) * 2048), [vRm3] "v"(lb + (3 ^ w) * 2048),
        [yt0] "v"((128 * w + 32 * (g & 1) + 16 * (g >> 1) + n) * 16), [idx] "v"((n < 8) ? 0x4444 : 0xEEEE), [cns] "v"(0xfffeffffu),
        [vWr] "v"(img + (32 * w + 16 * (g >> 1) + n) * 16 + (g & 1) * 8), [vRd] "v"(img + (4 * g + (n >> 2)) * 16 + (n & 1) * 8),
        [sel] "v"(sel), [sent] "v"(sent),
        [rc] "s"(mkrs(comm, 4 << 20)), [rW] "s"(mkrs(Wv + h * 128, (4 << 20) - h * 256)), [rW2] "s"(mkrs(Wo + h * 65536, 131072)),
        [vG] "v"(WS_G), [vSw] "v"(WS_SW), [vTr] "v"(WS_TR), [vG2] "v"(g * 1024 + w * 256 + n * 16),
        [vSw2] "v"(w * 32768 + g * 256 + ((n >> 1) ^ (2 * (g & 1))) * 32 + (n & 1) * 16),
        [vTr2] "v"(w * 32768 + (16 * (g & 1) + 4 * (g >> 1) + ((n >> 2) & 1)) * 256 + ((n >> 3) ^ (2 * ((n >> 2) & 1) + 4 * (g & 1))) * 32 + (n & 3) * 8),
        [rY] "s"(mkrs(ws + OFF_YT + (size_t)h * 8192, (17 << 20) - h * 8192)), [wv] "s"(w), [ms] "s"(w + h),
        [mo4] "v"(((n >> 3) - (lane >> 3)) * 16384 + (g & 1) * 8 + (g >> 1) * 4),
        [tm0] "s"(now32()) TS_OPS(w < 2 ? 32 + h + 64 * w : -1)
      :
        "v40", "v41", "v42", "v43", "v44", "v45", "v46", "v47", "v48", "v49", "v50", "v51", "v52", "v53", "v54", "v55", "v56", "v57", "v58", "v59", "v60", "v61", "v62", "v63",
        "v64", "v65", "v66", "v67", "v68", "v69", "v70", "v71", "v72", "v73", "v74", "v75", "v76", "v77", "v78", "v79", "v80", "v81", "v82", "v83", "v84", "v85", "v86", "v87",
        "v88", "v89", "v90", "v91", "v92", "v93", "v94", "v95", "v96", "v97", "v98", "v99", "v100", "v101", "v102", "v103", "v104", "v105", "v106", "v107", "v108", "v109", "v110", "v111",
        "v112", "v113", "v114", "v115", "v116", "v117", "v118", "v119", "v120", "v121", "v122", "v123", "v124", "v125", "v126", "v127", "v128", "v129", "v130", "v131", "v132", "v133", "v134", "v135",
        "v136", "v137", "v138", "v139", "v140", "v141", "v142", "v143", "v144", "v145", "v146", "v147", "v148", "v149", "v150", "v151", "v152", "v153", "v154", "v155", "v156", "v157", "v158", "v159",
        "v160", "v161", "v162", "v163", "v164", "v165", "v166", "v167", "v168", "v169", "v170", "v171", "v172", "v173", "v174", "v175", "v176", "v177", "v178", "v179", "v180", "v181", "v182", "v183",
        "v184", "v185", "v186", "v187", "v188", "v189", "v190", "v191", "v192", "v193", "v194", "v195", "v196", "v197", "v198", "v199", "v200", "v201", "v202", "v203", "v204", "v205", "v206", "v207",
        "v208", "v209", "v210", "v211", "v212", "v213", "v214", "v215", "v216", "v217", "v218", "v219", "v220", "v221", "v222", "v223", "v224", "v225", "v226", "v227", "v228", "a0", "a1", "a2",
        "a3", "a4", "a5", "a6", "a7", "a8", "a9", "a10", "a11", "a12", "a13", "a14", "a15", "a16", "a17", "a18", "a19", "a20", "a21", "a22", "a23", "a24", "a25", "a26",
        "a27", "a28", "a29", "a30", "a31", "a32", "a33", "a34", "a35", "a36", "a37", "a38", "a39", "a40", "a41", "a42", "a43", "a44", "a45", "a46", "a47", "a48", "a49", "a50",
        "a51", "a52", "a53", "a54", "a55", "a56", "a57", "a58", "a59", "a60", "a61", "a62", "a63", "a64", "a65", "a66", "a67", "a68", "a69", "a70", "a71", "a72", "a73", "a74",
        "a75", "a76", "a77", "a78", "a79", "a80", "a81", "a82", "a83", "a84", "a85", "a86", "a87", "a88", "a89", "a90", "a91", "a92", "a93", "a94", "a95", "a96", "a97", "a98",
        "a99", "a100", "a101", "a102", "a103", "a104", "a105", "a106", "a107", "a108", "a109", "a110", "a111", "a112", "a113", "a114", "a115", "a116", "a117", "a118", "a119", "a120", "a121", "a122",
        "a123", "a124", "a125", "a126", "a127", "a128", "a129", "a130", "a131", "a132", "a133", "a134", "a135", "a136", "a137", "a138", "a139", "a140", "a141", "a142", "a143", "a144", "a145", "a146",
        "a147", "a148", "a149", "a150", "a151", "a152", "a153", "a154", "a155", "a156", "a157", "a158", "a159", "a160", "a161", "a162", "a163", "a164", "a165", "a166", "a167", "a168", "a169", "a170",
        "a171", "a172", "a173", "a174", "a175", "a176", "a177", "a178", "a179", "a180", "a181", "a182", "a183", "a184", "a185", "a186", "a187", "a188", "a189", "a190", "a191", "a192", "a193", "a194",
        "a195", "a196", "a197", "a198", "a199", "a200", "a201", "a202", "a203", "a204", "a205", "a206", "a207", "a208", "a209", "a210", "a211", "a212", "a213", "a214", "a215", "a216", "a217", "a218",
        "a219", "a220", "a221", "a222", "a223", "a224", "a225", "a226", "a227", "a228", "a229", "a230", "a231", "a232", "a233", "a234", "a235", "a236", "a237", "a238", "a239", "a240", "a241", "a242",
        "a243", "a244", "a245", "a246", "a247", "a248", "a249", "a250", "a251", "a252", "a253", "a254", "a255", "s62", "s63", "s64", "s65", "s66", "s67", "s68", "s69", "s70", "s71", "s72",
        "s73", "s74", "s75", "s76", "s77", "s78", "s79", "s80", "s81", "s82", "s83", "s84", "s85", "s86", "s87", "s88", "s89", "s90", "s91", "s92", "s93", "s94", "s95", "s96",
        "s97", "s98", "s99", "s100", "s101", "s52", "s53", "s54", "s55", "s56", "s57", "s58", "s59", "s60", "s61", "vcc", "scc", "m0", "memory"
  );
  return st == 0;
}

struct PX { v4u qf[2][4]; v4f t00, t01, t11; };
DI bool p_x(rsrc_t rw, rsrc_t rc, int layer, int s, int h, PX& x) {
  const int lane = ltid() & 63;
  if (!wait_flags<SLOW>(rc, L_PR + h * 128 + (lane & 3) * 4, layer * 64 + s + 1)) return false;
  const unsigned qo = OFF_Q + h * HQ + s * 8192u, ko = OFF_K + h * HQ + s * 8192u;
  v4u kf[2][4];
#pragma unroll
  for (int ks = 0; ks < 4; ++ks) { kf[0][ks] = ldc16s(rw, lane * 16 + ks * 1024, ko); kf[1][ks] = ldc16s(rw, lane * 16 + ks * 1024, ko + 4096); }
#pragma unroll
  for (int ks = 0; ks < 4; ++ks) { x.qf[0][ks] = ldc16s(rw, lane * 16 + ks * 1024, qo); x.qf[1][ks] = ldc16s(rw, lane * 16 + ks * 1024, qo + 4096); }
  x.t00 = (v4f){0, 0, 0, 0}; x.t01 = x.t00; x.t11 = x.t00;
#pragma unroll
  for (int ks = 0; ks < 4; ++ks) {
    x.t00 = mfma32(kf[0][ks], x.qf[0][ks], x.t00); x.t01 = mfma32(kf[0][ks], x.qf[1][ks], x.t01); x.t11 = mfma32(kf[1][ks], x.qf[1][ks], x.t11);
  }
  return true;
}
DI bool p_m(rsrc_t rw, rsrc_t rc, int layer, int s, int h, const PX& x, unsigned& tn) {
  const int tag = layer * 64 + s + 1, sl = slot_of(s, layer);
  const int lane = ltid() & 63, n = lane & 15, g = lane >> 4;
  nap(s < SF ? tn : tn - 2 * STEP);
  if (!wait_flags<4>(rc, L_A + h * 128, s < SF ? tag : tag - 2)) return false;
  tn = now32() + 4 * STEP - NAPM;
  const unsigned mo = OFF_KM + (sl * 32 + h) * 2048u + (g * 8 + (n & 7)) * 16;
  v4u mf[4];
  for (int it = 0;; ++it) {
    CBAR();
    unsigned m = 0;
#pragma unroll
    for (int ks = 0; ks < 4; ++ks) mf[ks] = ldc16(rw, mo + ks * 512);
#pragma unroll
    for (int ks = 0; ks < 4; ++ks) m = mx4(mf[ks], m);
    if (s < SF || all_set(m)) break;
    if (it > POLL_LIMIT) return false;
    __builtin_amdgcn_s_sleep(1);
  }
#pragma unroll
  for (int nt = 0; nt < 2; ++nt) {
    v4f mm = {0, 0, 0, 0};
#pragma unroll
    for (int ks = 0; ks < 4; ++ks) mm = mfma32(mf[ks], x.qf[nt][ks], mm);
    const v4f t0 = nt ? x.t01 : x.t00, t1 = x.t11;
    const int tok = 16 * nt + n;
    float v[12];
#pragma unroll
    for (int r = 0; r < 4; ++r) {
      v[r] = (4 * g + r <= tok) ? t0[r] : -INFINITY;
      v[4 + r] = (nt == 1 && 16 + 4 * g + r <= tok) ? t1[r] : -INFINITY;
      v[8 + r] = (g < 2) ? mm[r] : -INFINITY;
    }
    float mx = v[0];
#pragma unroll
    for (int r = 1; r < 12; ++r) mx = fmaxf(mx, v[r]);
    mx = gmax(mx) * SCL;
    float sum = 0.f;
#pragma unroll
    for (int r = 0; r < 12; ++r) { v[r] = __builtin_amdgcn_exp2f(v[r] * SCL - mx); sum += v[r]; }
    const float inv = __builtin_amdgcn_rcpf(gsum(sum));
    v4u o = {pack2(v[0] * inv, v[1] * inv), pack2(v[2] * inv, v[3] * inv), pack2(v[4] * inv, v[5] * inv), pack2(v[6] * inv, v[7] * inv)};
    stc16(rw, OFF_PF + (sl * 32 + h) * 2048u + (nt * 64 + lane) * 16, nosent4(o));
    if (g < 2) {
      v2u om = {nosent(pack2(v[8] * inv, v[9] * inv)), nosent(pack2(v[10] * inv, v[11] * inv))};
      stc8(rw, OFF_PM + (sl * 8 + (h >> 2)) * 2048u + (nt * 64 + (h & 3) * 16 + n) * 16 + g * 8, om);
    }
  }
  set_flag(rc, L_P + ((s & 3) * 32 + h) * 128, tag);
  return true;
}

DI void x_out(rsrc_t rw, int lane, int buf, int s, int nt, int ks, v4u xv, v4f c0, v4f c1) {
  const v4u o = {pack2(bflo(xv[0]) + rbf(c0[0]), bfhi(xv[0]) + rbf(c0[1])), pack2(bflo(xv[1]) + rbf(c0[2]), bfhi(xv[1]) + rbf(c0[3])),
                 pack2(bflo(xv[2]) + rbf(c1[0]), bfhi(xv[2]) + rbf(c1[1])), pack2(bflo(xv[3]) + rbf(c1[2]), bfhi(xv[3]) + rbf(c1[3]))};
  stc16s(rw, lane * 16, OFF_XF + buf * 2 * MB + ((s * 2 + nt) * 16 + ks) * 1024, nosent4(o));
  const v2u st = chunk_stats(o);
  if (lane < 16) stc8(rw, st_off(buf, s, nt, ks, lane), (v2u){nosent(st[0]), nosent(st[1])});
}

DI bool unit_o(rsrc_t rw, rsrc_t rc, int layer, int s, int cb, u8* lds, unsigned& tn) {
  const int tid = ltid(), w = __builtin_amdgcn_readfirstlane(tid >> 6), lane = tid & 63, n = lane & 15, g = lane >> 4;
  const int tag = layer * 64 + s + 1;
  if (!wait_flags<SLOW>(rc, L_PR + (32 + w * 8 + (lane & 7)) * 128 + ((lane >> 3) & 3) * 4, tag)) return false;
  v4u uf[8][4];
#pragma unroll
  for (int hh = 0; hh < 8; ++hh) {
    const unsigned uo = OFF_UT + (w * 8 + hh) * HU + s * 32768u + cb * 4096, uv = (g >> 1) * 1024 + ((g & 1) * 32 + n) * 16;
#pragma unroll
    for (int mp = 0; mp < 2; ++mp) { uf[hh][2 * mp] = ldc16s(rw, uv + mp * 2048, uo); uf[hh][2 * mp + 1] = ldc16s(rw, uv + mp * 2048 + 256, uo); }
    if (s >= SF) __builtin_amdgcn_s_sleep(16);
  }
  const int oa = w & 1, ont = w >> 1, oks = 2 * cb + oa;
  const v4u xv = ldc16s(rw, lane * 16, OFF_XF + (2 * layer) * 2 * MB + ((s * 2 + ont) * 16 + oks) * 1024);
  const int sl = slot_of(s, layer);
  if ((TCH & 4) && s >= SF)
    touch(rw, lane & 1 ? (lane & 2 ? OFF_YT + sl * 262144u : OFF_PF + sl * 65536u) : lane & 2 ? OFF_PM + sl * 16384u : lane & 4 ? OFF_XF + (2 * layer + 1) * 2 * MB : OFF_ST);
  if (s >= SF) wg_prewait(rc, L_P + ((s + 2) & 3) * 4096u + (lane & 31) * 128, tag - 2);
  else {
    nap(tn);
    if (!wait_flags<4>(rc, ((lane & 8) ? L_A + 4096u : L_P + (s & 3) * 4096u) + (w * 8 + (lane & 7)) * 128, tag)) return false;
    tn = now32() + 4 * STEP - NAPM;
  }
  if (s >= SF)
    for (int it = 0; it < POLL_LIMIT; ++it) {
      CBAR();
      if (all_set(ldc4(rw, OFF_KM + (sl * 32 + w * 8 + (lane & 7)) * 2048u))) break;
      __builtin_amdgcn_s_sleep(2);
    }
  v4u pf[8][2], pm[2][2], yt[2][4];
  unsigned need = 0xfffu;
  for (int it = 0;; ++it) {
    CBAR();
#pragma unroll
    for (int hq2 = 0; hq2 < 2; ++hq2)
      if (need >> (10 + hq2) & 1) {
        const unsigned yo = OFF_YT + (sl * 32 + (w * 2 + hq2) * 4) * 8192u + cb * 1024;
#pragma unroll
        for (int mt = 0; mt < 4; ++mt) yt[hq2][mt] = ldc16s(rw, g * 8192 + n * 16 + mt * 256, yo);
      }
#pragma unroll
    for (int hh = 0; hh < 8; ++hh)
      if (need >> hh & 1) {
        const unsigned po = OFF_PF + (sl * 32 + w * 8 + hh) * 2048u;
        pf[hh][0] = ldc16s(rw, lane * 16, po); pf[hh][1] = ldc16s(rw, lane * 16 + 1024, po);
      }
#pragma unroll
    for (int hq2 = 0; hq2 < 2; ++hq2)
      if (need >> (8 + hq2) & 1) {
        const unsigned po = OFF_PM + (sl * 8 + w * 2 + hq2) * 2048u;
        pm[hq2][0] = ldc16s(rw, lane * 16, po); pm[hq2][1] = ldc16s(rw, lane * 16 + 1024, po);
      }
    if (s < SF) break;
#pragma unroll
    for (int hq2 = 0; hq2 < 2; ++hq2) {
      if (all_set(mx4(yt[hq2][0], mx4(yt[hq2][1], mx4(yt[hq2][2], mx4(yt[hq2][3], 0)))))) need &= ~(1u << (10 + hq2));
      if (all_set(mx4(pm[hq2][0], mx4(pm[hq2][1], 0)))) need &= ~(1u << (8 + hq2));
    }
#pragma unroll
    for (int hh = 0; hh < 8; ++hh)
      if (all_set(mx4(pf[hh][0], mx4(pf[hh][1], 0)))) need &= ~(1u << hh);
    if (!need) break;
    if (it > POLL_LIMIT) return false;
    __builtin_amdgcn_s_sleep(1);
  }
  v4f acc[4][2];
#pragma unroll
  for (int mt = 0; mt < 4; ++mt) { acc[mt][0] = (v4f){0, 0, 0, 0}; acc[mt][1] = (v4f){0, 0, 0, 0}; }
#pragma unroll
  for (int hh = 0; hh < 8; ++hh) {
#pragma unroll
    for (int mp = 0; mp < 2; ++mp) {
      const v4u lo = uf[hh][2 * mp], hi = uf[hh][2 * mp + 1];
      const v4u aa = {lo[0], lo[2], hi[0], hi[2]}, ab = {lo[1], lo[3], hi[1], hi[3]};
      acc[2 * mp][0] = mfma32(aa, pf[hh][0], acc[2 * mp][0]); acc[2 * mp][1] = mfma32(aa, pf[hh][1], acc[2 * mp][1]);
      acc[2 * mp + 1][0] = mfma32(ab, pf[hh][0], acc[2 * mp + 1][0]); acc[2 * mp + 1][1] = mfma32(ab, pf[hh][1], acc[2 * mp + 1][1]);
    }
  }
#pragma unroll
  for (int hq2 = 0; hq2 < 2; ++hq2)
#pragma unroll
    for (int mt = 0; mt < 4; ++mt) {
      acc[mt][0] = mfma32(yt[hq2][mt], pm[hq2][0], acc[mt][0]); acc[mt][1] = mfma32(yt[hq2][mt], pm[hq2][1], acc[mt][1]);
    }
  v4f* red = (v4f*)lds;
#pragma unroll
  for (int mt = 0; mt < 4; ++mt) { red[(w * 8 + mt * 2 + 0) * 64 + lane] = acc[mt][0]; red[(w * 8 + mt * 2 + 1) * 64 + lane] = acc[mt][1]; }
  __syncthreads();
  v4f c[2];
#pragma unroll
  for (int i = 0; i < 2; ++i) {
    const int tile = (2 * oa + i) * 2 + ont;
    c[i] = (red[tile * 64 + lane] + red[(8 + tile) * 64 + lane]) + (red[(16 + tile) * 64 + lane] + red[(24 + tile) * 64 + lane]);
  }
  x_out(rw, lane, 2 * layer + 1, s, ont, oks, xv, c[0], c[1]);
  if (s < SF) wg_flag(rc, L_O + ((s & 3) * 8 + cb) * 128, tag);
  return true;
}

DI float phi(float t) {
  const float a = t > 0.f ? rbf(t + 1.f) : rbf(__expf(t));
  return a + 1e-6f;
}
#define B1_NEXT(late) do { \
    CBAR(); \
    if (!have && s + 2 < NSEG && ((late && s + 2 >= SF) || __all(((lane >> 3) & 3) != ((s + 2) & 3) || *fl >= fx_val(buf, s + 2)))) { \
      have = true; tile_dma(rs, lane * 16, w, buf, s + 2, ldsb + (~s & 2) * 16384, ldsb + 65536 + (~s & 2) * 2048); } \
    asm volatile("s_mov_b32 m0, %2\n\ts_nop 0\n\tbuffer_load_dword %0, %1, 0 offen sc1 lds" \
                 :: "v"(L_O + (lane & 31) * 128), "s"(rcs), "s"(ldsb + 74752 + w * 256) : "m0", "memory"); } while (0)
DI bool role_b1(u8* __restrict__ ws, const u16* __restrict__ wqkv, rsrc_t rw, rsrc_t rc, int layer, int j, int part, u8* lds) {
  const int tid = ltid(), w = __builtin_amdgcn_readfirstlane(tid >> 6), lane = tid & 63, n = lane & 15, g = lane >> 4;
  const bool isq = j >= 8;
  const int ct0 = 8 * (j - 8) + 2 * w;
  const int buf = 2 * layer + 1;
  unsigned one1;
  asm volatile("v_mov_b32 %0, 0x3f803f80" : "=v"(one1));
  const v4u ones = {one1, one1, one1, one1};
  v4u W[2][16];
  v4f cs[2] = {{0, 0, 0, 0}, {0, 0, 0, 0}};
  {
    const u16* pa = wqkv + (isq ? 128 * (j - 8) : 512 + 64 * j);
    w_stage(pa, pa + (isq ? 64 : 512), 1536, isq ? 2 * w : w, isq ? 2 * w + 1 : 4 + w, W, lds);
    if (isq) {
#pragma unroll
      for (int ks = 0; ks < 16; ++ks) { cs[0] = mfma32(W[0][ks], ones, cs[0]); cs[1] = mfma32(W[1][ks], ones, cs[1]); }
    } else {
#pragma unroll
      for (int ks = 0; ks < 16; ++ks) { cs[0] = mfma32(ones, W[0][ks], cs[0]); cs[1] = mfma32(ones, W[1][ks], cs[1]); }
    }
  }
  v4f kv = {0, 0, 0, 0}, zz = {0, 0, 0, 0};
  v2f* stl = (v2f*)(lds + 73728 + w * 256);
  const v4i rs = mkrs(ws, WS_BYTES), rcs = mkrs(ws + OFF_COMM + near_par(rw), 2 << 20);
  const unsigned ldsb = (unsigned)(uintptr_t)(__attribute__((address_space(3))) u8*)lds;
  const unsigned fb = L_B1 + (part * 12 + j) * 128;
  bool have = false;
  unsigned tn = now32();
  volatile __attribute__((address_space(3))) int* fl = (volatile __attribute__((address_space(3))) int*)(ldsb + 74752 + tid * 4);
  *fl = -1;
  __syncthreads();
  for (int s = part; s < NSEG; s += 2) {
    u8* tile = lds + (s & 2) * 16384;
    const v4u* sn = (const v4u*)(lds + 65536 + (s & 2) * 2048);
    for (int it = 0;; ++it) {
      if (!have) {
        if (s < SF) {
          nap(tn);
          if (!wait_flags2(rc, L_O + (s & 3) * 1024 + (lane & 7) * 128, fx_val(buf, s), s >= SF - 4)) return false;
          tn = now32() + 2 * STEP - NAPM;
        }
        tile_dma(rs, lane * 16, w, buf, s, ldsb + (s & 2) * 16384, ldsb + 65536 + (s & 2) * 2048);
        DRAIN();
      } else if (isq) {
        asm volatile("s_waitcnt vmcnt(3)" ::: "memory");
      } else {
        asm volatile("s_waitcnt vmcnt(1)" ::: "memory");
      }
      have = false;
      if (s < SF) break;
      unsigned m = mx4(sn[w * 64 + lane], 0);
#pragma unroll
      for (int i = 0; i < 8; ++i) m = mx4(((const v4u*)tile)[(w * 8 + i) * 64 + lane], m);
      if (all_set(m)) break;
      if (it > POLL_LIMIT) return false;
    }
    __syncthreads();
    if (tid == 0) stc4(rc, fb, layer * 128 + s - 3);
    TS(128 + part * 12 + j, layer * 512 + 8 + s);
    if ((TCH & 8) && s == SF - 4 + part) touch(rw, OFF_KV);
    B1_NEXT(false);
    float mr0, mr1, r0, r1;
    v4u bf[32], sa[4];
#pragma unroll
    for (int i = 0; i < 4; ++i) sa[i] = sn[i * 64 + lane];
#pragma unroll
    for (int i = 0; i < 24; ++i) bf[i] = ((const v4u*)tile)[((i & 1) * 16 + (i >> 1)) * 64 + lane];
    __builtin_amdgcn_sched_barrier(0);
    row_stats(sa, r0, mr0, r1, mr1);
    if (g < 2) stl[lane] = g ? (v2f){r1, mr1} : (v2f){r0, mr0};
    v4f acc[2][2];
#pragma unroll
    for (int c = 0; c < 2; ++c) { acc[c][0] = (v4f){0, 0, 0, 0}; acc[c][1] = (v4f){0, 0, 0, 0}; }
    if (isq) {
#pragma unroll
      for (int ks = 0; ks < 16; ++ks) {
        if (ks == 4) {
          __builtin_amdgcn_sched_barrier(0);
#pragma unroll
          for (int i = 24; i < 32; ++i) bf[i] = ((const v4u*)tile)[((i & 1) * 16 + (i >> 1)) * 64 + lane];
          __builtin_amdgcn_sched_barrier(0);
        }
        const v4u b0 = bf[2 * ks], b1 = bf[2 * ks + 1];
#pragma unroll
        for (int c = 0; c < 2; ++c) { acc[c][0] = mfma32(W[c][ks], b0, acc[c][0]); acc[c][1] = mfma32(W[c][ks], b1, acc[c][1]); }
      }
      B1_NEXT(true);
#pragma unroll
      for (int nt = 0; nt < 2; ++nt) {
        const float rs = nt ? r1 : r0, mr = nt ? mr1 : mr0;
        v4u o;
#pragma unroll
        for (int c = 0; c < 2; ++c) {
          o[2 * c] = pack2(phi(rbf(acc[c][nt][0] * rs + cs[c][0] * mr)), phi(rbf(acc[c][nt][1] * rs + cs[c][1] * mr)));
          o[2 * c + 1] = pack2(phi(rbf(acc[c][nt][2] * rs + cs[c][2] * mr)), phi(rbf(acc[c][nt][3] * rs + cs[c][3] * mr)));
        }
        stc16s(rw, lane * 16, OFF_PQ + ((slot_of(s, layer) * 2 + nt) * 16 + (ct0 >> 1)) * 1024, nosent4(o));
      }
    } else {
#pragma unroll
      for (int ks = 0; ks < 16; ++ks) {
        if (ks == 4) {
          __builtin_amdgcn_sched_barrier(0);
#pragma unroll
          for (int i = 24; i < 32; ++i) bf[i] = ((const v4u*)tile)[((i & 1) * 16 + (i >> 1)) * 64 + lane];
          __builtin_amdgcn_sched_barrier(0);
        }
        const v4u b0 = bf[2 * ks], b1 = bf[2 * ks + 1];
#pragma unroll
        for (int c = 0; c < 2; ++c) { acc[c][0] = mfma32(b0, W[c][ks], acc[c][0]); acc[c][1] = mfma32(b1, W[c][ks], acc[c][1]); }
      }
      B1_NEXT(true);
      v4u pk, vv;
#pragma unroll
      for (int t = 0; t < 2; ++t) {
        float kk[4], vx[4];
#pragma unroll
        for (int r = 0; r < 4; ++r) {
          const v2f sr = stl[16 * t + 4 * g + r];
          kk[r] = phi(rbf(acc[0][t][r] * sr[0] + cs[0][0] * sr[1]));
          vx[r] = acc[1][t][r] * sr[0] + cs[1][0] * sr[1];
        }
        pk[2 * t] = pack2(kk[0], kk[1]); pk[2 * t + 1] = pack2(kk[2], kk[3]);
        vv[2 * t] = pack2(vx[0], vx[1]); vv[2 * t + 1] = pack2(vx[2], vx[3]);
      }
      kv = mfma32(pk, vv, kv);
      zz = mfma32(pk, ones, zz);
    }
  }
  if (!isq) {
    const unsigned o = OFF_KV + layer * 65536 + (4 * j + w) * 2048 + lane * 16 + part * 8;
    stc8(rw, o, (v2u){nosent(pack2(kv[0], kv[1])), nosent(pack2(kv[2], kv[3]))});
    stc8(rw, o + 1024, (v2u){nosent(pack2(zz[0], zz[1])), nosent(pack2(zz[2], zz[3]))});
    if (lane == 0) stc4(rc, L_KV + (part * 8 + j) * 128 + w * 4, layer + 1);
  }
  wg_flag(rc, fb, layer * 128 + 100);
  return true;
}

DI void b2_w(const u16* __restrict__ wo2, int c, v4u (&Wo)[2][16], u8* lds) {
  const int w = __builtin_amdgcn_readfirstlane(threadIdx.x >> 6);
  w_stage(wo2 + 128 * c, wo2 + 128 * c + 64, 512, 2 * w, 2 * w + 1, Wo, lds);
}
DI void b2_pq(rsrc_t rw, int lane, int w, int tl, v4u (&pq)[4]) {
#pragma unroll
  for (int i = 0; i < 4; ++i) pq[i] = ldc16s(rw, lane * 16 + i * 1024, OFF_PQ + (tl * 16 + 4 * w) * 1024);
}
DI void b2_dup(const v4u (&pq)[4], v4u (&q)[8]) {
#pragma unroll
  for (int i = 0; i < 8; ++i) { q[i] = (v4u){pq[i >> 1][2 * (i & 1)], pq[i >> 1][2 * (i & 1) + 1], pq[i >> 1][2 * (i & 1)], pq[i >> 1][2 * (i & 1) + 1]}; asm volatile("" : "+v"(q[i])); }
}
DI unsigned b2_kz(rsrc_t rw, int lane, int w, int layer, v4u (&kz)[16]) {
  unsigned m = 0;
#pragma unroll
  for (int i = 0; i < 16; ++i) kz[i] = ldc16s(rw, lane * 16 + (i & 3) * 1024, OFF_KV + layer * 65536 + w * 16384 + (i >> 2) * 4096);
#pragma unroll
  for (int i = 0; i < 16; ++i) m = mx4(kz[i], m);
  return m;
}
DI void b2_norm(int lane, int w, const v4u (&q)[8], const v4u (&kz)[16], v4u* tile) {
  const v4f zr = {0, 0, 0, 0};
  v4f d[8], nu[8];
#pragma unroll
  for (int i = 0; i < 8; ++i) d[i] = mfma32(kz[2 * i + 1], q[i], zr);
#pragma unroll
  for (int i = 0; i < 8; ++i) nu[i] = mfma32(kz[2 * i], q[i], zr);
#pragma unroll
  for (int i = 0; i < 4; ++i) {
    const v4f na = nu[2 * i], nb = nu[2 * i + 1];
    const float ia = __builtin_amdgcn_rcpf(d[2 * i][0] + 1e-6f), ib = __builtin_amdgcn_rcpf(d[2 * i + 1][0] + 1e-6f);
    tile[(4 * w + i) * 64 + lane] =
        (v4u){pack2(na[0] * ia, na[1] * ia), pack2(na[2] * ia, na[3] * ia), pack2(nb[0] * ib, nb[1] * ib), pack2(nb[2] * ib, nb[3] * ib)};
  }
}
DI bool b2_wait(rsrc_t rc, int lane, int w, int layer) {
  wg_prewait(rc, L_P + (96 + (lane & 31)) * 128, layer * 64 + 64);
  return wait_flags<2>(rc, L_KV + ((lane & 8) + 2 * w + ((lane >> 2) & 1)) * 128 + (lane & 3) * 4, layer + 1);
}
DI bool role_b2a(u8* __restrict__ ws, rsrc_t rw, rsrc_t rc, int cb, int s0, const v4u (&Wo)[2][16], u8* lds) {
  const int tid = ltid(), w = __builtin_amdgcn_readfirstlane(tid >> 6), lane = tid & 63;
  const int ont = cb >> 2, oks = 4 * (cb & 3) + w, ds = 16;
  v4u pq[4], q[8], xv, kz[16];
  v4u* tile = (v4u*)lds;
  const unsigned xo = OFF_XF + 2 * MB + (ont * 16 + oks) * 1024;
  wg_prewait(rc, L_B1 + ((s0 & 1) * 12 + 8 + (lane & 3)) * 128, s0 + 1);
  for (int s = s0; s < NSEG; s += ds) {
    if (s > s0 && !wait_flags<2>(rc, L_B1 + ((s0 & 1) * 12 + 8 + (lane & 3)) * 128, s + 1)) return false;
    b2_pq(rw, lane, w, s * 2 + ont, pq);
    xv = ldc16s(rw, lane * 16, xo + s * 32768);
    b2_dup(pq, q);
    if (s == s0) {
      if (TCH & 1) touch(rw, lane & 1 ? OFF_KV : lane & 2 ? OFF_XF + 4 * MB : OFF_ST);
      if (!b2_wait(rc, lane, w, 0)) return false;
      TS(s0 * 8 + cb, 100);
      for (int it = 0;; ++it) {
        CBAR();
        if (all_set(b2_kz(rw, lane, w, 0, kz))) break;
        if (it > POLL_LIMIT) return false;
      }
      TS(s0 * 8 + cb, 101);
    }
    b2_norm(lane, w, q, kz, tile);
    __syncthreads();
    TS(s0 * 8 + cb, 104 + (s / ds) * 4);
    v4f c0 = {0, 0, 0, 0}, c1 = {0, 0, 0, 0};
    v4u bf[16];
#pragma unroll
    for (int ks = 0; ks < 16; ++ks) bf[ks] = tile[ks * 64 + lane];
#pragma unroll
    for (int ks = 0; ks < 16; ++ks) { c0 = mfma32(Wo[0][ks], bf[ks], c0); c1 = mfma32(Wo[1][ks], bf[ks], c1); }
    x_out(rw, lane, 2, s, ont, oks, xv, c0, c1);
    TS(s0 * 8 + cb, 105 + (s / ds) * 4);
    wg_flag(rc, L_X2 + s0 * 128 + cb * 4, 64 + s + 1);
    TS(s0 * 8 + cb, 106 + (s / ds) * 4);
  }
  return true;
}
DI bool role_b2b(u8* __restrict__ ws, rsrc_t rw, rsrc_t rc, int b, const v4u (&Wo)[2][16], u16* __restrict__ out, u8* lds) {
  const int tid = ltid(), w = __builtin_amdgcn_readfirstlane(tid >> 6), lane = tid & 63, n = lane & 15, g = lane >> 4;
  const int s = b >> 2, oks = 4 * (b & 3) + w;
  v4u pq[2][4], q[2][8], xv[2];
  const unsigned xo = OFF_XF + 6 * MB + s * 32768 + oks * 1024;
  if (s < SF) wg_prewait(rc, L_B1 + ((s & 1) * 12 + 8 + (lane & 3)) * 128, 128 + s + 1);
  b2_pq(rw, lane, w, s * 2, pq[0]); b2_pq(rw, lane, w, s * 2 + 1, pq[1]);
  xv[0] = ldc16s(rw, lane * 16, xo); xv[1] = ldc16s(rw, lane * 16, xo + 16384);
  b2_dup(pq[0], q[0]); b2_dup(pq[1], q[1]);
  if (TCH & 2) {
    touch(rw, OFF_KV);
    const unsigned d = *(const volatile unsigned*)(out + (size_t)(s * 32 + (lane & 31)) * 512 + 32 * oks);
    asm volatile("" :: "v"(d));
  }
  if (!b2_wait(rc, lane, w, 1)) return false;
  TS(b, 512 + 100);
  v4u kz[16];
  for (int it = 0;; ++it) {
    CBAR();
    if (s >= SF) {
      b2_pq(rw, lane, w, (s + NF) * 2, pq[0]); b2_pq(rw, lane, w, (s + NF) * 2 + 1, pq[1]);
      xv[0] = ldc16s(rw, lane * 16, xo); xv[1] = ldc16s(rw, lane * 16, xo + 16384);
    }
    unsigned m = b2_kz(rw, lane, w, 1, kz);
    if (s >= SF) {
      m = mx4(xv[0], mx4(xv[1], m));
#pragma unroll
      for (int i = 0; i < 8; ++i) m = mx4(pq[i >> 2][i & 3], m);
      b2_dup(pq[0], q[0]); b2_dup(pq[1], q[1]);
    }
    if (all_set(m)) break;
    if (it > POLL_LIMIT) return false;
  }
  TS(b, 512 + 101);
  v4u* tile = (v4u*)lds;
  b2_norm(lane, w, q[0], kz, tile); b2_norm(lane, w, q[1], kz, tile + 1024);
  __syncthreads();
  TS(b, 512 + 104);
  v4u bf[32];
#pragma unroll
  for (int i = 0; i < 32; ++i) bf[i] = tile[i * 64 + lane];
#pragma unroll
  for (int nt = 0; nt < 2; ++nt) {
    v4f c0 = {0, 0, 0, 0}, c1 = {0, 0, 0, 0};
#pragma unroll
    for (int ks = 0; ks < 16; ++ks) { c0 = mfma32(Wo[0][ks], bf[nt * 16 + ks], c0); c1 = mfma32(Wo[1][ks], bf[nt * 16 + ks], c1); }
    const v4u xc = xv[nt];
    u16* o = out + (size_t)(s * 32 + 16 * nt + n) * 512 + 32 * oks + 4 * g;
    *(v2u*)o = (v2u){pack2(bflo(xc[0]) + rbf(c0[0]), bfhi(xc[0]) + rbf(c0[1])), pack2(bflo(xc[1]) + rbf(c0[2]), bfhi(xc[1]) + rbf(c0[3]))};
    *(v2u*)(o + 16) = (v2u){pack2(bflo(xc[2]) + rbf(c1[0]), bfhi(xc[2]) + rbf(c1[1])), pack2(bflo(xc[3]) + rbf(c1[2]), bfhi(xc[3]) + rbf(c1[3]))};
  }
  return true;
}

DI bool role_tail(u8* __restrict__ ws, const u16* __restrict__ wqkv, const u16* __restrict__ wo2, rsrc_t rw, rsrc_t rc, int t, int c1, v4u (&Wo)[2][16], u8* lds) {
  const int w = __builtin_amdgcn_readfirstlane(threadIdx.x >> 6);
  for (int layer = 0; layer < 2; ++layer) {
    bool ok = true;
    unsigned tn = now32();
    asm volatile("" : "+s"(t));
    if (t >= 40 && t < 104) TS(t - 40, layer * 512 + 7 * (1 - layer));
    const bool pre = layer || t < 40 || t >= 104;
    if (pre && (!layer || c1 != (t & 3))) b2_w(wo2, layer ? c1 : t & 3, Wo, lds);
    if (t >= 104) {
      ok = role_b1(ws, wqkv, rw, rc, layer, t < 116 ? t - 104 : t - 116, t >= 116, lds);
      TS(t + 24, layer * 512 + 1);
    } else if (t >= 72) {
      const int h = (t & 7) * 4 + w;
      for (int s = (t - 72) >> 3; s < NSEG && ok; s += 4) {
        PX x;
        ok = p_x(rw, rc, layer, s, h, x) && p_m(rw, rc, layer, s, h, x, tn);
        TS(t - 40, layer * 512 + 8 + s);
      }
    } else if (t < 32 || t >= 40) {
      if (t < 32) wg_prewait(rc, L_B1 + (8 + (threadIdx.x & 3)) * 128, layer * 128 + SF - 20 + 2 * (t >> 3));
      for (int s = t < 32 ? SF + (t >> 3) : (t - 40) >> 3; s < (t < 32 ? NSEG : SF) && ok; s += 4) {
        ok = unit_o(rw, rc, layer, s, t & 7, lds, tn);
        TS((s & 3) * 8 + (t & 7), layer * 512 + 8 + s);
      }
    }
    if (!pre) b2_w(wo2, t & 3, Wo, lds);
    if (ok && layer == 0) ok = role_b2a(ws, rw, rc, t & 7, b2_s0(t), Wo, lds);
    if (t >= 40 && t < 104) TS(t - 40, layer * 512 + 2);
    if (!ok) return false;
  }
  return true;
}

__global__ void __launch_bounds__(256, 1) k_main(u8* __restrict__ ws, u16* __restrict__ out, const u16* __restrict__ Wq, const u16* __restrict__ Wk,
                                                 const u16* __restrict__ Wv, const u16* __restrict__ Wo, const u16* __restrict__ wqkv,
                                                 const u16* __restrict__ wo2, const u16* __restrict__ mem0, const u16* __restrict__ x) {
  extern __shared__ __attribute__((aligned(16))) u8 lds[];
  const int b = blockIdx.x;
  u8* comm = ws + OFF_COMM;
  const rsrc_t rw = __builtin_amdgcn_make_buffer_rsrc(ws, 0, WS_BYTES, 0x00020000);
  const int q = (b >> 3) * 4 + (b & 3) + (b & 4) * 32, t = q < 104 ? q - 64 : q < 128 ? q : q - 152;
  bool ok;
  const unsigned ldsb = (unsigned)(uintptr_t)(__attribute__((address_space(3))) u8*)lds;
  v4u W2[2][16];
  unsigned tag = 0;
  if ((q & 127) < 64) {
    if (q < 32) ok = role_a(ws, Wq, Wk, comm, q, ldsb);
    else if (q < 64) ok = role_b(ws, Wv, Wo, comm, q - 32, ldsb);
    else if (q < 160) ok = role_qk(ws, Wq, Wk, q & 31, ldsb);
    else ok = role_vu(ws, Wv, Wo, q & 31, ldsb);
    b2_w(wo2, b & 3, W2, lds);
  } else {
    if (t >= 40 && t < 104) TS(t - 40, 0);
    tag = call_tag(ws);
    ok = init_ws(ws, rw, t, t < 32 ? (t >> 2) + (t & 3) * 8 : t, tag, mem0, x);
  }
  const rsrc_t rc = __builtin_amdgcn_make_buffer_rsrc(comm + near_par(rw), 0, 2 << 20, 0x00020000);
  if (ok && (q & 127) >= 64) ok = role_tail(ws, wqkv, wo2, rw, rc, t, b & 3, W2, lds);
  if (ok) ok = role_b2b(ws, rw, rc, b, W2, out, lds);
  if (q == 64 && threadIdx.x == 0) stc4(rw, OFF_EP, tag);
  TS(b & 63, 512 + 3 + (b >> 6));
  if (!ok) fail(ws);
}

extern "C" void kernel_launch(void* const* d_in, const int* in_sizes, int n_in, void* d_out, int out_size, void* d_ws,
                              size_t ws_size, hipStream_t stream) {
  u8* ws = (u8*)(((uintptr_t)d_ws + 0x1fffffu) & ~(uintptr_t)0x1fffffu);
  hipFuncSetAttribute((const void*)k_main, hipFuncAttributeMaxDynamicSharedMemorySize, 128 * 1024);
  k_main<<<256, 256, 128 * 1024, stream>>>(ws, (u16*)d_out, (const u16*)d_in[2], (const u16*)d_in[3], (const u16*)d_in[4], (const u16*)d_in[5],
                                           (const u16*)d_in[8], (const u16*)d_in[9], (const u16*)d_in[1], (const u16*)d_in[0]);
}
```
